```python
import torch

def _softmax(x, axis=-1):
    return torch.softmax(x, dim=axis)
def _gelu(x):
    return torch.nn.functional.gelu(x, approximate="tanh")
def _silu(x):
    return torch.nn.functional.silu(x)
def _sigmoid(x):
    return torch.sigmoid(x)
def _layernorm(x, axis=-1, eps=1e-5):
    m = x.mean(dim=axis, keepdim=True)
    v = x.var(dim=axis, keepdim=True, unbiased=False)
    return (x - m) / torch.sqrt(v + eps)
def _scatter_add(tgt, idx, src, axis=0):
    return tgt.clone().index_add_(axis, idx, src)
def _segment_sum(src, seg, n):
    out = torch.zeros((n,) + src.shape[1:], dtype=src.dtype)
    return out.index_add_(0, seg, src)
def _pad(x, pw):
    flat = tuple(v for pair in reversed(pw) for v in pair)
    return torch.nn.functional.pad(x, flat)

kernel_name = 'gated_mix__ssm_conv_h3__glu_gated_bilinear__prompt'

def setup_inputs():

    _w = (lambda a: (lambda t: t.to(torch.bfloat16) if t.is_floating_point() else t)(torch.as_tensor(a)))
    def _A_setup():
        B, S, D = 1, 2048, 640
        ks = 4
        return {
            "q": _w((lambda *s: torch.randn(*s, dtype=torch.bfloat16) * 0.05)(B, S, D)),
            "k": _w((lambda *s: torch.randn(*s, dtype=torch.bfloat16) * 0.05)(B, S, D)),
            "v": _w((lambda *s: torch.randn(*s, dtype=torch.bfloat16) * 0.05)(B, S, D)),
            "shift_k": _w((lambda *s: torch.randn(*s, dtype=torch.bfloat16) * 0.05)(D, ks) * 0.5),
            "ssm_k": _w((lambda *s: torch.randn(*s, dtype=torch.bfloat16) * 0.1)(D, S) * (1.0 / S ** 0.5)),
            "ssm_D": _w((lambda *s: torch.randn(*s, dtype=torch.bfloat16) * 0.05)(D)),
        }
    _A_in = _A_setup()

    def _B_setup():
        B, S, D, F = 1, 2048, 640, 1536
        return {
            "x": _w((lambda *s: torch.randn(*s, dtype=torch.bfloat16) * 0.05)(B, S, D)),
            "w_gate": _w((lambda *s: torch.randn(*s, dtype=torch.bfloat16) * 0.1)(D, F) * (D ** -0.5)),
            "w_up": _w((lambda *s: torch.randn(*s, dtype=torch.bfloat16) * 0.1)(D, F) * (D ** -0.5)),
            "w_down": _w((lambda *s: torch.randn(*s, dtype=torch.bfloat16) * 0.1)(F, D) * (F ** -0.5)),
        }
    _B_in = _B_setup()

    return {
        "A_q": _A_in["q"],
        "A_k": _A_in["k"],
        "A_v": _A_in["v"],
        "A_shift_k": _A_in["shift_k"],
        "A_ssm_k": _A_in["ssm_k"],
        "A_ssm_D": _A_in["ssm_D"],
        "B_x": _B_in["x"],
        "B_w_gate": _B_in["w_gate"],
        "B_w_up": _B_in["w_up"],
        "B_w_down": _B_in["w_down"],
        "gate_w": _w((lambda *s: torch.randn(*s, dtype=torch.bfloat16) * 0.05)(640) * 16.0),
    }

def reference(A_q, A_k, A_v, A_shift_k, A_ssm_k, A_ssm_D, B_x, B_w_gate, B_w_up, B_w_down, gate_w):
    def _A_ref(q, k, v, shift_k, ssm_k, ssm_D):
        B, S, D = q.shape
        ks = shift_k.shape[-1]
        k_pad = _pad(k, ((0, 0), (ks - 1, 0), (0, 0)))
        k_shift = torch.zeros_like(k)
        for t in range(ks):
            k_shift = k_shift + k_pad[:, t:t + S, :] * shift_k[:, ks - 1 - t][None, None, :]
        kv = k_shift * v
        L = 2 * S
        kv_pad = _pad(kv, ((0, 0), (0, S), (0, 0)))
        kf_pad = _pad(ssm_k, ((0, 0), (0, S)))
        kv_f = (lambda x, axes=(-2,-1): torch.fft.rfft2(x.float(), dim=axes))(kv_pad[:, None, :, :], axes=(1, 2))
        kf_f = (lambda x, axes=(-2,-1): torch.fft.rfft2(x.float(), dim=axes))(kf_pad[:, None, :], axes=(1, 2))
        kf_f = (lambda x, *d: x.permute(*d))(kf_f, 1, 2, 0)
        y_f = kv_f * kf_f[None, :, :, :]
        y = (lambda x, s, axes=(-2,-1): torch.fft.irfft2(x, s=s, dim=axes).to(torch.bfloat16))(y_f, (1, L), axes=(1, 2))[:, 0, :S, :]
        y = y + kv * ssm_D[None, None, :]
        return q * y

    def _B_ref(x, w_gate, w_up, w_down):
        g = torch.einsum("bsd,df->bsf", x, w_gate)
        u = torch.einsum("bsd,df->bsf", x, w_up)
        h = g * u
        return torch.einsum("bsf,fd->bsd", h, w_down)

    x = A_q
    g = _sigmoid((x * gate_w).sum(-1, keepdims=True))
    a = _A_ref(x, A_k, A_v, A_shift_k, A_ssm_k, A_ssm_D)
    b = _B_ref(x, B_w_gate, B_w_up, B_w_down)
    return g * a + (1 - g) * b
```

```cpp
#include <hip/hip_runtime.h>
#include <stdint.h>

#define LAS __attribute__((address_space(3)))
#define GAS __attribute__((address_space(1)))
typedef unsigned short u16;
typedef unsigned v4u __attribute__((ext_vector_type(4)));
typedef unsigned v2u __attribute__((ext_vector_type(2)));
typedef __bf16 bf8 __attribute__((ext_vector_type(8)));
typedef __bf16 bf2 __attribute__((ext_vector_type(2)));
typedef short s4 __attribute__((ext_vector_type(4)));
typedef float f32x2 __attribute__((ext_vector_type(2)));
typedef float f32x4 __attribute__((ext_vector_type(4)));
typedef float f32x16 __attribute__((ext_vector_type(16)));

constexpr int SEQ = 2048, DM = 640, FF = 1536;
constexpr size_t OFF_KVR = 65536;
constexpr size_t OFF_WDT = OFF_KVR + (size_t)DM * SEQ * 2;
constexpr size_t OFF_HF = OFF_WDT + (size_t)FF * DM * 2;
constexpr size_t OFF_YT = OFF_HF + (size_t)SEQ * FF * 2;
constexpr size_t OFF_GATE = OFF_YT + (size_t)DM * SEQ * 2;
constexpr size_t WS_BYTES = OFF_GATE + SEQ * 4;

struct Params {
    const u16 *q, *k, *v, *sk, *hk, *sd, *wg, *wu, *wd, *gw;
    u16* out;
    unsigned char* ws;
};

__device__ __forceinline__ unsigned pk2(float a, float b) {
    f32x2 x = {a, b};
    return __builtin_bit_cast(unsigned, __builtin_convertvector(x, bf2));
}
__device__ __forceinline__ float blo(unsigned u) { return __builtin_bit_cast(float, u << 16); }
__device__ __forceinline__ float bhi(unsigned u) { return __builtin_bit_cast(float, u & 0xffff0000u); }
__device__ __forceinline__ float rb(float x) { return blo(pk2(x, x)); }
__device__ __forceinline__ f32x4 mfma16(v4u a, v4u b, f32x4 c) {
    return __builtin_amdgcn_mfma_f32_16x16x32_bf16(__builtin_bit_cast(bf8, a), __builtin_bit_cast(bf8, b), c, 0, 0, 0);
}
__device__ __forceinline__ f32x16 mfma32(v4u a, v4u b, f32x16 c) {
    return __builtin_amdgcn_mfma_f32_32x32x16_bf16(__builtin_bit_cast(bf8, a), __builtin_bit_cast(bf8, b), c, 0, 0, 0);
}
__device__ __forceinline__ v2u tr_read(LAS unsigned char* p) {
    return __builtin_bit_cast(v2u, __builtin_amdgcn_ds_read_tr16_b64_v4i16((LAS s4*)p));
}
__device__ __forceinline__ v4u ldg16(const void* p) { return *(const GAS v4u*)p; }
__device__ __forceinline__ void stg16(void* p, v4u v) { *(GAS v4u*)p = v; }
#define WAIT_V(n) asm volatile("s_waitcnt vmcnt(" #n ")" ::: "memory")
#define WAIT_L(n) asm volatile("s_waitcnt lgkmcnt(" #n ")" ::: "memory")
#define BAR() __builtin_amdgcn_s_barrier()

constexpr int PREP_LDS = 20480;
__device__ __forceinline__ void prep_kv(const Params& p, LAS unsigned char* L, int bid) {
    const int tid = threadIdx.x, tt = bid & 15, dtile = bid >> 4;
    const int cc = tid & 7, tg = tid >> 3;
    const int t0 = 128 * tt + 4 * tg, ch0 = 64 * dtile + 8 * cc;
    v4u kk[7], vv[4], w[4];
#pragma unroll
    for (int i = 0; i < 7; ++i) {
        const int row = t0 - 3 + i;
        kk[i] = row >= 0 ? ldg16(p.k + (size_t)row * DM + ch0) : v4u{0, 0, 0, 0};
    }
#pragma unroll
    for (int j = 0; j < 4; ++j) vv[j] = ldg16(p.v + (size_t)(t0 + j) * DM + ch0);
#pragma unroll
    for (int i = 0; i < 4; ++i) w[i] = ldg16(p.sk + (size_t)ch0 * 4 + 8 * i);
#pragma unroll
    for (int e = 0; e < 8; ++e) {
        const unsigned wa = w[e >> 1][2 * (e & 1)], wb = w[e >> 1][2 * (e & 1) + 1];
        const float w0 = blo(wa), w1 = bhi(wa), w2 = blo(wb), w3 = bhi(wb);
        float kf[7], r[4];
#pragma unroll
        for (int i = 0; i < 7; ++i) kf[i] = (e & 1) ? bhi(kk[i][e >> 1]) : blo(kk[i][e >> 1]);
#pragma unroll
        for (int j = 0; j < 4; ++j) {
            float ks = rb(kf[j] * w3);
            ks = rb(ks + rb(kf[j + 1] * w2));
            ks = rb(ks + rb(kf[j + 2] * w1));
            ks = rb(ks + rb(kf[j + 3] * w0));
            const float vf = (e & 1) ? bhi(vv[j][e >> 1]) : blo(vv[j][e >> 1]);
            r[j] = ks * vf;
        }
        v2u o = {pk2(r[3], r[2]), pk2(r[1], r[0])};
        const int chunk = 15 - (tg >> 1), sub = ((tg & 1) ^ 1) * 8;
        *(LAS v2u*)(L + (8 * cc + e) * 256 + ((chunk ^ cc) << 4) + sub) = o;
    }
    __syncthreads();
    u16* dst = (u16*)(p.ws + OFF_KVR);
#pragma unroll
    for (int i = 0; i < 4; ++i) {
        const int idx = tid + 256 * i, ch = idx >> 4, c = idx & 15;
        const v4u val = *(LAS v4u*)(L + ch * 256 + ((c ^ (ch >> 3)) << 4));
        stg16(dst + (size_t)(64 * dtile + ch) * SEQ + (1920 - 128 * tt) + 8 * c, val);
    }
}

__device__ __forceinline__ void prep_wd(const Params& p, LAS unsigned char* L, int idx) {
    const int tid = threadIdx.x, lane = tid & 63, w = tid >> 6;
    const int grp = 4 * idx + w, dt = grp & 7, ks = grp >> 3;
    LAS unsigned char* T = L + w * 5120;
#pragma unroll
    for (int i = 0; i < 5; ++i) {
        const int c = lane + 64 * i, row = c / 10, col = c % 10;
        *(LAS v4u*)(T + c * 16) = ldg16(p.wd + (size_t)(32 * ks + row) * DM + 80 * dt + 8 * col);
    }
    asm volatile("" ::: "memory");
    const int g = lane >> 4, q = (lane >> 2) & 3, pp = lane & 3;
    const int row = 16 * (g >> 1) + 4 * (g & 1) + q;
    unsigned char* dst = p.ws + OFF_WDT + (size_t)((dt * 48 + ks) * 5) * 1024 + lane * 16;
#pragma unroll
    for (int at = 0; at < 5; ++at) {
        const v2u lo = tr_read(T + row * 160 + (16 * at + 4 * pp) * 2);
        const v2u hi = tr_read(T + (row + 8) * 160 + (16 * at + 4 * pp) * 2);
        stg16(dst + at * 1024, v4u{lo.x, lo.y, hi.x, hi.y});
    }
}

__device__ __forceinline__ void prep_gate(const Params& p, int idx) {
    const int tid = threadIdx.x, lane = tid & 63, w = tid >> 6;
    const int c16 = lane & 15, r4 = lane >> 4;
    v4u gw[5];
#pragma unroll
    for (int i = 0; i < 5; ++i) gw[i] = ldg16(p.gw + 8 * (c16 + 16 * i));
#pragma unroll
    for (int ps = 0; ps < 2; ++ps) {
        const int row = 32 * idx + 8 * w + 4 * ps + r4;
        float s = 0.f;
#pragma unroll
        for (int i = 0; i < 5; ++i) {
            const v4u x = ldg16(p.q + (size_t)row * DM + 8 * (c16 + 16 * i));
#pragma unroll
            for (int e = 0; e < 4; ++e) {
                s += rb(blo(x[e]) * blo(gw[i][e]));
                s += rb(bhi(x[e]) * bhi(gw[i][e]));
            }
        }
        s += __shfl_xor(s, 1); s += __shfl_xor(s, 2); s += __shfl_xor(s, 4); s += __shfl_xor(s, 8);
        const float sb = rb(s);
        const float g = rb(1.0f / (1.0f + __expf(-sb)));
        if (c16 == 0) ((float*)(p.ws + OFF_GATE))[row] = g;
    }
}

constexpr int G1_STAGE = 41024, G1_G = 16384, G1_U = 16384 + 12288 + 32;
constexpr int G1_LDS = 3 * G1_STAGE;
struct G1Frags { v4u b[2]; v4u a[3]; };

__device__ __forceinline__ void glds16(const void* gbase, unsigned voff, LAS unsigned char* ldst) {
    __builtin_amdgcn_global_load_lds((const GAS void*)((const GAS char*)gbase + voff), (LAS void*)ldst, 16, 0, 0);
}

struct G1State {
    unsigned xoff[4], woff[3];
};
__device__ __forceinline__ void g1_issue(const Params& p, LAS unsigned char* L, const G1State& s, int w, int kt, int stage_off) {
    const char* xb = (const char*)p.q + kt * 128;
    const char* gb = (const char*)p.wg + (size_t)kt * 64 * 3072;
    const char* ub = (const char*)p.wu + (size_t)kt * 64 * 3072;
    LAS unsigned char* st = L + stage_off;
#pragma unroll
    for (int n = 0; n < 4; ++n) glds16(xb, s.xoff[n], st + (4 * w + n) * 1024);
#pragma unroll
    for (int n = 0; n < 3; ++n) glds16(gb, s.woff[n], st + G1_G + (3 * w + n) * 1024);
#pragma unroll
    for (int n = 0; n < 3; ++n) glds16(ub, s.woff[n], st + G1_U + (3 * w + n) * 1024);
}

__device__ __forceinline__ void gemm1(const Params& p, LAS unsigned char* L, int bid, bool issue_prologue) {
    const int tid = threadIdx.x, lane = tid & 63, w = __builtin_amdgcn_readfirstlane(tid >> 6);
    const int xcd = bid & 7, i = bid >> 3, mq = xcd >> 1, half = xcd & 1;
    const int mt1 = 4 * mq + (i & 3), ft = 8 * half + (i >> 2);
    const int wm = w & 1, wf = w >> 1;
    G1State s;
#pragma unroll
    for (int n = 0; n < 4; ++n) {
        const int row = 8 * (4 * w + n) + (lane >> 3);
        s.xoff[n] = (unsigned)((128 * mt1 + row) * 1280 + (((lane & 7) ^ ((row >> 1) & 7)) << 4));
    }
#pragma unroll
    for (int n = 0; n < 3; ++n) {
        const int Lc = 64 * (3 * w + n) + lane, k = Lc / 12, c = Lc % 12;
        s.woff[n] = (unsigned)(k * 3072 + 192 * ft + 16 * c);
    }
    g1_issue(p, L, s, w, 0, 0);
    g1_issue(p, L, s, w, 1, G1_STAGE);
    g1_issue(p, L, s, w, 2, 2 * G1_STAGE);

    {
        LAS unsigned char* PL = L + G1_LDS;
        if (bid < 160) prep_kv(p, PL, bid);
        else {
            prep_wd(p, PL, bid - 160);
            if (bid - 160 < 64) prep_gate(p, bid - 160);
        }
    }

    const int l31 = lane & 31, hh = lane >> 5;
    const int brow = 64 * wm + l31;
    unsigned baddr[4];
#pragma unroll
    for (int kk = 0; kk < 4; ++kk) baddr[kk] = (unsigned)(brow * 128 + (((2 * kk + hh) ^ ((brow >> 1) & 7)) << 4));
    const int G = lane >> 4, q = (lane >> 2) & 3, pp = lane & 3;
    const unsigned aaddr = (unsigned)(G1_G + (G & 1) * (G1_U - G1_G) + (8 * (G >> 1) + q) * 192 + 3 * wf * 32 + 8 * pp);

    f32x16 acc[3][2];
#pragma unroll
    for (int a = 0; a < 3; ++a)
#pragma unroll
        for (int b = 0; b < 2; ++b)
#pragma unroll
            for (int r = 0; r < 16; ++r) acc[a][b][r] = 0.f;

    auto load_frags = [&](G1Frags& f, int stage_off, int kk) {
        LAS unsigned char* st = L + stage_off;
#pragma unroll
        for (int mt = 0; mt < 2; ++mt) f.b[mt] = *(LAS v4u*)(st + baddr[kk] + mt * 4096);
#pragma unroll
        for (int fb = 0; fb < 3; ++fb) {
            const v2u lo = tr_read(st + aaddr + kk * 16 * 192 + fb * 32);
            const v2u hi = tr_read(st + aaddr + kk * 16 * 192 + fb * 32 + 4 * 192);
            f.a[fb] = v4u{lo.x, lo.y, hi.x, hi.y};
        }
    };
    auto mma = [&](const G1Frags& f) {
#pragma unroll
        for (int fb = 0; fb < 3; ++fb)
#pragma unroll
            for (int mt = 0; mt < 2; ++mt) acc[fb][mt] = mfma32(f.a[fb], f.b[mt], acc[fb][mt]);
    };

    G1Frags F0, F1;
    WAIT_V(20);
    BAR();
    int st = 0;
    load_frags(F0, 0, 0);
#pragma unroll 1
    for (int kt = 0; kt < 10; ++kt) {
        load_frags(F1, st, 1);
        mma(F0);
        load_frags(F0, st, 2);
        mma(F1);
        load_frags(F1, st, 3);
        mma(F0);
        const int nst = (st == 2 * G1_STAGE) ? 0 : st + G1_STAGE;
        if (kt < 9) {
            WAIT_L(0);
            if (kt < 8) WAIT_V(10); else WAIT_V(0);
            BAR();
            if (kt < 7) g1_issue(p, L, s, w, kt + 3, st);
            load_frags(F0, nst, 0);
        }
        mma(F1);
        st = nst;
    }
    unsigned char* hf = p.ws + OFF_HF;
#pragma unroll
    for (int fb = 0; fb < 3; ++fb)
#pragma unroll
        for (int mt = 0; mt < 2; ++mt) {
            const f32x16& a = acc[fb][mt];
            v4u o;
#pragma unroll
            for (int j = 0; j < 4; ++j) {
                const unsigned gp = pk2(a[2 * j], a[2 * j + 1]), up = pk2(a[8 + 2 * j], a[8 + 2 * j + 1]);
                o[j] = pk2(blo(gp) * blo(up), bhi(gp) * bhi(up));
            }
            const int Ft = 6 * ft + 3 * wf + fb;
            const int mrow = 128 * mt1 + 64 * wm + 32 * mt + l31;
            const int pos = (lane & 15) + 16 * (2 * (Ft & 1) + hh);
            stg16(hf + ((size_t)(mrow >> 4) * 48 + (Ft >> 1)) * 1024 + pos * 16, o);
        }
}

constexpr int CV_CS = 4160, CV_XR = 4 * CV_CS, CV_WAVE = CV_XR + 4608;
__device__ __forceinline__ void conv_channel(const Params& p, LAS unsigned char* T, int ch) {
    const int lane = threadIdx.x & 63;
    const u16* hrow = p.hk + (size_t)ch * SEQ;
#pragma unroll
    for (int ps = 0; ps < 5; ++ps) {
        const int n = 64 * ps + lane;
        if (n < 258) {
            const v4u c0 = n >= 2 ? ldg16(hrow + 8 * (n - 2)) : v4u{0, 0, 0, 0};
            const v4u c1 = (n >= 1 && n <= 256) ? ldg16(hrow + 8 * (n - 1)) : v4u{0, 0, 0, 0};
            const unsigned Dw[6] = {c0.x, c0.y, c0.z, c0.w, c1.x, c1.y};
            v4u o0, o1, o2, o3;
#pragma unroll
            for (int q = 0; q < 4; ++q) {
                o0[q] = __builtin_amdgcn_alignbit(Dw[q + 1], Dw[q], 16);
                o1[q] = Dw[q + 1];
                o2[q] = __builtin_amdgcn_alignbit(Dw[q + 2], Dw[q + 1], 16);
                o3[q] = Dw[q + 2];
            }
            *(LAS v4u*)(T + 0 * CV_CS + 16 * n) = o0;
            *(LAS v4u*)(T + 1 * CV_CS + 16 * n) = o1;
            *(LAS v4u*)(T + 2 * CV_CS + 16 * n) = o2;
            *(LAS v4u*)(T + 3 * CV_CS + 16 * n) = o3;
        }
    }
    const u16* xrow = (const u16*)(p.ws + OFF_KVR) + (size_t)ch * SEQ;
#pragma unroll
    for (int ps = 0; ps < 4; ++ps) {
        const int n = 64 * ps + lane;
        *(LAS v4u*)(T + CV_XR + 16 * n) = ldg16(xrow + 8 * n);
    }
    if (lane < 32) *(LAS v4u*)(T + CV_XR + 4096 + 16 * lane) = v4u{0, 0, 0, 0};
    asm volatile("" ::: "memory");

    const int r = lane & 15, g = lane >> 4;
    LAS unsigned char* ap = T + (r & 3) * CV_CS + 2 * ((r & ~3) + 8 * g);
    LAS unsigned char* bp = T + CV_XR + 2 * (240 - 16 * r + 8 * g);
    f32x4 acc[8];
#pragma unroll
    for (int i = 0; i < 8; ++i) acc[i] = f32x4{0.f, 0.f, 0.f, 0.f};
#pragma unroll 1
    for (int s = 0; s < 8; ++s) {
        v4u A[8], B[8];
#pragma unroll
        for (int i = 0; i < 8; ++i) {
            B[i] = *(LAS v4u*)(bp + 512 * (7 - i));
            const v2u lo = *(LAS v2u*)(ap + 512 * i), hi = *(LAS v2u*)(ap + 512 * i + 8);
            A[i] = v4u{lo.x, lo.y, hi.x, hi.y};
        }
#pragma unroll
        for (int gm = 0; gm < 8; ++gm)
#pragma unroll
            for (int be = 0; be <= gm; ++be) acc[gm] = mfma16(A[be], B[gm - be], acc[gm]);
        ap += 64; bp += 64;
    }
    const float Dc = blo(p.sd[ch]);
#pragma unroll
    for (int gm = 0; gm < 8; ++gm) {
        const int t0 = 256 * gm + 16 * r + 4 * g;
        const v2u kvp = *(LAS v2u*)(T + CV_XR + 2 * (2044 - t0));
        const float k0 = bhi(kvp.y), k1 = blo(kvp.y), k2 = bhi(kvp.x), k3 = blo(kvp.x);
        const float y0 = rb(acc[gm][0]) + rb(k0 * Dc), y1 = rb(acc[gm][1]) + rb(k1 * Dc), y2 = rb(acc[gm][2]) + rb(k2 * Dc), y3 = rb(acc[gm][3]) + rb(k3 * Dc);
        *(LAS v2u*)(T + 2 * t0) = v2u{pk2(y0, y1), pk2(y2, y3)};
    }
    asm volatile("" ::: "memory");
    u16* yrow = (u16*)(p.ws + OFF_YT) + (size_t)ch * SEQ;
#pragma unroll
    for (int ps = 0; ps < 4; ++ps) {
        const int n = 64 * ps + lane;
        stg16(yrow + 8 * n, *(LAS v4u*)(T + 16 * n));
    }
}

constexpr int G2_RED = 0, G2_Y = 81920, G2_LDS = G2_Y + 80 * 128;
template <int W>
__device__ __forceinline__ void gemm2(const Params& p, LAS unsigned char* L, int bid) {
    const int tid = threadIdx.x, lane = tid & 63, w = __builtin_amdgcn_readfirstlane(tid >> 6);
    const int xcd = bid & 7, i = bid >> 3, mq = xcd >> 1, half = xcd & 1;
    const int mt2 = 8 * mq + (i & 7), dt = 4 * half + (i >> 3);
    const unsigned char* ab = p.ws + OFF_WDT + (size_t)((dt * 48 + 12 * w) * 5) * 1024 + lane * 16;
    const unsigned char* bb = p.ws + OFF_HF + (size_t)(4 * mt2 * 48 + 12 * w) * 1024 + lane * 16;
    v4u A[W][5], B[W][4];
#pragma unroll
    for (int s = 0; s < W; ++s) {
#pragma unroll
        for (int n = 0; n < 4; ++n) B[s][n] = ldg16(bb + (size_t)(n * 48 + s) * 1024);
#pragma unroll
        for (int a = 0; a < 5; ++a) A[s][a] = ldg16(ab + (size_t)(s * 5 + a) * 1024);
    }
    __builtin_amdgcn_sched_barrier(0);
    f32x4 acc[5][4];
#pragma unroll
    for (int a = 0; a < 5; ++a)
#pragma unroll
        for (int n = 0; n < 4; ++n) acc[a][n] = f32x4{0.f, 0.f, 0.f, 0.f};
#pragma unroll
    for (int s = 0; s < 12; ++s) {
        v4u ca[5], cb[4];
#pragma unroll
        for (int a = 0; a < 5; ++a) ca[a] = A[s % W][a];
#pragma unroll
        for (int n = 0; n < 4; ++n) cb[n] = B[s % W][n];
        if (s + W < 12) {
#pragma unroll
            for (int n = 0; n < 4; ++n) B[s % W][n] = ldg16(bb + (size_t)(n * 48 + s + W) * 1024);
#pragma unroll
            for (int a = 0; a < 5; ++a) A[s % W][a] = ldg16(ab + (size_t)((s + W) * 5 + a) * 1024);
        }
#pragma unroll
        for (int a = 0; a < 5; ++a)
#pragma unroll
            for (int n = 0; n < 4; ++n) acc[a][n] = mfma16(ca[a], cb[n], acc[a][n]);
        __builtin_amdgcn_sched_barrier(0);
    }
    {
        const u16* yt = (const u16*)(p.ws + OFF_YT);
#pragma unroll
        for (int it = 0; it < 3; ++it) {
            const int c = tid + 256 * it;
            if (c < 640) {
                const int row = c >> 3, col = c & 7;
                *(LAS v4u*)(L + G2_Y + c * 16) = ldg16(yt + (size_t)(80 * dt + row) * SEQ + 64 * mt2 + 8 * col);
            }
        }
    }
#pragma unroll
    for (int a = 0; a < 5; ++a)
#pragma unroll
        for (int n = 0; n < 4; ++n) *(LAS f32x4*)(L + G2_RED + ((w * 5 + a) * 4 + n) * 1024 + lane * 16) = acc[a][n];
    __syncthreads();
    const int t = 64 * mt2 + 16 * w + (lane & 15), g4 = lane >> 4;
    const float gt = ((const float*)(p.ws + OFF_GATE))[t];
    const float omg = rb(1.0f - gt);
    const int q = (lane >> 2) & 3, pp = lane & 3;
#pragma unroll
    for (int a = 0; a < 5; ++a) {
        f32x4 b = {0.f, 0.f, 0.f, 0.f};
#pragma unroll
        for (int ww = 0; ww < 4; ++ww) b += *(LAS f32x4*)(L + G2_RED + ((ww * 5 + a) * 4 + w) * 1024 + lane * 16);
        const v2u y = tr_read(L + G2_Y + (16 * a + 4 * g4 + q) * 128 + (16 * w + 4 * pp) * 2);
        const int d0 = 80 * dt + 16 * a + 4 * g4;
        const v2u qv = *(const GAS v2u*)(p.q + (size_t)t * DM + d0);
        const float a0 = rb(blo(qv.x) * blo(y.x)), a1 = rb(bhi(qv.x) * bhi(y.x)), a2 = rb(blo(qv.y) * blo(y.y)), a3 = rb(bhi(qv.y) * bhi(y.y));
        const float o0 = rb(gt * a0) + rb(omg * rb(b[0])), o1 = rb(gt * a1) + rb(omg * rb(b[1]));
        const float o2 = rb(gt * a2) + rb(omg * rb(b[2])), o3 = rb(gt * a3) + rb(omg * rb(b[3]));
        *(GAS v2u*)(p.out + (size_t)t * DM + d0) = v2u{pk2(o0, o1), pk2(o2, o3)};
    }
}

__global__ void __launch_bounds__(256, 1) k_phase1(Params p) {
    extern __shared__ __attribute__((aligned(16))) unsigned char smem[];
    gemm1(p, (LAS unsigned char*)smem, blockIdx.x, true);
}
__global__ void __launch_bounds__(256, 1) k_conv(Params p) {
    extern __shared__ __attribute__((aligned(16))) unsigned char smem[];
    const int bid = blockIdx.x, w = __builtin_amdgcn_readfirstlane(threadIdx.x >> 6);
    const int nch = bid < 128 ? 3 : 2, c0 = bid < 128 ? 3 * bid : 384 + 2 * (bid - 128);
    if (w < nch) conv_channel(p, (LAS unsigned char*)smem + w * CV_WAVE, c0 + w);
}
__global__ void __launch_bounds__(256, 1) k_gemm2(Params p) {
    extern __shared__ __attribute__((aligned(16))) unsigned char smem[];
    gemm2<6>(p, (LAS unsigned char*)smem, blockIdx.x);
}

extern "C" void kernel_launch(void* const* d_in, const int* in_sizes, int n_in, void* d_out, int out_size, void* d_ws, size_t ws_size, hipStream_t stream) {
    Params p;
    p.q = (const u16*)d_in[0]; p.k = (const u16*)d_in[1]; p.v = (const u16*)d_in[2]; p.sk = (const u16*)d_in[3];
    p.hk = (const u16*)d_in[4]; p.sd = (const u16*)d_in[5];
    p.wg = (const u16*)d_in[7]; p.wu = (const u16*)d_in[8]; p.wd = (const u16*)d_in[9]; p.gw = (const u16*)d_in[10];
    p.out = (u16*)d_out; p.ws = (unsigned char*)d_ws;
    constexpr int L1 = G1_LDS + PREP_LDS, L2 = 4 * CV_WAVE, L3 = G2_LDS;
    (void)hipFuncSetAttribute((const void*)k_phase1, hipFuncAttributeMaxDynamicSharedMemorySize, L1);
    (void)hipFuncSetAttribute((const void*)k_conv, hipFuncAttributeMaxDynamicSharedMemorySize, L2);
    (void)hipFuncSetAttribute((const void*)k_gemm2, hipFuncAttributeMaxDynamicSharedMemorySize, L3);
    k_phase1<<<256, 256, L1, stream>>>(p);
    k_conv<<<256, 256, L2, stream>>>(p);
    k_gemm2<<<256, 256, L3, stream>>>(p);
}
```

```cpp
#include <hip/hip_runtime.h>
#include <stdint.h>

#define LAS __attribute__((address_space(3)))
#define GAS __attribute__((address_space(1)))
typedef unsigned short u16;
typedef unsigned v4u __attribute__((ext_vector_type(4)));
typedef unsigned v2u __attribute__((ext_vector_type(2)));
typedef __bf16 bf8 __attribute__((ext_vector_type(8)));
typedef __bf16 bf2 __attribute__((ext_vector_type(2)));
typedef short s4 __attribute__((ext_vector_type(4)));
typedef float f32x2 __attribute__((ext_vector_type(2)));
typedef float f32x4 __attribute__((ext_vector_type(4)));
typedef float f32x16 __attribute__((ext_vector_type(16)));
typedef __amdgpu_buffer_rsrc_t rsrc_t;

constexpr int SEQ = 2048, DM = 640, FF = 1536;
constexpr int OFF_EPOCH = 0;
constexpr int OFF_FA = 32768;
constexpr int OFF_CB = 2048;
constexpr int OFF_CC = 8192;
constexpr int OFF_RB = 12288;
constexpr int OFF_RC = 16384;
constexpr int OFF_STAMP = 32 * 1024 * 1024;
constexpr int OFF_KVR = 65536;
constexpr int OFF_WDT = OFF_KVR + DM * SEQ * 2;
constexpr int OFF_HF = OFF_WDT + FF * DM * 2;
constexpr int OFF_YT = OFF_HF + SEQ * FF * 2;
constexpr int OFF_GATE = OFF_YT + DM * SEQ * 2;
constexpr int WS_BYTES = OFF_GATE + SEQ * 4;
constexpr int SC1 = 16;
constexpr unsigned OOB = 0x80000000u;
constexpr unsigned SPIN_MAX = 1u << 16;

struct Params {
    const u16 *q, *k, *v, *sk, *hk, *sd, *wg, *wu, *wd, *gw;
    u16* out;
    unsigned char* ws;
};

__device__ __forceinline__ unsigned pk2(float a, float b) {
    f32x2 x = {a, b};
    return __builtin_bit_cast(unsigned, __builtin_convertvector(x, bf2));
}
__device__ __forceinline__ float blo(unsigned u) { return __builtin_bit_cast(float, u << 16); }
__device__ __forceinline__ float bhi(unsigned u) { return __builtin_bit_cast(float, u & 0xffff0000u); }
__device__ __forceinline__ float rb(float x) { return blo(pk2(x, x)); }
__device__ __forceinline__ f32x4 mfma16(v4u a, v4u b, f32x4 c) {
    return __builtin_amdgcn_mfma_f32_16x16x32_bf16(__builtin_bit_cast(bf8, a), __builtin_bit_cast(bf8, b), c, 0, 0, 0);
}
__device__ __forceinline__ f32x16 mfma32(v4u a, v4u b, f32x16 c) {
    return __builtin_amdgcn_mfma_f32_32x32x16_bf16(__builtin_bit_cast(bf8, a), __builtin_bit_cast(bf8, b), c, 0, 0, 0);
}
__device__ __forceinline__ float dot2(unsigned a, unsigned b, float c) {
    return __builtin_amdgcn_fdot2_f32_bf16(__builtin_bit_cast(bf2, a), __builtin_bit_cast(bf2, b), c, false);
}
template <class T> __device__ __forceinline__ T lds_ld(unsigned a) { return *(LAS T*)(uintptr_t)a; }
template <class T> __device__ __forceinline__ void lds_st(unsigned a, T v) { *(LAS T*)(uintptr_t)a = v; }
__device__ __forceinline__ v2u tr_read(unsigned a) {
    return __builtin_bit_cast(v2u, __builtin_amdgcn_ds_read_tr16_b64_v4i16((LAS s4*)(uintptr_t)a));
}
__device__ __forceinline__ rsrc_t mkrs(const void* p, unsigned bytes) { return __builtin_amdgcn_make_buffer_rsrc((void*)p, 0, bytes, 0x00020000); }
template <int AUX> __device__ __forceinline__ v4u bld16(rsrc_t rs, unsigned voff, unsigned soff) { return __builtin_amdgcn_raw_buffer_load_b128(rs, voff, soff, AUX); }
template <int AUX> __device__ __forceinline__ void bst16(rsrc_t rs, unsigned voff, unsigned soff, v4u v) { __builtin_amdgcn_raw_buffer_store_b128(v, rs, voff, soff, AUX); }
#define WAIT_V(n) asm volatile("s_waitcnt vmcnt(" #n ")" ::: "memory")
#define WAIT_L(n) asm volatile("s_waitcnt lgkmcnt(" #n ")" ::: "memory")
#define BAR() do { asm volatile("" ::: "memory"); __builtin_amdgcn_s_barrier(); asm volatile("" ::: "memory"); } while (0)
#define SB() __builtin_amdgcn_sched_barrier(0)
#define PIN_S(x) asm volatile("" : "+s"(x))
#define PIN_V(x) asm volatile("" : "+v"(x))
#define RLX_AGENT __ATOMIC_RELAXED, __HIP_MEMORY_SCOPE_AGENT
#define STAMP(p, i) do {} while (0)

__device__ __forceinline__ void dma16(rsrc_t rs, unsigned voff, unsigned soff, unsigned lds_addr) {
    asm volatile("s_mov_b32 m0, %0\n\ts_nop 0\n\tbuffer_load_dwordx4 %1, %2, %3 offen lds" ::"s"(lds_addr), "v"(voff), "s"(rs), "s"(soff) : "memory", "m0");
}
__device__ __forceinline__ void dma16_sc1(rsrc_t rs, unsigned voff, unsigned soff, unsigned lds_addr) {
    asm volatile("s_mov_b32 m0, %0\n\ts_nop 0\n\tbuffer_load_dwordx4 %1, %2, %3 offen sc1 lds" ::"s"(lds_addr), "v"(voff), "s"(rs), "s"(soff) : "memory", "m0");
}
#define SET_M0(base, imm) asm volatile("s_add_u32 m0, %0, %1" ::"s"(base), "i"(imm) : "memory", "m0", "scc")
__device__ __forceinline__ void dma16_m0(rsrc_t rs, unsigned voff, unsigned soff) {
    asm volatile("buffer_load_dwordx4 %0, %1, %2 offen lds" ::"v"(voff), "s"(rs), "s"(soff) : "memory");
}
__device__ __forceinline__ void dma16_m0_sc1(rsrc_t rs, unsigned voff, unsigned soff) {
    asm volatile("buffer_load_dwordx4 %0, %1, %2 offen sc1 lds" ::"v"(voff), "s"(rs), "s"(soff) : "memory");
}
__device__ __forceinline__ void aload16(v4u& d, rsrc_t rs, unsigned voff) {
    asm volatile("buffer_load_dwordx4 %0, %1, %2, 0 offen" : "=v"(d) : "v"(voff), "s"(rs) : "memory");
}

__device__ __forceinline__ void wait_words(rsrc_t rsW, unsigned voff, unsigned e) {
    unsigned t0, left = SPIN_MAX;
    asm volatile(
        "1:\n\t"
        "buffer_load_dword %0, %2, %3, 0 offen sc1\n\t"
        "s_waitcnt vmcnt(0)\n\t"
        "v_cmp_ne_u32 vcc, %4, %0\n\t"
        "s_nop 4\n\t"
        "s_cbranch_vccz 2f\n\t"
        "s_sleep 8\n\t"
        "s_sub_u32 %1, %1, 1\n\t"
        "s_cbranch_scc0 1b\n\t"
        "2:"
        : "=&v"(t0), "+s"(left)
        : "v"(voff), "s"(rsW), "s"(e)
        : "memory", "vcc", "scc");
}
__device__ __forceinline__ void dma4_sc1(rsrc_t rs, unsigned voff, unsigned lds_addr) {
    asm volatile("s_mov_b32 m0, %0\n\ts_nop 0\n\tbuffer_load_dword %1, %2, 0 offen sc1 lds" ::"s"(lds_addr), "v"(voff), "s"(rs) : "memory", "m0");
}
__device__ __forceinline__ unsigned add_count(const Params& p, int off, unsigned n) {
    return __hip_atomic_fetch_add((GAS unsigned*)(p.ws + off), n, RLX_AGENT);
}
template <int SLEEP>
__device__ __forceinline__ void wait_ready(const Params& p, int off, unsigned e) {
    GAS unsigned* c = (GAS unsigned*)(p.ws + off);
    for (unsigned it = 0; it < SPIN_MAX; ++it) {
        if ((unsigned)__builtin_amdgcn_readfirstlane(__hip_atomic_load(c, RLX_AGENT)) == e) break;
        __builtin_amdgcn_s_sleep(SLEEP);
    }
    __builtin_amdgcn_fence(__ATOMIC_ACQUIRE, "wavefront");
}
__device__ __forceinline__ void lds_arrive(unsigned off) {
    if ((threadIdx.x & 63) == 0) (void)__hip_atomic_fetch_add((LAS unsigned*)(uintptr_t)off, 1u, __ATOMIC_RELAXED, __HIP_MEMORY_SCOPE_WORKGROUP);
}
__device__ __forceinline__ void lds_wait(unsigned off, unsigned n) {
    for (unsigned it = 0; it < (SPIN_MAX << 4); ++it) {
        if ((unsigned)__builtin_amdgcn_readfirstlane(__hip_atomic_load((LAS unsigned*)(uintptr_t)off, __ATOMIC_RELAXED, __HIP_MEMORY_SCOPE_WORKGROUP)) >= n) break;
        __builtin_amdgcn_s_sleep(1);
    }
    asm volatile("" ::: "memory");
}
__device__ __forceinline__ void set_flag(const Params& p, int off, int idx, unsigned e) {
    __hip_atomic_store((GAS unsigned*)(p.ws + off) + idx, e, RLX_AGENT);
}

constexpr int G1_STAGE = 40960, G1_G = 16384, G1_U = 16384 + 12288;
constexpr int PREP_OFF = 3 * G1_STAGE;
constexpr int CV_XR = 0, CV_XRS = 4608;
constexpr int CV_CS = 4160, CV_HP = 3 * CV_XRS, CV_HPS = 4 * CV_CS;
constexpr int G2_Y = CV_HP + 3 * CV_HPS;
constexpr int CV_RAW = 2 * G1_STAGE;
constexpr int DUMMY_W = 2 * G1_STAGE;
constexpr int DUMMY_X = 2 * G1_STAGE + G1_G;
constexpr int FLG_SCR = 102400;
constexpr int G2_RED = 98304;
constexpr int LC_B = 163808, LC_C = 163824;
constexpr int LDS_BYTES = 163840;
static_assert(G2_Y + 80 * 128 <= CV_RAW && G2_RED + 60 * 1024 <= LC_B, "lds");

__global__ void __launch_bounds__(256, 1) k_fused(Params p) {
    extern __shared__ __attribute__((aligned(16))) unsigned char smem[];
    const int tid = threadIdx.x, lane = tid & 63, w = __builtin_amdgcn_readfirstlane(tid >> 6);
    const int bid = blockIdx.x;
    const int xcd = bid & 7, bi = bid >> 3, mq = xcd >> 1, half = xcd & 1;
    STAMP(p, 0);
    rsrc_t rsW = mkrs(p.ws, WS_BYTES);
    rsrc_t rsQ = mkrs(p.q, SEQ * DM * 2);

    const int mt1 = 4 * mq + (bi & 3), ft = 8 * half + (bi >> 2);
    const int wm = w & 1, wf = w >> 1;
    const int rot1 = ((ft & 7) + ((5 * (mt1 & 3) + 1) >> 1)) % 10;
    rsrc_t rsWg = mkrs(p.wg, DM * FF * 2), rsWu = mkrs(p.wu, DM * FF * 2), rsX = rsQ;
    PIN_S(rsW); PIN_S(rsWg); PIN_S(rsWu); PIN_S(rsX);
    unsigned xoff[4], goff[3], uoff[3];
#pragma unroll
    for (int n = 0; n < 4; ++n) {
        const int row = 32 * w + 8 * n + (lane >> 3);
        xoff[n] = (unsigned)((128 * mt1 + row) * 1280 + (((lane & 7) ^ ((4 * n + (lane >> 4)) & 7)) << 4));
    }
#pragma unroll
    for (int n = 0; n < 3; ++n) {
        const int Lc = 64 * (3 * w + n) + lane, k = Lc / 12, c = Lc - 12 * k;
        goff[n] = (unsigned)(k * 3072 + 192 * ft + 16 * c);
        uoff[n] = (unsigned)(k * 3072 + 192 * ft + 16 * (c ^ 2));
    }
    int kap = rot1;
#pragma unroll
    for (int t = 0; t < 3; ++t) {
#pragma unroll
        for (int n = 0; n < 4; ++n) dma16_sc1(rsX, xoff[n], kap * 128, t * G1_STAGE + (4 * w + n) * 1024);
#pragma unroll
        for (int n = 0; n < 3; ++n) {
            dma16(rsWg, goff[n], kap * 64 * 3072, t * G1_STAGE + G1_G + (3 * w + n) * 1024);
            dma16(rsWu, uoff[n], kap * 64 * 3072, t * G1_STAGE + G1_U + (3 * w + n) * 1024);
        }
        kap = kap == 9 ? 0 : kap + 1;
    }
    const unsigned epoch = *(const __attribute__((address_space(4))) unsigned*)(uintptr_t)(p.ws + OFF_EPOCH) + 1u;
    f32x16 acc[3][2];
#pragma unroll
    for (int a = 0; a < 3; ++a)
#pragma unroll
        for (int b = 0; b < 2; ++b)
#pragma unroll
            for (int r = 0; r < 16; ++r) acc[a][b][r] = 0.f;
    STAMP(p, 1);

    v4u pre[15];
    const bool role_kv = bid < 160;
    auto prep_loads = [&]() {
    if (role_kv) {
        const rsrc_t rsK = mkrs(p.k, SEQ * DM * 2), rsV = mkrs(p.v, SEQ * DM * 2), rsS = mkrs(p.sk, DM * 4 * 2);
        const int tt = bid & 15, dtile = bid >> 4, cc = tid & 7, tg = tid >> 3;
        const int t0 = 128 * tt + 4 * tg, ch0 = 64 * dtile + 8 * cc;
        const unsigned o0 = (unsigned)((t0 * DM + ch0) * 2);
#pragma unroll
        for (int j = 0; j < 4; ++j) aload16(pre[3 + j], rsK, o0 + (unsigned)(j * DM * 2));
#pragma unroll
        for (int j = 0; j < 4; ++j) aload16(pre[7 + j], rsV, o0 + (unsigned)(j * DM * 2));
#pragma unroll
        for (int i = 0; i < 3; ++i) aload16(pre[i], rsK, (t0 - 3 + i >= 0) ? o0 + (unsigned)((i - 3) * DM * 2) : OOB);
#pragma unroll
        for (int i = 0; i < 4; ++i) aload16(pre[11 + i], rsS, (unsigned)(ch0 * 8 + 16 * i));
    } else {
        const int idx = bid - 160;
        const rsrc_t rsD = mkrs(p.wd, FF * DM * 2);
        const int dt = idx & 7, ks = 4 * (idx >> 3) + w;
#pragma unroll
        for (int i = 0; i < 5; ++i) {
            const int c = lane + 64 * i, row = c / 10, col = c - 10 * row;
            aload16(pre[i], rsD, (unsigned)(((32 * ks + row) * DM + 80 * dt + 8 * col) * 2));
        }
#pragma unroll
        for (int i = 5; i < 15; ++i) aload16(pre[i], rsD, OOB);
    }

    };
    auto prep_compute = [&](unsigned scr) {
        asm volatile("" : "+v"(pre[0]), "+v"(pre[1]), "+v"(pre[2]), "+v"(pre[3]), "+v"(pre[4]), "+v"(pre[5]), "+v"(pre[6]), "+v"(pre[7]), "+v"(pre[8]), "+v"(pre[9]),
                     "+v"(pre[10]), "+v"(pre[11]), "+v"(pre[12]), "+v"(pre[13]), "+v"(pre[14]) :: "memory");
    if (bid < 24 && tid == 0) set_flag(p, bid < 16 ? OFF_CB + 256 * bid : OFF_CC + 256 * (bid - 16), 0, 0u);
    if (role_kv) {
        const int tt = bid & 15, dtile = bid >> 4, cc = tid & 7, tg = tid >> 3;
        const unsigned wofs = (scr + (unsigned)((((15 - (tg >> 1)) ^ cc) << 4) + ((tg & 1) ^ 1) * 8 + cc * 2048));
#pragma unroll
        for (int e2 = 0; e2 < 4; ++e2) {
            const unsigned a01 = pre[11 + e2][0], a23 = pre[11 + e2][1], b01 = pre[11 + e2][2], b23 = pre[11 + e2][3];
            const f32x2 w0 = {blo(a01), blo(b01)}, w1 = {bhi(a01), bhi(b01)}, w2 = {blo(a23), blo(b23)}, w3 = {bhi(a23), bhi(b23)};
            f32x2 kf[7], r[4];
#pragma unroll
            for (int i = 0; i < 7; ++i) kf[i] = f32x2{blo(pre[i][e2]), bhi(pre[i][e2])};
#pragma unroll
            for (int j = 0; j < 4; ++j) {
                f32x2 ks = kf[j] * w3;
                ks = __builtin_elementwise_fma(kf[j + 1], w2, ks);
                ks = __builtin_elementwise_fma(kf[j + 2], w1, ks);
                ks = __builtin_elementwise_fma(kf[j + 3], w0, ks);
                r[j] = ks * f32x2{blo(pre[7 + j][e2]), bhi(pre[7 + j][e2])};
            }
            lds_st<v2u>(wofs + (2 * e2) * 256, v2u{pk2(r[3].x, r[2].x), pk2(r[1].x, r[0].x)});
            lds_st<v2u>(wofs + (2 * e2 + 1) * 256, v2u{pk2(r[3].y, r[2].y), pk2(r[1].y, r[0].y)});
        }
        WAIT_L(0);
        BAR();
#pragma unroll
        for (int i = 0; i < 4; ++i) {
            const int idx = tid + 256 * i, ch = idx >> 4, c = idx & 15;
            const v4u val = lds_ld<v4u>(scr + (unsigned)(ch * 256 + ((c ^ (ch >> 3)) << 4)));
            bst16<SC1>(rsW, (unsigned)(OFF_KVR + ((64 * dtile + ch) * SEQ + (1920 - 128 * tt) + 8 * c) * 2), 0, val);
        }
    } else {
        const int idx = bid - 160;
        const int dt = idx & 7, ks = 4 * (idx >> 3) + w;
        const unsigned T = scr + (unsigned)(w * 5120);
#pragma unroll
        for (int i = 0; i < 5; ++i) lds_st<v4u>(T + (lane + 64 * i) * 16, pre[i]);
        asm volatile("" ::: "memory");
        const int g = lane >> 4, q = (lane >> 2) & 3, pp = lane & 3;
        const int row = 16 * (g >> 1) + 4 * (g & 1) + q;
#pragma unroll
        for (int at = 0; at < 5; ++at) {
            const v2u lo = tr_read(T + row * 160 + (16 * at + 4 * pp) * 2);
            const v2u hi = tr_read(T + (row + 8) * 160 + (16 * at + 4 * pp) * 2);
            bst16<SC1>(rsW, (unsigned)(OFF_WDT + ((dt * 48 + ks) * 5 + at) * 1024 + lane * 16), 0, v4u{lo.x, lo.y, hi.x, hi.y});
        }
        BAR();
    }

        WAIT_L(0);
        BAR();
    };

    const int nch = bid < 128 ? 3 : 2, ch = (bid < 128 ? 3 * bid : 384 + 2 * (bid - 128)) + w;
    const bool has_ch = w < nch;
    const int chs = has_ch ? ch : 0;
    const int l31 = lane & 31, hh = lane >> 5;
    const int mt2 = 8 * mq + (bi & 7), dt = 4 * half + (bi >> 3);
    unsigned favo;
    {
        const int c0 = ch - w, g0 = c0 / 80;
        int who = 160 + dt + 8 * min(max(lane - 16, 0), 11);
        if (lane < 16 && has_ch) who = 16 * (ch >> 6) + lane;
        if (lane == 28) who = mt1;
        if (lane == 29) who = 16 + g0;
        if (lane == 30) who = 16 + min(g0 + 1, 7);
        favo = (unsigned)(OFF_FA + 128 * who);
    }
    {
        const int brow = 64 * wm + l31;
        unsigned ba[4];
#pragma unroll
        for (int kk = 0; kk < 4; ++kk) ba[kk] = (unsigned)(brow * 128 + (((2 * kk + hh) ^ ((brow >> 1) & 7)) << 4));
        const int G = lane >> 4, q = (lane >> 2) & 3, pp = lane & 3;
        unsigned aa[3];
#pragma unroll
        for (int fb = 0; fb < 3; ++fb) {
            const int fbg = 3 * wf + fb;
            aa[fb] = (unsigned)(((G & 1) ? G1_U + (fbg ^ 1) * 32 : G1_G + fbg * 32) + (8 * (G >> 1) + q) * 192 + 8 * pp);
        }
        v4u fb0[2], fb1[2];
        v2u fa0[3][2], fa1[3][2];
#define RDB(d, kk, mt) d = lds_ld<v4u>(ba[kk] + (mt) * 4096)
#define RDA(d, kk, fb, hi) d[fb][hi] = tr_read(aa[fb] + (kk) * 16 * 192 + (hi) * 4 * 192)
#define MM(A, B, fb, mt) acc[fb][mt] = mfma32(v4u{A[fb][0].x, A[fb][0].y, A[fb][1].x, A[fb][1].y}, B[mt], acc[fb][mt])
        WAIT_V(20);
        BAR();
        STAMP(p, 2);
        RDB(fb0[0], 0, 0); RDA(fa0, 0, 0, 0); RDA(fa0, 0, 0, 1); RDB(fb0[1], 0, 1); RDA(fa0, 0, 1, 0); RDA(fa0, 0, 1, 1); RDA(fa0, 0, 2, 0); RDA(fa0, 0, 2, 1);
        int st = 0;
        unsigned mx = 3 * G1_STAGE + 4096u * w, mw = 3 * G1_STAGE + 3072u * w;
        unsigned sx = kap * 128, sw = kap * 64 * 3072;
#define STEP(FA, FB, GA, GB, kn, D0, D1, D2)                                                     \
            MM(FA, FB, 0, 0); SB(); RDB(GB[0], kn, 0); RDA(GA, kn, 0, 0); SB();                 \
            MM(FA, FB, 0, 1); SB(); RDA(GA, kn, 0, 1); RDB(GB[1], kn, 1); SB();                 \
            MM(FA, FB, 1, 0); SB(); RDA(GA, kn, 1, 0); RDA(GA, kn, 1, 1); D0##_M; SB();         \
            MM(FA, FB, 1, 1); SB(); D0##_L; RDA(GA, kn, 2, 0); D1##_M; SB();                    \
            MM(FA, FB, 2, 0); SB(); D1##_L; RDA(GA, kn, 2, 1); D2##_M; SB();                    \
            MM(FA, FB, 2, 1); SB(); D2##_L; SB();
#define X0_M SET_M0(mx, 0)
#define X0_L dma16_m0_sc1(rsX, xoff[0], sx)
#define X1_M SET_M0(mx, 1024)
#define X1_L dma16_m0_sc1(rsX, xoff[1], sx)
#define X2_M SET_M0(mx, 2048)
#define X2_L dma16_m0_sc1(rsX, xoff[2], sx)
#define X3_M SET_M0(mx, 3072)
#define X3_L dma16_m0_sc1(rsX, xoff[3], sx)
#define G0_M SET_M0(mw, G1_G)
#define G0_L dma16_m0(rsWg, goff[0], sw)
#define G1_M SET_M0(mw, G1_G + 1024)
#define G1_L dma16_m0(rsWg, goff[1], sw)
#define G2_M SET_M0(mw, G1_G + 2048)
#define G2_L dma16_m0(rsWg, goff[2], sw)
#define U0_M SET_M0(mw, G1_U)
#define U0_L dma16_m0(rsWu, uoff[0], sw)
#define U1_M SET_M0(mw, G1_U + 1024)
#define U1_L dma16_m0(rsWu, uoff[1], sw)
#define U2_M SET_M0(mw, G1_U + 2048)
#define U2_L dma16_m0(rsWu, uoff[2], sw)
#define KBODY(WAITV) { \
            const int dlt = (st == 3 * G1_STAGE) ? -3 * G1_STAGE : G1_STAGE; \
            SB(); \
            STEP(fa0, fb0, fa1, fb1, 1, X0, X1, X2) \
            STEP(fa1, fb1, fa0, fb0, 2, X3, G0, U0) \
            STEP(fa0, fb0, fa1, fb1, 3, G1, U1, G2) \
            U2_M; SB(); \
            MM(fa1, fb1, 0, 0); SB(); U2_L; ba[0] += dlt; aa[0] += dlt; SB(); \
            MM(fa1, fb1, 0, 1); SB(); aa[1] += dlt; aa[2] += dlt; \
            kap = kap == 9 ? 0 : kap + 1; \
            mx = (unsigned)st + 4096u * w; mw = (unsigned)st + 3072u * w; sx = kap * 128; sw = kap * 64 * 3072; \
            SB(); \
            MM(fa1, fb1, 1, 0); SB(); \
            WAIT_L(0); \
            WAITV; \
            BAR(); \
            RDB(fb0[0], 0, 0); RDA(fa0, 0, 0, 0); RDA(fa0, 0, 0, 1); SB(); \
            MM(fa1, fb1, 1, 1); SB(); RDB(fb0[1], 0, 1); RDA(fa0, 0, 1, 0); RDA(fa0, 0, 1, 1); SB(); \
            MM(fa1, fb1, 2, 0); SB(); RDA(fa0, 0, 2, 0); RDA(fa0, 0, 2, 1); ba[1] += dlt; SB(); \
            MM(fa1, fb1, 2, 1); SB(); ba[2] += dlt; ba[3] += dlt; \
            st += dlt; \
            }
        KBODY(WAIT_V(20))
        prep_loads();
#pragma unroll 1
        for (int kt = 1; kt < 9; ++kt) {
            KBODY(if (__builtin_expect(kt == 1, 0)) WAIT_V(35); else WAIT_V(20))
            if (__builtin_expect(kt <= 4, 0)) {
                if (kt == 2) { STAMP(p, 15); prep_compute((unsigned)(st == 0 ? 3 * G1_STAGE : st - G1_STAGE)); STAMP(p, 16); }
                if (kt == 4) __builtin_amdgcn_raw_buffer_store_b32(epoch, rsW, tid == 0 ? (unsigned)(OFF_FA + 128 * bid) : OOB, 0, SC1);
            }
            if (__builtin_expect(kt >= 6, 0)) {
                mw = DUMMY_W; sw = 0x40000000u;
                sx = (kt == 6 && has_ch) ? 0u : 0x40000000u;
                mx = (kt == 6) ? CV_RAW + 4096u * w : (unsigned)DUMMY_X;
                if (kt == 6) {
                    rsX = mkrs(p.hk, DM * SEQ * 2);
#pragma unroll
                    for (int n = 0; n < 4; ++n) xoff[n] = (unsigned)(chs * SEQ * 2 + n * 1024 + lane * 16);
                    dma4_sc1(rsW, favo, FLG_SCR + 256u * w);
                }
            }
        }
        {
            const unsigned flv = lds_ld<unsigned>((unsigned)(FLG_SCR + 256 * w + lane * 4));
            if (__builtin_amdgcn_ballot_w64(flv != epoch) != 0ull) wait_words(rsW, favo, epoch);
            if (has_ch) {
#pragma unroll
                for (int n = 0; n < 4; ++n) dma16_sc1(rsW, (unsigned)(lane * 16), (unsigned)(OFF_KVR + ch * SEQ * 2 + n * 1024), (unsigned)(CV_XR + CV_XRS * w + n * 1024));
            }
#define N_M do {} while (0)
#define N_L do {} while (0)
            SB();
            STEP(fa0, fb0, fa1, fb1, 1, N, N, N)
            STEP(fa1, fb1, fa0, fb0, 2, N, N, N)
            STEP(fa0, fb0, fa1, fb1, 3, N, N, N)
            MM(fa1, fb1, 0, 0); MM(fa1, fb1, 0, 1); MM(fa1, fb1, 1, 0); MM(fa1, fb1, 1, 1); MM(fa1, fb1, 2, 0); MM(fa1, fb1, 2, 1);
        }
#undef MM
#undef STEP
#undef KBODY
    }
    STAMP(p, 3);
    if (tid == 0) { lds_st<unsigned>(LC_B, 0u); lds_st<unsigned>(LC_C, 0u); }
    WAIT_L(0);
    BAR();
    const int rot2 = (3 * (bi & 7) / 2 + 3 * (bi >> 3)) % 12;
    unsigned a2o[12];
#pragma unroll
    for (int s = 0; s < 12; ++s) { const int ks = s + rot2; a2o[s] = (unsigned)(12 * w + (ks >= 12 ? ks - 12 : ks)) * 1024u; }
    const unsigned a2base = (unsigned)(OFF_WDT + dt * 48 * 5 * 1024);
    v4u A2[12][5];
#pragma unroll
    for (int slot = 0; slot < 8; ++slot) A2[slot / 5][slot % 5] = bld16<SC1>(rsW, lane * 16, a2base + a2o[slot / 5] * 5 + (slot % 5) * 1024);
    SB();
#pragma unroll
    for (int fb = 0; fb < 3; ++fb)
#pragma unroll
        for (int mt = 0; mt < 2; ++mt) {
            const f32x16& a = acc[fb][mt];
            v4u o;
#pragma unroll
            for (int j = 0; j < 4; ++j) o[j] = pk2(a[2 * j] * a[8 + 2 * j], a[2 * j + 1] * a[8 + 2 * j + 1]);
            const int Ft = 6 * ft + 3 * wf + fb;
            const int mrow = 128 * mt1 + 64 * wm + 32 * mt + l31;
            const int pos = (lane & 15) + 16 * (2 * (Ft & 1) + hh);
            bst16<SC1>(rsW, (unsigned)(OFF_HF + ((mrow >> 4) * 48 + (Ft >> 1)) * 1024 + pos * 16), 0, o);
        }
    STAMP(p, 4);

    if (has_ch) {
        const unsigned HP = (unsigned)(CV_HP + w * CV_HPS), XR = (unsigned)(CV_XR + w * CV_XRS), RAW = (unsigned)(CV_RAW + w * 4096);
        const unsigned sdv = p.sd[ch];
        WAIT_V(14);
#pragma unroll
        for (int ps = 0; ps < 5; ++ps) {
            const int n = 64 * ps + lane;
            v4u c0 = {0, 0, 0, 0}, c1 = {0, 0, 0, 0};
            if (ps < 4) {
                c0 = lds_ld<v4u>(RAW + 16 * (ps == 0 ? max(n - 2, 0) : n - 2));
                c1 = lds_ld<v4u>(RAW + 16 * (ps == 0 ? max(n - 1, 0) : n - 1));
                if (ps == 0) { if (n < 2) c0 = v4u{0, 0, 0, 0}; if (n < 1) c1 = v4u{0, 0, 0, 0}; }
            } else {
                c0 = lds_ld<v4u>(RAW + 16 * (254 + (lane & 1)));
                c1 = lds_ld<v4u>(RAW + 16 * 255);
                if (lane & 1) c1 = v4u{0, 0, 0, 0};
            }
            const unsigned Dw[6] = {c0.x, c0.y, c0.z, c0.w, c1.x, c1.y};
            v4u o0, o1, o2, o3;
#pragma unroll
            for (int q = 0; q < 4; ++q) {
                o0[q] = __builtin_amdgcn_alignbit(Dw[q + 1], Dw[q], 16);
                o1[q] = Dw[q + 1];
                o2[q] = __builtin_amdgcn_alignbit(Dw[q + 2], Dw[q + 1], 16);
                o3[q] = Dw[q + 2];
            }
            if (ps < 4 || lane < 2) {
                lds_st<v4u>(HP + 0 * CV_CS + 16 * n, o0);
                lds_st<v4u>(HP + 1 * CV_CS + 16 * n, o1);
                lds_st<v4u>(HP + 2 * CV_CS + 16 * n, o2);
                lds_st<v4u>(HP + 3 * CV_CS + 16 * n, o3);
            }
        }
        if (lane < 32) lds_st<v4u>(XR + 4096 + 16 * lane, v4u{0, 0, 0, 0});
        asm volatile("" ::: "memory");
        STAMP(p, 5);

        const int r = lane & 15, g = lane >> 4;
        unsigned ap0 = HP + (unsigned)((r & 3) * CV_CS + 2 * ((r & ~3) + 8 * g));
        unsigned ap1 = ap0 + 8;
        PIN_V(ap0); PIN_V(ap1);
        const unsigned bp = XR + (unsigned)(2 * (240 - 16 * r + 8 * g));
        f32x4 cacc[8];
#pragma unroll
        for (int i = 0; i < 8; ++i) cacc[i] = f32x4{0.f, 0.f, 0.f, 0.f};
        v4u CA[2][8], CB[2][8];
        auto cvrd = [&](int set, int s, int i) {
            CB[set][i] = lds_ld<v4u>(bp + 512 * (7 - i) + 64 * s);
            const v2u lo = lds_ld<v2u>(ap0 + 512 * i + 64 * s), hi = lds_ld<v2u>(ap1 + 512 * i + 64 * s);
            CA[set][i] = v4u{lo.x, lo.y, hi.x, hi.y};
        };
#pragma unroll
        for (int i = 0; i < 8; ++i) cvrd(0, 0, i);
#pragma unroll
        for (int s = 0; s < 8; ++s) {
            const int cur = s & 1;
            SB();
#pragma unroll
            for (int gm = 0; gm < 8; ++gm) {
#pragma unroll
                for (int be = 0; be <= gm; ++be) cacc[gm] = mfma16(CA[cur][be], CB[cur][gm - be], cacc[gm]);
                SB();
                if (s < 7) cvrd(cur ^ 1, s + 1, gm);
                { const int slot = 8 + 8 * s + gm; if (slot < 60) A2[slot / 5][slot % 5] = bld16<SC1>(rsW, lane * 16, a2base + a2o[slot / 5] * 5 + (slot % 5) * 1024); }
                SB();
            }
            if (s == 1) {
                WAIT_V(16);
                lds_arrive(LC_B);
            }
        }
        STAMP(p, 6);
        const float Dc = blo(sdv);
#pragma unroll
        for (int gm = 0; gm < 8; ++gm) {
            const int t0 = 256 * gm + 16 * r + 4 * g;
            const v2u kvp = lds_ld<v2u>(XR + 2 * (2044 - t0));
            const float y0 = cacc[gm][0] + bhi(kvp.y) * Dc, y1 = cacc[gm][1] + blo(kvp.y) * Dc, y2 = cacc[gm][2] + bhi(kvp.x) * Dc, y3 = cacc[gm][3] + blo(kvp.x) * Dc;
            lds_st<v2u>(HP + 2 * t0, v2u{pk2(y0, y1), pk2(y2, y3)});
        }
        asm volatile("" ::: "memory");
#pragma unroll
        for (int ps = 0; ps < 4; ++ps) {
            const int n = 64 * ps + lane;
            bst16<SC1>(rsW, (unsigned)(OFF_YT + (ch * SEQ + 8 * n) * 2), 0, lds_ld<v4u>(HP + 16 * n));
        }
    } else {
        if (w == 3) {
            const rsrc_t rsG = mkrs(p.gw, DM * 2);
            const int c16 = lane & 15, row0 = 128 * mt1 + 8 * ft + (lane >> 4);
            v4u gwv[5], xa[5], xb[5];
#pragma unroll
            for (int i = 0; i < 5; ++i) {
                gwv[i] = bld16<0>(rsG, (unsigned)(16 * (c16 + 16 * i)), 0);
                xa[i] = bld16<0>(rsQ, (unsigned)(row0 * DM * 2 + 16 * (c16 + 16 * i)), 0);
                xb[i] = bld16<0>(rsQ, (unsigned)((row0 + 4) * DM * 2 + 16 * (c16 + 16 * i)), 0);
            }
            float s0 = 0.f, s1 = 0.f;
#pragma unroll
            for (int i = 0; i < 5; ++i)
#pragma unroll
                for (int e = 0; e < 4; ++e) { s0 = dot2(xa[i][e], gwv[i][e], s0); s1 = dot2(xb[i][e], gwv[i][e], s1); }
#pragma unroll
            for (int m = 1; m < 16; m <<= 1) { s0 += __shfl_xor(s0, m); s1 += __shfl_xor(s1, m); }
            const float g0 = rb(1.0f / (1.0f + __expf(-rb(s0)))), g1 = rb(1.0f / (1.0f + __expf(-rb(s1))));
            __builtin_amdgcn_raw_buffer_store_b32(__builtin_bit_cast(unsigned, g0), rsW, c16 == 0 ? (unsigned)(OFF_GATE + row0 * 4) : OOB, 0, SC1);
            __builtin_amdgcn_raw_buffer_store_b32(__builtin_bit_cast(unsigned, g1), rsW, c16 == 0 ? (unsigned)(OFF_GATE + (row0 + 4) * 4) : OOB, 0, SC1);
        }
        WAIT_V(0);
        lds_arrive(LC_B);
        lds_wait(LC_B, 4);
        if (w == 3) {
            if (lane == 0) {
                const unsigned old = add_count(p, OFF_CB + 256 * mt1, 1u);
                if (old == 15u) set_flag(p, OFF_RB + 256 * mt1, 0, epoch);
            }
            if (bid == 0) {
#pragma unroll 1
                for (int i = 0; i < 4; ++i) wait_words(rsW, (unsigned)(OFF_FA + 128 * (64 * i + lane)), epoch);
                if (lane == 0) set_flag(p, OFF_EPOCH, 0, epoch);
            }
        }
#pragma unroll
        for (int slot = 8; slot < 60; ++slot) A2[slot / 5][slot % 5] = bld16<SC1>(rsW, lane * 16, a2base + a2o[slot / 5] * 5 + (slot % 5) * 1024);
    }
    STAMP(p, 7);

    wait_ready<4>(p, OFF_RB + 256 * (mt2 >> 1), epoch);
    STAMP(p, 8);
    constexpr int W2 = 8;
    unsigned b2off[4];
#pragma unroll
    for (int n = 0; n < 4; ++n) b2off[n] = (unsigned)(OFF_HF + (4 * mt2 + ((w + n) & 3)) * 48 * 1024);
    v4u B2[W2][4];
#pragma unroll
    for (int s = 0; s < W2; ++s)
#pragma unroll
        for (int n = 0; n < 4; ++n) B2[s][n] = bld16<SC1>(rsW, lane * 16, b2off[n] + a2o[s]);
    SB();
    if (has_ch) {
        WAIT_V(32);
        lds_arrive(LC_C);
    }
    f32x4 acc2[5][4];
#pragma unroll
    for (int a = 0; a < 5; ++a)
#pragma unroll
        for (int n = 0; n < 4; ++n) acc2[a][n] = f32x4{0.f, 0.f, 0.f, 0.f};
    const int t = 64 * mt2 + 16 * w + (lane & 15), g4 = lane >> 4;
    v2u qv[5];
    float gt;
#pragma unroll
    for (int s = 0; s < 12; ++s) {
        v4u cb[4];
#pragma unroll
        for (int n = 0; n < 4; ++n) cb[n] = B2[s % W2][n];
        if (s + W2 < 12) {
#pragma unroll
            for (int n = 0; n < 4; ++n) B2[s % W2][n] = bld16<SC1>(rsW, lane * 16, b2off[n] + a2o[s + W2]);
        }
        if (s == 6) {
#pragma unroll
            for (int a = 0; a < 5; ++a) qv[a] = *(const GAS v2u*)(p.q + (size_t)t * DM + 80 * dt + 16 * a + 4 * g4);
            gt = __builtin_bit_cast(float, __builtin_amdgcn_raw_buffer_load_b32(rsW, (unsigned)(OFF_GATE + t * 4), 0, SC1));
        }
#pragma unroll
        for (int n = 0; n < 4; ++n)
#pragma unroll
            for (int a = 0; a < 5; ++a) acc2[a][n] = mfma16(A2[s][a], cb[n], acc2[a][n]);
        SB();
    }
    STAMP(p, 9);
    lds_wait(LC_B, 4);
#pragma unroll
    for (int a = 0; a < 5; ++a)
#pragma unroll
        for (int j = 1; j < 4; ++j) lds_st<f32x4>((unsigned)(G2_RED + ((((w + j) & 3) * 5 + a) * 3 + (j - 1)) * 1024 + lane * 16), acc2[a][j]);
    if (w == 3) {
        STAMP(p, 12);
        lds_wait(LC_C, (unsigned)nch);
        {
            const int c0 = ch - 3, g0 = c0 / 80, n0 = min(nch, 80 * (g0 + 1) - c0), gi = g0 + lane, ni = lane == 0 ? n0 : nch - n0;
            if (lane < 2 && ni > 0) {
                const unsigned old = add_count(p, OFF_CC + 256 * gi, (unsigned)ni);
                if (old + (unsigned)ni == 80u) set_flag(p, OFF_RC + 256 * gi, 0, epoch);
            }
        }
        wait_ready<4>(p, OFF_RC + 256 * dt, epoch);
        STAMP(p, 13);
        v4u yv[10];
#pragma unroll
        for (int it = 0; it < 10; ++it) {
            const int c = lane + 64 * it;
            yv[it] = bld16<SC1>(rsW, (unsigned)(OFF_YT + ((80 * dt + (c >> 3)) * SEQ + 64 * mt2 + 8 * (c & 7)) * 2), 0);
        }
#pragma unroll
        for (int it = 0; it < 10; ++it) lds_st<v4u>((unsigned)(G2_Y + (lane + 64 * it) * 16), yv[it]);
    }
    WAIT_L(0);
    BAR();
    STAMP(p, 10);
    const float omg = rb(1.0f - gt);
    const int q = (lane >> 2) & 3, pp = lane & 3;
#pragma unroll
    for (int a = 0; a < 5; ++a) {
        f32x4 b = acc2[a][0];
#pragma unroll
        for (int j = 0; j < 3; ++j) b += lds_ld<f32x4>((unsigned)(G2_RED + ((w * 5 + a) * 3 + j) * 1024 + lane * 16));
        const v2u y = tr_read((unsigned)(G2_Y + (16 * a + 4 * g4 + q) * 128 + 32 * w + 8 * pp));
        const int d0 = 80 * dt + 16 * a + 4 * g4;
        const float o0 = gt * (blo(qv[a].x) * blo(y.x)) + omg * b[0], o1 = gt * (bhi(qv[a].x) * bhi(y.x)) + omg * b[1];
        const float o2 = gt * (blo(qv[a].y) * blo(y.y)) + omg * b[2], o3 = gt * (bhi(qv[a].y) * bhi(y.y)) + omg * b[3];
        *(GAS v2u*)(p.out + (size_t)t * DM + d0) = v2u{pk2(o0, o1), pk2(o2, o3)};
    }
    STAMP(p, 11);
}

extern "C" void kernel_launch(void* const* d_in, const int* in_sizes, int n_in, void* d_out, int out_size, void* d_ws, size_t ws_size, hipStream_t stream) {
    Params p;
    p.q = (const u16*)d_in[0]; p.k = (const u16*)d_in[1]; p.v = (const u16*)d_in[2]; p.sk = (const u16*)d_in[3];
    p.hk = (const u16*)d_in[4]; p.sd = (const u16*)d_in[5];
    p.wg = (const u16*)d_in[7]; p.wu = (const u16*)d_in[8]; p.wd = (const u16*)d_in[9]; p.gw = (const u16*)d_in[10];
    p.out = (u16*)d_out; p.ws = (unsigned char*)d_ws;
    (void)hipFuncSetAttribute((const void*)k_fused, hipFuncAttributeMaxDynamicSharedMemorySize, LDS_BYTES);
    k_fused<<<256, 256, LDS_BYTES, stream>>>(p);
}
```

```cpp
#include <hip/hip_runtime.h>
#include <stdint.h>

#define LAS __attribute__((address_space(3)))
#define GAS __attribute__((address_space(1)))
typedef unsigned short u16;
typedef unsigned v4u __attribute__((ext_vector_type(4)));
typedef unsigned v2u __attribute__((ext_vector_type(2)));
typedef __bf16 bf8 __attribute__((ext_vector_type(8)));
typedef __bf16 bf2 __attribute__((ext_vector_type(2)));
typedef short s4 __attribute__((ext_vector_type(4)));
typedef float f32x2 __attribute__((ext_vector_type(2)));
typedef float f32x4 __attribute__((ext_vector_type(4)));
typedef float f32x16 __attribute__((ext_vector_type(16)));
typedef __amdgpu_buffer_rsrc_t rsrc_t;

constexpr int SEQ = 2048, DM = 640, FF = 1536;
constexpr int OFF_EPOCH = 0;
constexpr int OFF_FA = 32768;
constexpr int OFF_CB = 2048;
constexpr int OFF_CC = 8192;
constexpr int OFF_RB = 12288;
constexpr int OFF_RC = 16384;
constexpr int OFF_STAMP = 32 * 1024 * 1024;
constexpr int OFF_KVR = 65536;
constexpr int OFF_WDT = OFF_KVR + DM * SEQ * 2;
constexpr int OFF_HF = OFF_WDT + FF * DM * 2;
constexpr int OFF_YT = OFF_HF + SEQ * FF * 2;
constexpr int OFF_GATE = OFF_YT + DM * SEQ * 2;
constexpr int WS_BYTES = OFF_GATE + SEQ * 4;
constexpr int SC1 = 16;
constexpr unsigned OOB = 0x80000000u;
constexpr unsigned SPIN_MAX = 1u << 16;

struct Params {
    const u16 *q, *k, *v, *sk, *hk, *sd, *wg, *wu, *wd, *gw;
    u16* out;
    unsigned char* ws;
};

__device__ __forceinline__ unsigned pk2(float a, float b) {
    f32x2 x = {a, b};
    return __builtin_bit_cast(unsigned, __builtin_convertvector(x, bf2));
}
__device__ __forceinline__ float blo(unsigned u) { return __builtin_bit_cast(float, u << 16); }
__device__ __forceinline__ float bhi(unsigned u) { return __builtin_bit_cast(float, u & 0xffff0000u); }
__device__ __forceinline__ float rb(float x) { return blo(pk2(x, x)); }
__device__ __forceinline__ f32x4 mfma16(v4u a, v4u b, f32x4 c) {
    return __builtin_amdgcn_mfma_f32_16x16x32_bf16(__builtin_bit_cast(bf8, a), __builtin_bit_cast(bf8, b), c, 0, 0, 0);
}
__device__ __forceinline__ f32x16 mfma32(v4u a, v4u b, f32x16 c) {
    return __builtin_amdgcn_mfma_f32_32x32x16_bf16(__builtin_bit_cast(bf8, a), __builtin_bit_cast(bf8, b), c, 0, 0, 0);
}
__device__ __forceinline__ float dot2(unsigned a, unsigned b, float c) {
    return __builtin_amdgcn_fdot2_f32_bf16(__builtin_bit_cast(bf2, a), __builtin_bit_cast(bf2, b), c, false);
}
template <class T> __device__ __forceinline__ T lds_ld(unsigned a) { return *(LAS T*)(uintptr_t)a; }
template <class T> __device__ __forceinline__ void lds_st(unsigned a, T v) { *(LAS T*)(uintptr_t)a = v; }
__device__ __forceinline__ v2u tr_read(unsigned a) {
    return __builtin_bit_cast(v2u, __builtin_amdgcn_ds_read_tr16_b64_v4i16((LAS s4*)(uintptr_t)a));
}
__device__ __forceinline__ rsrc_t mkrs(const void* p, unsigned bytes) { return __builtin_amdgcn_make_buffer_rsrc((void*)p, 0, bytes, 0x00020000); }
template <int AUX> __device__ __forceinline__ v4u bld16(rsrc_t rs, unsigned voff, unsigned soff) { return __builtin_amdgcn_raw_buffer_load_b128(rs, voff, soff, AUX); }
template <int AUX> __device__ __forceinline__ void bst16(rsrc_t rs, unsigned voff, unsigned soff, v4u v) { __builtin_amdgcn_raw_buffer_store_b128(v, rs, voff, soff, AUX); }
#define WAIT_V(n) asm volatile("s_waitcnt vmcnt(" #n ")" ::: "memory")
#define WAIT_L(n) asm volatile("s_waitcnt lgkmcnt(" #n ")" ::: "memory")
#define BAR() do { asm volatile("" ::: "memory"); __builtin_amdgcn_s_barrier(); asm volatile("" ::: "memory"); } while (0)
#define SB() __builtin_amdgcn_sched_barrier(0)
#define PIN_S(x) asm volatile("" : "+s"(x))
#define PIN_V(x) asm volatile("" : "+v"(x))
#define RLX_AGENT __ATOMIC_RELAXED, __HIP_MEMORY_SCOPE_AGENT
#define STAMP(p, i) do {} while (0)

__device__ __forceinline__ void dma16(rsrc_t rs, unsigned voff, unsigned soff, unsigned lds_addr) {
    asm volatile("s_mov_b32 m0, %0\n\ts_nop 0\n\tbuffer_load_dwordx4 %1, %2, %3 offen lds" ::"s"(lds_addr), "v"(voff), "s"(rs), "s"(soff) : "memory", "m0");
}
__device__ __forceinline__ void dma16_sc1(rsrc_t rs, unsigned voff, unsigned soff, unsigned lds_addr) {
    asm volatile("s_mov_b32 m0, %0\n\ts_nop 0\n\tbuffer_load_dwordx4 %1, %2, %3 offen sc1 lds" ::"s"(lds_addr), "v"(voff), "s"(rs), "s"(soff) : "memory", "m0");
}
#define SET_M0(base, imm) asm volatile("s_add_u32 m0, %0, %1" ::"s"(base), "i"(imm) : "memory", "m0", "scc")
__device__ __forceinline__ void dma16_m0(rsrc_t rs, unsigned voff, unsigned soff) {
    asm volatile("buffer_load_dwordx4 %0, %1, %2 offen lds" ::"v"(voff), "s"(rs), "s"(soff) : "memory");
}
__device__ __forceinline__ void dma16_m0_sc1(rsrc_t rs, unsigned voff, unsigned soff) {
    asm volatile("buffer_load_dwordx4 %0, %1, %2 offen sc1 lds" ::"v"(voff), "s"(rs), "s"(soff) : "memory");
}
__device__ __forceinline__ void aload16(v4u& d, rsrc_t rs, unsigned voff) {
    asm volatile("buffer_load_dwordx4 %0, %1, %2, 0 offen" : "=v"(d) : "v"(voff), "s"(rs) : "memory");
}

__device__ __forceinline__ void wait_words(rsrc_t rsW, unsigned voff, unsigned e) {
    unsigned t0, left = SPIN_MAX;
    asm volatile(
        "1:\n\t"
        "buffer_load_dword %0, %2, %3, 0 offen sc1\n\t"
        "s_waitcnt vmcnt(0)\n\t"
        "v_cmp_ne_u32 vcc, %4, %0\n\t"
        "s_nop 4\n\t"
        "s_cbranch_vccz 2f\n\t"
        "s_sleep 8\n\t"
        "s_sub_u32 %1, %1, 1\n\t"
        "s_cbranch_scc0 1b\n\t"
        "2:"
        : "=&v"(t0), "+s"(left)
        : "v"(voff), "s"(rsW), "s"(e)
        : "memory", "vcc", "scc");
}
__device__ __forceinline__ void dma4_sc1(rsrc_t rs, unsigned voff, unsigned lds_addr) {
    asm volatile("s_mov_b32 m0, %0\n\ts_nop 0\n\tbuffer_load_dword %1, %2, 0 offen sc1 lds" ::"s"(lds_addr), "v"(voff), "s"(rs) : "memory", "m0");
}
__device__ __forceinline__ unsigned add_count(const Params& p, int off, unsigned n) {
    return __hip_atomic_fetch_add((GAS unsigned*)(p.ws + off), n, RLX_AGENT);
}
template <int SLEEP>
__device__ __forceinline__ void wait_ready(const Params& p, int off, unsigned e) {
    GAS unsigned* c = (GAS unsigned*)(p.ws + off);
    for (unsigned it = 0; it < SPIN_MAX; ++it) {
        if ((unsigned)__builtin_amdgcn_readfirstlane(__hip_atomic_load(c, RLX_AGENT)) == e) break;
        __builtin_amdgcn_s_sleep(SLEEP);
    }
    __builtin_amdgcn_fence(__ATOMIC_ACQUIRE, "wavefront");
}
__device__ __forceinline__ void lds_arrive(unsigned off) {
    if ((threadIdx.x & 63) == 0) (void)__hip_atomic_fetch_add((LAS unsigned*)(uintptr_t)off, 1u, __ATOMIC_RELAXED, __HIP_MEMORY_SCOPE_WORKGROUP);
}
__device__ __forceinline__ void lds_wait(unsigned off, unsigned n) {
    for (unsigned it = 0; it < (SPIN_MAX << 4); ++it) {
        if ((unsigned)__builtin_amdgcn_readfirstlane(__hip_atomic_load((LAS unsigned*)(uintptr_t)off, __ATOMIC_RELAXED, __HIP_MEMORY_SCOPE_WORKGROUP)) >= n) break;
        __builtin_amdgcn_s_sleep(1);
    }
    asm volatile("" ::: "memory");
}
__device__ __forceinline__ void set_flag(const Params& p, int off, int idx, unsigned e) {
    __hip_atomic_store((GAS unsigned*)(p.ws + off) + idx, e, RLX_AGENT);
}

constexpr int G1_STAGE = 40960, G1_G = 16384, G1_U = 16384 + 12288;
constexpr int PR_K = 3 * G1_STAGE;
constexpr int PR_V = PR_K + 11264;
constexpr int PR_S = PR_V + 10240;
constexpr int PR_D = PR_S + 1024;
constexpr int CV_XR = 0, CV_XRS = 4608;
constexpr int CV_CS = 4160, CV_HP = 3 * CV_XRS, CV_HPS = 4 * CV_CS;
constexpr int G2_Y = CV_HP + 3 * CV_HPS;
constexpr int CV_RAW = 2 * G1_STAGE;
constexpr int DUMMY_W = 2 * G1_STAGE;
constexpr int DUMMY_X = 2 * G1_STAGE + G1_G;
constexpr int FLG_SCR = 102400;
constexpr int G2_RED = 98304;
constexpr int LC_B = 163808, LC_C = 163824;
constexpr int LDS_BYTES = 163840;
static_assert(G2_Y + 80 * 128 <= CV_RAW && G2_RED + 60 * 1024 <= LC_B, "lds");

__global__ void __launch_bounds__(256, 1) k_fused(Params p) {
    extern __shared__ __attribute__((aligned(16))) unsigned char smem[];
    const int tid = threadIdx.x, lane = tid & 63, w = __builtin_amdgcn_readfirstlane(tid >> 6);
    const int bid = blockIdx.x;
    const int xcd = bid & 7, bi = bid >> 3, mq = xcd >> 1, half = xcd & 1;
    STAMP(p, 0);
    {
        unsigned long long pc;
        asm volatile("s_getpc_b64 %0" : "=s"(pc));
        const rsrc_t rsC = mkrs((const void*)(pc & ~255ull), 24576 - 256);
        dma16(rsC, (unsigned)(((bi + 32 * w) << 10) + lane * 16), 0, 160000 - 1024);
    }
    rsrc_t rsW = mkrs(p.ws, WS_BYTES);
    rsrc_t rsQ = mkrs(p.q, SEQ * DM * 2);

    const int ptt = bid & 31, pd8 = bid >> 5;
    {
        const rsrc_t rsK = mkrs(p.k, SEQ * DM * 2), rsV = mkrs(p.v, SEQ * DM * 2), rsS = mkrs(p.sk, DM * 4 * 2), rsD = mkrs(p.wd, FF * DM * 2);
        auto kdma = [&](int i) {
            const int c = 64 * i + lane, row = c / 10, col = c - 10 * row, t = 64 * ptt - 3 + row;
            dma16(rsK, (t >= 0 && c < 670) ? (unsigned)((t * DM + 80 * pd8 + 8 * col) * 2) : OOB, 0, PR_K + 1024 * i);
        };
        auto vdma = [&](int i) {
            const int c = 64 * i + lane, row = c / 10, col = c - 10 * row;
            dma16(rsV, (unsigned)(((64 * ptt + row) * DM + 80 * pd8 + 8 * col) * 2), 0, PR_V + 1024 * i);
        };
        auto ddma = [&](int j, int i) {
            const int g = 2 * bid + j, gdt = g / 48, gks = g - 48 * gdt;
            const int c = 64 * i + lane, row = c / 10, col = c - 10 * row;
            dma16(rsD, g < 384 ? (unsigned)(((32 * gks + row) * DM + 80 * gdt + 8 * col) * 2) : OOB, 0, PR_D + 5120 * j + 1024 * i);
        };
        if (w == 0) {
#pragma unroll
            for (int i = 0; i < 8; ++i) kdma(i);
        } else if (w == 1) {
#pragma unroll
            for (int i = 8; i < 11; ++i) kdma(i);
#pragma unroll
            for (int i = 0; i < 5; ++i) vdma(i);
        } else if (w == 2) {
#pragma unroll
            for (int i = 5; i < 10; ++i) vdma(i);
#pragma unroll
            for (int i = 0; i < 5; ++i) ddma(0, i);
        } else {
            dma16(rsS, lane < 40 ? (unsigned)(80 * pd8 * 8 + 16 * lane) : OOB, 0, PR_S);
#pragma unroll
            for (int i = 0; i < 5; ++i) ddma(1, i);
        }
    }
    STAMP(p, 14);

    const int mt1 = 4 * mq + (bi & 3), ft = 8 * half + (bi >> 2);
    const int wm = w & 1, wf = w >> 1;
    const int rot1 = ((ft & 7) + ((5 * (mt1 & 3)) >> 1)) % 9;
    rsrc_t rsWg = mkrs(p.wg, DM * FF * 2), rsWu = mkrs(p.wu, DM * FF * 2), rsX = rsQ;
    PIN_S(rsW); PIN_S(rsWg); PIN_S(rsWu); PIN_S(rsX);
    unsigned xoff[4], goff[3], uoff[3];
#pragma unroll
    for (int n = 0; n < 4; ++n) {
        const int row = 32 * w + 8 * n + (lane >> 3);
        xoff[n] = (unsigned)((128 * mt1 + row) * 1280 + (((lane & 7) ^ ((4 * n + (lane >> 4)) & 7)) << 4));
    }
#pragma unroll
    for (int n = 0; n < 3; ++n) {
        const int Lc = 64 * (3 * w + n) + lane, k = Lc / 12, c = Lc - 12 * k;
        goff[n] = (unsigned)(k * 3072 + 192 * ft + 16 * c);
        uoff[n] = (unsigned)(k * 3072 + 192 * ft + 16 * (c ^ 2));
    }
    auto g1_issue = [&](int kt, int stg) {
#pragma unroll
        for (int n = 0; n < 4; ++n) dma16_sc1(rsX, xoff[n], kt * 128, stg + (4 * w + n) * 1024);
#pragma unroll
        for (int n = 0; n < 3; ++n) {
            dma16(rsWg, goff[n], kt * 64 * 3072, stg + G1_G + (3 * w + n) * 1024);
            dma16(rsWu, uoff[n], kt * 64 * 3072, stg + G1_U + (3 * w + n) * 1024);
        }
    };
    g1_issue(0, 0);
    const unsigned epoch = *(const __attribute__((address_space(4))) unsigned*)(uintptr_t)(p.ws + OFF_EPOCH) + 1u;
    f32x16 acc[3][2];
#pragma unroll
    for (int a = 0; a < 3; ++a)
#pragma unroll
        for (int b = 0; b < 2; ++b)
#pragma unroll
            for (int r = 0; r < 16; ++r) acc[a][b][r] = 0.f;
    STAMP(p, 1);
    WAIT_V(10);
    BAR();
    STAMP(p, 15);
    int kap = 1 + rot1;
    g1_issue(kap, G1_STAGE);
    kap = kap == 9 ? 1 : kap + 1;
    STAMP(p, 12);

    if (bid < 24 && tid == 0) set_flag(p, bid < 16 ? OFF_CB + 256 * bid : OFF_CC + 256 * (bid - 16), 0, 0u);
    {
        const int tg = tid / 10, cc = tid - 10 * tg;
        v2u ov[8];
        if (tid < 160) {
            v4u kr[7], vr[4], tw[4];
#pragma unroll
            for (int i = 0; i < 7; ++i) kr[i] = lds_ld<v4u>((unsigned)(PR_K + (4 * tg + i) * 160 + 16 * cc));
#pragma unroll
            for (int j = 0; j < 4; ++j) vr[j] = lds_ld<v4u>((unsigned)(PR_V + (4 * tg + j) * 160 + 16 * cc));
#pragma unroll
            for (int i = 0; i < 4; ++i) tw[i] = lds_ld<v4u>((unsigned)(PR_S + 64 * cc + 16 * i));
#pragma unroll
            for (int e2 = 0; e2 < 4; ++e2) {
                const unsigned a01 = tw[e2][0], a23 = tw[e2][1], b01 = tw[e2][2], b23 = tw[e2][3];
                const f32x2 w0 = {blo(a01), blo(b01)}, w1 = {bhi(a01), bhi(b01)}, w2 = {blo(a23), blo(b23)}, w3 = {bhi(a23), bhi(b23)};
                f32x2 kf[7], r[4];
#pragma unroll
                for (int i = 0; i < 7; ++i) kf[i] = f32x2{blo(kr[i][e2]), bhi(kr[i][e2])};
#pragma unroll
                for (int j = 0; j < 4; ++j) {
                    f32x2 ks = kf[j] * w3;
                    ks = __builtin_elementwise_fma(kf[j + 1], w2, ks);
                    ks = __builtin_elementwise_fma(kf[j + 2], w1, ks);
                    ks = __builtin_elementwise_fma(kf[j + 3], w0, ks);
                    r[j] = ks * f32x2{blo(vr[j][e2]), bhi(vr[j][e2])};
                }
                ov[2 * e2] = v2u{pk2(r[3].x, r[2].x), pk2(r[1].x, r[0].x)};
                ov[2 * e2 + 1] = v2u{pk2(r[3].y, r[2].y), pk2(r[1].y, r[0].y)};
            }
        }
        if (w >= 2) {
            const int g = 2 * bid + (w - 2), gdt = g / 48, gks = g - 48 * gdt;
            const unsigned T = (unsigned)(PR_D + 5120 * (w - 2));
            const int gq = lane >> 4, q = (lane >> 2) & 3, pp = lane & 3;
            const int row = 16 * (gq >> 1) + 4 * (gq & 1) + q;
#pragma unroll
            for (int at = 0; at < 5; ++at) {
                const v2u lo = tr_read(T + row * 160 + (16 * at + 4 * pp) * 2);
                const v2u hi = tr_read(T + (row + 8) * 160 + (16 * at + 4 * pp) * 2);
                bst16<SC1>(rsW, g < 384 ? (unsigned)(OFF_WDT + ((gdt * 48 + gks) * 5 + at) * 1024 + lane * 16) : OOB, 0, v4u{lo.x, lo.y, hi.x, hi.y});
            }
        }
        STAMP(p, 13);
        WAIT_L(0);
        BAR();
        if (tid < 160) {
            const unsigned wo = (unsigned)(PR_V + cc * 1024 + (((7 - (tg >> 1)) ^ (cc & 7)) << 4) + ((tg & 1) ^ 1) * 8);
#pragma unroll
            for (int e = 0; e < 8; ++e) lds_st<v2u>(wo + e * 128, ov[e]);
        }
        WAIT_L(0);
        BAR();
        STAMP(p, 17);
#pragma unroll
        for (int i = 0; i < 3; ++i) {
            const int idx = tid + 256 * i, c = idx & 7, chl = min(idx >> 3, 79);
            const v4u val = lds_ld<v4u>((unsigned)(PR_V + chl * 128 + ((c ^ ((chl >> 3) & 7)) << 4)));
            bst16<SC1>(rsW, idx < 640 ? (unsigned)(OFF_KVR + ((80 * pd8 + chl) * SEQ + (SEQ - 64 - 64 * ptt) + 8 * c) * 2) : OOB, 0, val);
        }
    }
    STAMP(p, 16);

    const int nch = bid < 128 ? 3 : 2, ch = (bid < 128 ? 3 * bid : 384 + 2 * (bid - 128)) + w;
    const bool has_ch = w < nch;
    const int chs = has_ch ? ch : 0;
    const int l31 = lane & 31, hh = lane >> 5;
    const int mt2 = 8 * mq + (bi & 7), dt = 4 * half + (bi >> 3);
    unsigned favo;
    {
        const int c0 = ch - w, g0 = c0 / 80;
        int who = 24 * dt + min(max(lane - 32, 0), 23);
        if (lane < 32 && has_ch) who = 32 * (ch / 80) + lane;
        if (lane == 56) who = mt1;
        if (lane == 57) who = 16 + g0;
        if (lane == 58) who = 16 + min(g0 + 1, 7);
        favo = (unsigned)(OFF_FA + 128 * who);
    }
    {
        const int brow = 64 * wm + l31;
        unsigned ba[4];
#pragma unroll
        for (int kk = 0; kk < 4; ++kk) ba[kk] = (unsigned)(brow * 128 + (((2 * kk + hh) ^ ((brow >> 1) & 7)) << 4));
        const int G = lane >> 4, q = (lane >> 2) & 3, pp = lane & 3;
        unsigned aa[3];
#pragma unroll
        for (int fb = 0; fb < 3; ++fb) {
            const int fbg = 3 * wf + fb;
            aa[fb] = (unsigned)(((G & 1) ? G1_U + (fbg ^ 1) * 32 : G1_G + fbg * 32) + (8 * (G >> 1) + q) * 192 + 8 * pp);
        }
        v4u fb0[2], fb1[2];
        v2u fa0[3][2], fa1[3][2];
#define RDB(d, kk, mt) d = lds_ld<v4u>(ba[kk] + (mt) * 4096)
#define RDA(d, kk, fb, hi) d[fb][hi] = tr_read(aa[fb] + (kk) * 16 * 192 + (hi) * 4 * 192)
#define MM(A, B, fb, mt) acc[fb][mt] = mfma32(v4u{A[fb][0].x, A[fb][0].y, A[fb][1].x, A[fb][1].y}, B[mt], acc[fb][mt])
        if (w < 2) WAIT_V(13); else WAIT_V(18);
        WAIT_L(0);
        BAR();
        STAMP(p, 2);
        RDB(fb0[0], 0, 0); RDA(fa0, 0, 0, 0); RDA(fa0, 0, 0, 1); RDB(fb0[1], 0, 1); RDA(fa0, 0, 1, 0); RDA(fa0, 0, 1, 1); RDA(fa0, 0, 2, 0); RDA(fa0, 0, 2, 1);
        int st = 0;
        unsigned mx = 2 * G1_STAGE + 4096u * w, mw = 2 * G1_STAGE + 3072u * w;
        unsigned sx = kap * 128, sw = kap * 64 * 3072;
#define STEP(FA, FB, GA, GB, kn, D0, D1, D2)                                                     \
            MM(FA, FB, 0, 0); SB(); RDB(GB[0], kn, 0); RDA(GA, kn, 0, 0); SB();                 \
            MM(FA, FB, 0, 1); SB(); RDA(GA, kn, 0, 1); RDB(GB[1], kn, 1); SB();                 \
            MM(FA, FB, 1, 0); SB(); RDA(GA, kn, 1, 0); RDA(GA, kn, 1, 1); D0##_M; SB();         \
            MM(FA, FB, 1, 1); SB(); D0##_L; RDA(GA, kn, 2, 0); D1##_M; SB();                    \
            MM(FA, FB, 2, 0); SB(); D1##_L; RDA(GA, kn, 2, 1); D2##_M; SB();                    \
            MM(FA, FB, 2, 1); SB(); D2##_L; SB();
#define X0_M SET_M0(mx, 0)
#define X0_L dma16_m0_sc1(rsX, xoff[0], sx)
#define X1_M SET_M0(mx, 1024)
#define X1_L dma16_m0_sc1(rsX, xoff[1], sx)
#define X2_M SET_M0(mx, 2048)
#define X2_L dma16_m0_sc1(rsX, xoff[2], sx)
#define X3_M SET_M0(mx, 3072)
#define X3_L dma16_m0_sc1(rsX, xoff[3], sx)
#define G0_M SET_M0(mw, G1_G)
#define G0_L dma16_m0(rsWg, goff[0], sw)
#define G1_M SET_M0(mw, G1_G + 1024)
#define G1_L dma16_m0(rsWg, goff[1], sw)
#define G2_M SET_M0(mw, G1_G + 2048)
#define G2_L dma16_m0(rsWg, goff[2], sw)
#define U0_M SET_M0(mw, G1_U)
#define U0_L dma16_m0(rsWu, uoff[0], sw)
#define U1_M SET_M0(mw, G1_U + 1024)
#define U1_L dma16_m0(rsWu, uoff[1], sw)
#define U2_M SET_M0(mw, G1_U + 2048)
#define U2_L dma16_m0(rsWu, uoff[2], sw)
#define KBODY(WAITV) { \
            const int dlt = (st == 3 * G1_STAGE) ? -3 * G1_STAGE : G1_STAGE; \
            SB(); \
            STEP(fa0, fb0, fa1, fb1, 1, X0, X1, X2) \
            STEP(fa1, fb1, fa0, fb0, 2, X3, G0, U0) \
            STEP(fa0, fb0, fa1, fb1, 3, G1, U1, G2) \
            U2_M; SB(); \
            MM(fa1, fb1, 0, 0); SB(); U2_L; ba[0] += dlt; aa[0] += dlt; SB(); \
            MM(fa1, fb1, 0, 1); SB(); aa[1] += dlt; aa[2] += dlt; \
            kap = kap == 9 ? 1 : kap + 1; \
            { const unsigned nx = (unsigned)(st >= G1_STAGE ? st - G1_STAGE : st + 3 * G1_STAGE); mx = nx + 4096u * w; mw = nx + 3072u * w; } sx = kap * 128; sw = kap * 64 * 3072; \
            SB(); \
            MM(fa1, fb1, 1, 0); SB(); \
            WAIT_L(0); \
            WAITV; \
            BAR(); \
            RDB(fb0[0], 0, 0); RDA(fa0, 0, 0, 0); RDA(fa0, 0, 0, 1); SB(); \
            MM(fa1, fb1, 1, 1); SB(); RDB(fb0[1], 0, 1); RDA(fa0, 0, 1, 0); RDA(fa0, 0, 1, 1); SB(); \
            MM(fa1, fb1, 2, 0); SB(); RDA(fa0, 0, 2, 0); RDA(fa0, 0, 2, 1); ba[1] += dlt; SB(); \
            MM(fa1, fb1, 2, 1); SB(); ba[2] += dlt; ba[3] += dlt; \
            st += dlt; \
            }
#pragma unroll 1
        for (int kt = 0; kt < 9; ++kt) {
            KBODY(if (__builtin_expect(kt == 0, 0)) { if (w < 2) WAIT_V(13); else WAIT_V(18); } else WAIT_V(10))
            if (__builtin_expect(kt == 1, 0)) __builtin_amdgcn_raw_buffer_store_b32(epoch, rsW, tid == 0 ? (unsigned)(OFF_FA + 128 * bid) : OOB, 0, SC1);
            if (__builtin_expect(kt == 7, 0)) {
                mw = DUMMY_W; sw = 0x40000000u;
                sx = has_ch ? 0u : 0x40000000u;
                mx = CV_RAW + 4096u * w;
                rsX = mkrs(p.hk, DM * SEQ * 2);
#pragma unroll
                for (int n = 0; n < 4; ++n) xoff[n] = (unsigned)(chs * SEQ * 2 + n * 1024 + lane * 16);
                dma4_sc1(rsW, favo, FLG_SCR + 256u * w);
            }
        }
        {
            const unsigned flv = lds_ld<unsigned>((unsigned)(FLG_SCR + 256 * w + lane * 4));
            if (__builtin_amdgcn_ballot_w64(flv != epoch) != 0ull) wait_words(rsW, favo, epoch);
            if (has_ch) {
#pragma unroll
                for (int n = 0; n < 4; ++n) dma16_sc1(rsW, (unsigned)(lane * 16), (unsigned)(OFF_KVR + ch * SEQ * 2 + n * 1024), (unsigned)(CV_XR + CV_XRS * w + n * 1024));
            }
#define N_M do {} while (0)
#define N_L do {} while (0)
            SB();
            STEP(fa0, fb0, fa1, fb1, 1, N, N, N)
            STEP(fa1, fb1, fa0, fb0, 2, N, N, N)
            STEP(fa0, fb0, fa1, fb1, 3, N, N, N)
            MM(fa1, fb1, 0, 0); MM(fa1, fb1, 0, 1); MM(fa1, fb1, 1, 0); MM(fa1, fb1, 1, 1); MM(fa1, fb1, 2, 0); MM(fa1, fb1, 2, 1);
        }
#undef MM
#undef STEP
#undef KBODY
    }
    STAMP(p, 3);
    if (tid == 0) { lds_st<unsigned>(LC_B, 0u); lds_st<unsigned>(LC_C, 0u); }
    WAIT_L(0);
    BAR();
    const int rot2 = (3 * (bi & 7) / 2 + 3 * (bi >> 3)) % 12;
    unsigned a2o[12];
#pragma unroll
    for (int s = 0; s < 12; ++s) { const int ks = s + rot2; a2o[s] = (unsigned)(12 * w + (ks >= 12 ? ks - 12 : ks)) * 1024u; }
    const unsigned a2base = (unsigned)(OFF_WDT + dt * 48 * 5 * 1024);
    v4u A2[12][5];
#pragma unroll
    for (int slot = 0; slot < 8; ++slot) A2[slot / 5][slot % 5] = bld16<SC1>(rsW, lane * 16, a2base + a2o[slot / 5] * 5 + (slot % 5) * 1024);
    SB();
#pragma unroll
    for (int fb = 0; fb < 3; ++fb)
#pragma unroll
        for (int mt = 0; mt < 2; ++mt) {
            const f32x16& a = acc[fb][mt];
            v4u o;
#pragma unroll
            for (int j = 0; j < 4; ++j) o[j] = pk2(a[2 * j] * a[8 + 2 * j], a[2 * j + 1] * a[8 + 2 * j + 1]);
            const int Ft = 6 * ft + 3 * wf + fb;
            const int mrow = 128 * mt1 + 64 * wm + 32 * mt + l31;
            const int pos = (lane & 15) + 16 * (2 * (Ft & 1) + hh);
            bst16<SC1>(rsW, (unsigned)(OFF_HF + ((mrow >> 4) * 48 + (Ft >> 1)) * 1024 + pos * 16), 0, o);
        }
    STAMP(p, 4);

    if (has_ch) {
        const unsigned HP = (unsigned)(CV_HP + w * CV_HPS), XR = (unsigned)(CV_XR + w * CV_XRS), RAW = (unsigned)(CV_RAW + w * 4096);
        const unsigned sdv = p.sd[ch];
        WAIT_V(14);
#pragma unroll
        for (int ps = 0; ps < 5; ++ps) {
            const int n = 64 * ps + lane;
            v4u c0 = {0, 0, 0, 0}, c1 = {0, 0, 0, 0};
            if (ps < 4) {
                c0 = lds_ld<v4u>(RAW + 16 * (ps == 0 ? max(n - 2, 0) : n - 2));
                c1 = lds_ld<v4u>(RAW + 16 * (ps == 0 ? max(n - 1, 0) : n - 1));
                if (ps == 0) { if (n < 2) c0 = v4u{0, 0, 0, 0}; if (n < 1) c1 = v4u{0, 0, 0, 0}; }
            } else {
                c0 = lds_ld<v4u>(RAW + 16 * (254 + (lane & 1)));
                c1 = lds_ld<v4u>(RAW + 16 * 255);
                if (lane & 1) c1 = v4u{0, 0, 0, 0};
            }
            const unsigned Dw[6] = {c0.x, c0.y, c0.z, c0.w, c1.x, c1.y};
            v4u o0, o1, o2, o3;
#pragma unroll
            for (int q = 0; q < 4; ++q) {
                o0[q] = __builtin_amdgcn_alignbit(Dw[q + 1], Dw[q], 16);
                o1[q] = Dw[q + 1];
                o2[q] = __builtin_amdgcn_alignbit(Dw[q + 2], Dw[q + 1], 16);
                o3[q] = Dw[q + 2];
            }
            if (ps < 4 || lane < 2) {
                lds_st<v4u>(HP + 0 * CV_CS + 16 * n, o0);
                lds_st<v4u>(HP + 1 * CV_CS + 16 * n, o1);
                lds_st<v4u>(HP + 2 * CV_CS + 16 * n, o2);
                lds_st<v4u>(HP + 3 * CV_CS + 16 * n, o3);
            }
        }
        if (lane < 32) lds_st<v4u>(XR + 4096 + 16 * lane, v4u{0, 0, 0, 0});
        asm volatile("" ::: "memory");
        STAMP(p, 5);

        const int r = lane & 15, g = lane >> 4;
        unsigned ap0 = HP + (unsigned)((r & 3) * CV_CS + 2 * ((r & ~3) + 8 * g));
        unsigned ap1 = ap0 + 8;
        PIN_V(ap0); PIN_V(ap1);
        const unsigned bp = XR + (unsigned)(2 * (240 - 16 * r + 8 * g));
        f32x4 cacc[8];
#pragma unroll
        for (int i = 0; i < 8; ++i) cacc[i] = f32x4{0.f, 0.f, 0.f, 0.f};
        v4u CA[2][8], CB[2][8];
        auto cvrd = [&](int set, int s, int i) {
            CB[set][i] = lds_ld<v4u>(bp + 512 * (7 - i) + 64 * s);
            const v2u lo = lds_ld<v2u>(ap0 + 512 * i + 64 * s), hi = lds_ld<v2u>(ap1 + 512 * i + 64 * s);
            CA[set][i] = v4u{lo.x, lo.y, hi.x, hi.y};
        };
#pragma unroll
        for (int i = 0; i < 8; ++i) cvrd(0, 0, i);
#pragma unroll
        for (int s = 0; s < 8; ++s) {
            const int cur = s & 1;
            SB();
#pragma unroll
            for (int gm = 0; gm < 8; ++gm) {
#pragma unroll
                for (int be = 0; be <= gm; ++be) cacc[gm] = mfma16(CA[cur][be], CB[cur][gm - be], cacc[gm]);
                SB();
                if (s < 7) cvrd(cur ^ 1, s + 1, gm);
                { const int slot = 8 + 8 * s + gm; if (slot < 60) A2[slot / 5][slot % 5] = bld16<SC1>(rsW, lane * 16, a2base + a2o[slot / 5] * 5 + (slot % 5) * 1024); }
                SB();
            }
            if (s == 1) {
                WAIT_V(16);
                lds_arrive(LC_B);
            }
        }
        STAMP(p, 6);
        const float Dc = blo(sdv);
#pragma unroll
        for (int gm = 0; gm < 8; ++gm) {
            const int t0 = 256 * gm + 16 * r + 4 * g;
            const v2u kvp = lds_ld<v2u>(XR + 2 * (2044 - t0));
            const float y0 = cacc[gm][0] + bhi(kvp.y) * Dc, y1 = cacc[gm][1] + blo(kvp.y) * Dc, y2 = cacc[gm][2] + bhi(kvp.x) * Dc, y3 = cacc[gm][3] + blo(kvp.x) * Dc;
            lds_st<v2u>(HP + 2 * t0, v2u{pk2(y0, y1), pk2(y2, y3)});
        }
        asm volatile("" ::: "memory");
#pragma unroll
        for (int ps = 0; ps < 4; ++ps) {
            const int n = 64 * ps + lane;
            bst16<SC1>(rsW, (unsigned)(OFF_YT + (ch * SEQ + 8 * n) * 2), 0, lds_ld<v4u>(HP + 16 * n));
        }
    } else {
        if (w == 3) {
            const rsrc_t rsG = mkrs(p.gw, DM * 2);
            const int c16 = lane & 15, row0 = 128 * mt1 + 8 * ft + (lane >> 4);
            v4u gwv[5], xa[5], xb[5];
#pragma unroll
            for (int i = 0; i < 5; ++i) {
                gwv[i] = bld16<0>(rsG, (unsigned)(16 * (c16 + 16 * i)), 0);
                xa[i] = bld16<0>(rsQ, (unsigned)(row0 * DM * 2 + 16 * (c16 + 16 * i)), 0);
                xb[i] = bld16<0>(rsQ, (unsigned)((row0 + 4) * DM * 2 + 16 * (c16 + 16 * i)), 0);
            }
            float s0 = 0.f, s1 = 0.f;
#pragma unroll
            for (int i = 0; i < 5; ++i)
#pragma unroll
                for (int e = 0; e < 4; ++e) { s0 = dot2(xa[i][e], gwv[i][e], s0); s1 = dot2(xb[i][e], gwv[i][e], s1); }
#pragma unroll
            for (int m = 1; m < 16; m <<= 1) { s0 += __shfl_xor(s0, m); s1 += __shfl_xor(s1, m); }
            const float g0 = rb(1.0f / (1.0f + __expf(-rb(s0)))), g1 = rb(1.0f / (1.0f + __expf(-rb(s1))));
            __builtin_amdgcn_raw_buffer_store_b32(__builtin_bit_cast(unsigned, g0), rsW, c16 == 0 ? (unsigned)(OFF_GATE + row0 * 4) : OOB, 0, SC1);
            __builtin_amdgcn_raw_buffer_store_b32(__builtin_bit_cast(unsigned, g1), rsW, c16 == 0 ? (unsigned)(OFF_GATE + (row0 + 4) * 4) : OOB, 0, SC1);
        }
        WAIT_V(0);
        lds_arrive(LC_B);
        lds_wait(LC_B, 4);
        if (w == 3) {
            if (lane == 0) {
                const unsigned old = add_count(p, OFF_CB + 256 * mt1, 1u);
                if (old == 15u) set_flag(p, OFF_RB + 256 * mt1, 0, epoch);
            }
            if (bid == 0) {
#pragma unroll 1
                for (int i = 0; i < 4; ++i) wait_words(rsW, (unsigned)(OFF_FA + 128 * (64 * i + lane)), epoch);
                if (lane == 0) set_flag(p, OFF_EPOCH, 0, epoch);
            }
        }
#pragma unroll
        for (int slot = 8; slot < 60; ++slot) A2[slot / 5][slot % 5] = bld16<SC1>(rsW, lane * 16, a2base + a2o[slot / 5] * 5 + (slot % 5) * 1024);
    }
    STAMP(p, 7);

    wait_ready<4>(p, OFF_RB + 256 * (mt2 >> 1), epoch);
    STAMP(p, 8);
    constexpr int W2 = 8;
    unsigned b2off[4];
#pragma unroll
    for (int n = 0; n < 4; ++n) b2off[n] = (unsigned)(OFF_HF + (4 * mt2 + ((w + n) & 3)) * 48 * 1024);
    v4u B2[W2][4];
#pragma unroll
    for (int s = 0; s < W2; ++s)
#pragma unroll
        for (int n = 0; n < 4; ++n) B2[s][n] = bld16<SC1>(rsW, lane * 16, b2off[n] + a2o[s]);
    SB();
    if (has_ch) {
        WAIT_V(32);
        lds_arrive(LC_C);
    }
    f32x4 acc2[5][4];
#pragma unroll
    for (int a = 0; a < 5; ++a)
#pragma unroll
        for (int n = 0; n < 4; ++n) acc2[a][n] = f32x4{0.f, 0.f, 0.f, 0.f};
    const int t = 64 * mt2 + 16 * w + (lane & 15), g4 = lane >> 4;
    v2u qv[5];
    float gt;
#pragma unroll
    for (int s = 0; s < 12; ++s) {
        v4u cb[4];
#pragma unroll
        for (int n = 0; n < 4; ++n) cb[n] = B2[s % W2][n];
        if (s + W2 < 12) {
#pragma unroll
            for (int n = 0; n < 4; ++n) B2[s % W2][n] = bld16<SC1>(rsW, lane * 16, b2off[n] + a2o[s + W2]);
        }
        if (s == 6) {
#pragma unroll
            for (int a = 0; a < 5; ++a) qv[a] = *(const GAS v2u*)(p.q + (size_t)t * DM + 80 * dt + 16 * a + 4 * g4);
            gt = __builtin_bit_cast(float, __builtin_amdgcn_raw_buffer_load_b32(rsW, (unsigned)(OFF_GATE + t * 4), 0, SC1));
        }
#pragma unroll
        for (int n = 0; n < 4; ++n)
#pragma unroll
            for (int a = 0; a < 5; ++a) acc2[a][n] = mfma16(A2[s][a], cb[n], acc2[a][n]);
        SB();
    }
    STAMP(p, 9);
    lds_wait(LC_B, 4);
#pragma unroll
    for (int a = 0; a < 5; ++a)
#pragma unroll
        for (int j = 1; j < 4; ++j) lds_st<f32x4>((unsigned)(G2_RED + ((((w + j) & 3) * 5 + a) * 3 + (j - 1)) * 1024 + lane * 16), acc2[a][j]);
    if (w == 3) {
        lds_wait(LC_C, (unsigned)nch);
        {
            const int c0 = ch - 3, g0 = c0 / 80, n0 = min(nch, 80 * (g0 + 1) - c0), gi = g0 + lane, ni = lane == 0 ? n0 : nch - n0;
            if (lane < 2 && ni > 0) {
                const unsigned old = add_count(p, OFF_CC + 256 * gi, (unsigned)ni);
                if (old + (unsigned)ni == 80u) set_flag(p, OFF_RC + 256 * gi, 0, epoch);
            }
        }
        wait_ready<4>(p, OFF_RC + 256 * dt, epoch);
        v4u yv[10];
#pragma unroll
        for (int it = 0; it < 10; ++it) {
            const int c = lane + 64 * it;
            yv[it] = bld16<SC1>(rsW, (unsigned)(OFF_YT + ((80 * dt + (c >> 3)) * SEQ + 64 * mt2 + 8 * (c & 7)) * 2), 0);
        }
#pragma unroll
        for (int it = 0; it < 10; ++it) lds_st<v4u>((unsigned)(G2_Y + (lane + 64 * it) * 16), yv[it]);
    }
    WAIT_L(0);
    BAR();
    STAMP(p, 10);
    const float omg = rb(1.0f - gt);
    const int q = (lane >> 2) & 3, pp = lane & 3;
#pragma unroll
    for (int a = 0; a < 5; ++a) {
        f32x4 b = acc2[a][0];
#pragma unroll
        for (int j = 0; j < 3; ++j) b += lds_ld<f32x4>((unsigned)(G2_RED + ((w * 5 + a) * 3 + j) * 1024 + lane * 16));
        const v2u y = tr_read((unsigned)(G2_Y + (16 * a + 4 * g4 + q) * 128 + 32 * w + 8 * pp));
        const int d0 = 80 * dt + 16 * a + 4 * g4;
        const float o0 = gt * (blo(qv[a].x) * blo(y.x)) + omg * b[0], o1 = gt * (bhi(qv[a].x) * bhi(y.x)) + omg * b[1];
        const float o2 = gt * (blo(qv[a].y) * blo(y.y)) + omg * b[2], o3 = gt * (bhi(qv[a].y) * bhi(y.y)) + omg * b[3];
        *(GAS v2u*)(p.out + (size_t)t * DM + d0) = v2u{pk2(o0, o1), pk2(o2, o3)};
    }
    STAMP(p, 11);
}

extern "C" void kernel_launch(void* const* d_in, const int* in_sizes, int n_in, void* d_out, int out_size, void* d_ws, size_t ws_size, hipStream_t stream) {
    Params p;
    p.q = (const u16*)d_in[0]; p.k = (const u16*)d_in[1]; p.v = (const u16*)d_in[2]; p.sk = (const u16*)d_in[3];
    p.hk = (const u16*)d_in[4]; p.sd = (const u16*)d_in[5];
    p.wg = (const u16*)d_in[7]; p.wu = (const u16*)d_in[8]; p.wd = (const u16*)d_in[9]; p.gw = (const u16*)d_in[10];
    p.out = (u16*)d_out; p.ws = (unsigned char*)d_ws;
    (void)hipFuncSetAttribute((const void*)k_fused, hipFuncAttributeMaxDynamicSharedMemorySize, LDS_BYTES);
    k_fused<<<256, 256, LDS_BYTES, stream>>>(p);
}
```

```cpp
#include <hip/hip_runtime.h>
#include <stdint.h>
#include <stdio.h>
#define STAMPS

#define LAS __attribute__((address_space(3)))
#define GAS __attribute__((address_space(1)))
typedef unsigned short u16;
typedef unsigned v4u __attribute__((ext_vector_type(4)));
typedef unsigned v2u __attribute__((ext_vector_type(2)));
typedef __bf16 bf8 __attribute__((ext_vector_type(8)));
typedef __bf16 bf2 __attribute__((ext_vector_type(2)));
typedef short s4 __attribute__((ext_vector_type(4)));
typedef float f32x2 __attribute__((ext_vector_type(2)));
typedef float f32x4 __attribute__((ext_vector_type(4)));
typedef float f32x16 __attribute__((ext_vector_type(16)));
typedef __amdgpu_buffer_rsrc_t rsrc_t;

constexpr int SEQ = 2048, DM = 640, FF = 1536;
constexpr int OFF_EPOCH = 0;
constexpr int OFF_FA = 32768;
constexpr int OFF_CB = 2048;
constexpr int OFF_CC = 8192;
constexpr int OFF_RB = 12288;
constexpr int OFF_RC = 16384;
constexpr int OFF_STAMP = 32 * 1024 * 1024;
constexpr int OFF_KVR = 65536;
constexpr int OFF_WDT = OFF_KVR + DM * SEQ * 2;
constexpr int OFF_HF = OFF_WDT + FF * DM * 2;
constexpr int OFF_YT = OFF_HF + SEQ * FF * 2;
constexpr int OFF_GATE = OFF_YT + DM * SEQ * 2;
constexpr int WS_BYTES = OFF_GATE + SEQ * 4;
constexpr int SC1 = 16;
constexpr unsigned OOB = 0x80000000u;
constexpr unsigned SPIN_MAX = 1u << 16;

struct Params {
    const u16 *q, *k, *v, *sk, *hk, *sd, *wg, *wu, *wd, *gw;
    u16* out;
    unsigned char* ws;
};

__device__ __forceinline__ unsigned pk2(float a, float b) {
    f32x2 x = {a, b};
    return __builtin_bit_cast(unsigned, __builtin_convertvector(x, bf2));
}
__device__ __forceinline__ float blo(unsigned u) { return __builtin_bit_cast(float, u << 16); }
__device__ __forceinline__ float bhi(unsigned u) { return __builtin_bit_cast(float, u & 0xffff0000u); }
__device__ __forceinline__ float rb(float x) { return blo(pk2(x, x)); }
__device__ __forceinline__ f32x4 mfma16(v4u a, v4u b, f32x4 c) {
    return __builtin_amdgcn_mfma_f32_16x16x32_bf16(__builtin_bit_cast(bf8, a), __builtin_bit_cast(bf8, b), c, 0, 0, 0);
}
__device__ __forceinline__ f32x16 mfma32(v4u a, v4u b, f32x16 c) {
    return __builtin_amdgcn_mfma_f32_32x32x16_bf16(__builtin_bit_cast(bf8, a), __builtin_bit_cast(bf8, b), c, 0, 0, 0);
}
__device__ __forceinline__ float dot2(unsigned a, unsigned b, float c) {
    return __builtin_amdgcn_fdot2_f32_bf16(__builtin_bit_cast(bf2, a), __builtin_bit_cast(bf2, b), c, false);
}
template <class T> __device__ __forceinline__ T lds_ld(unsigned a) { return *(LAS T*)(uintptr_t)a; }
template <class T> __device__ __forceinline__ void lds_st(unsigned a, T v) { *(LAS T*)(uintptr_t)a = v; }
__device__ __forceinline__ v2u tr_read(unsigned a) {
    return __builtin_bit_cast(v2u, __builtin_amdgcn_ds_read_tr16_b64_v4i16((LAS s4*)(uintptr_t)a));
}
__device__ __forceinline__ rsrc_t mkrs(const void* p, unsigned bytes) { return __builtin_amdgcn_make_buffer_rsrc((void*)p, 0, bytes, 0x00020000); }
template <int AUX> __device__ __forceinline__ v4u bld16(rsrc_t rs, unsigned voff, unsigned soff) { return __builtin_amdgcn_raw_buffer_load_b128(rs, voff, soff, AUX); }
template <int AUX> __device__ __forceinline__ void bst16(rsrc_t rs, unsigned voff, unsigned soff, v4u v) { __builtin_amdgcn_raw_buffer_store_b128(v, rs, voff, soff, AUX); }
#define WAIT_V(n) asm volatile("s_waitcnt vmcnt(" #n ")" ::: "memory")
#define WAIT_L(n) asm volatile("s_waitcnt lgkmcnt(" #n ")" ::: "memory")
#define BAR() do { asm volatile("" ::: "memory"); __builtin_amdgcn_s_barrier(); asm volatile("" ::: "memory"); } while (0)
#define SB() __builtin_amdgcn_sched_barrier(0)
#define PIN_S(x) asm volatile("" : "+s"(x))
#define PIN_V(x) asm volatile("" : "+v"(x))
#define RLX_AGENT __ATOMIC_RELAXED, __HIP_MEMORY_SCOPE_AGENT
#define STAMP_WAVE 0
#define STAMP(p, i) do { if ((threadIdx.x & 63) == 0 && (threadIdx.x >> 6) == STAMP_WAVE) { ((GAS unsigned long long*)((p).ws + OFF_STAMP))[blockIdx.x * 32 + (i)] = __builtin_amdgcn_s_memrealtime(); } } while (0)

__device__ __forceinline__ void dma16(rsrc_t rs, unsigned voff, unsigned soff, unsigned lds_addr) {
    asm volatile("s_mov_b32 m0, %0\n\ts_nop 0\n\tbuffer_load_dwordx4 %1, %2, %3 offen lds" ::"s"(lds_addr), "v"(voff), "s"(rs), "s"(soff) : "memory", "m0");
}
__device__ __forceinline__ void dma16_sc1(rsrc_t rs, unsigned voff, unsigned soff, unsigned lds_addr) {
    asm volatile("s_mov_b32 m0, %0\n\ts_nop 0\n\tbuffer_load_dwordx4 %1, %2, %3 offen sc1 lds" ::"s"(lds_addr), "v"(voff), "s"(rs), "s"(soff) : "memory", "m0");
}
#define SET_M0(base, imm) asm volatile("s_add_u32 m0, %0, %1" ::"s"(base), "i"(imm) : "memory", "m0", "scc")
__device__ __forceinline__ void dma16_m0(rsrc_t rs, unsigned voff, unsigned soff) {
    asm volatile("buffer_load_dwordx4 %0, %1, %2 offen lds" ::"v"(voff), "s"(rs), "s"(soff) : "memory");
}
__device__ __forceinline__ void dma16_m0_sc1(rsrc_t rs, unsigned voff, unsigned soff) {
    asm volatile("buffer_load_dwordx4 %0, %1, %2 offen sc1 lds" ::"v"(voff), "s"(rs), "s"(soff) : "memory");
}
__device__ __forceinline__ void aload16(v4u& d, rsrc_t rs, unsigned voff) {
    asm volatile("buffer_load_dwordx4 %0, %1, %2, 0 offen" : "=v"(d) : "v"(voff), "s"(rs) : "memory");
}

__device__ __forceinline__ void wait_words(rsrc_t rsW, unsigned voff, unsigned e) {
    unsigned t0, left = SPIN_MAX;
    asm volatile(
        "1:\n\t"
        "buffer_load_dword %0, %2, %3, 0 offen sc1\n\t"
        "s_waitcnt vmcnt(0)\n\t"
        "v_cmp_ne_u32 vcc, %4, %0\n\t"
        "s_nop 4\n\t"
        "s_cbranch_vccz 2f\n\t"
        "s_sleep 8\n\t"
        "s_sub_u32 %1, %1, 1\n\t"
        "s_cbranch_scc0 1b\n\t"
        "2:"
        : "=&v"(t0), "+s"(left)
        : "v"(voff), "s"(rsW), "s"(e)
        : "memory", "vcc", "scc");
}
__device__ __forceinline__ void dma4_sc1(rsrc_t rs, unsigned voff, unsigned lds_addr) {
    asm volatile("s_mov_b32 m0, %0\n\ts_nop 0\n\tbuffer_load_dword %1, %2, 0 offen sc1 lds" ::"s"(lds_addr), "v"(voff), "s"(rs) : "memory", "m0");
}
__device__ __forceinline__ unsigned add_count(const Params& p, int off, unsigned n) {
    return __hip_atomic_fetch_add((GAS unsigned*)(p.ws + off), n, RLX_AGENT);
}
template <int SLEEP>
__device__ __forceinline__ void wait_ready(const Params& p, int off, unsigned e) {
    GAS unsigned* c = (GAS unsigned*)(p.ws + off);
    for (unsigned it = 0; it < SPIN_MAX; ++it) {
        if ((unsigned)__builtin_amdgcn_readfirstlane(__hip_atomic_load(c, RLX_AGENT)) == e) break;
        __builtin_amdgcn_s_sleep(SLEEP);
    }
    __builtin_amdgcn_fence(__ATOMIC_ACQUIRE, "wavefront");
}
__device__ __forceinline__ void lds_arrive(unsigned off) {
    if ((threadIdx.x & 63) == 0) (void)__hip_atomic_fetch_add((LAS unsigned*)(uintptr_t)off, 1u, __ATOMIC_RELAXED, __HIP_MEMORY_SCOPE_WORKGROUP);
}
__device__ __forceinline__ void lds_wait(unsigned off, unsigned n) {
    for (unsigned it = 0; it < (SPIN_MAX << 4); ++it) {
        if ((unsigned)__builtin_amdgcn_readfirstlane(__hip_atomic_load((LAS unsigned*)(uintptr_t)off, __ATOMIC_RELAXED, __HIP_MEMORY_SCOPE_WORKGROUP)) >= n) break;
        __builtin_amdgcn_s_sleep(1);
    }
    asm volatile("" ::: "memory");
}
__device__ __forceinline__ void set_flag(const Params& p, int off, int idx, unsigned e) {
    __hip_atomic_store((GAS unsigned*)(p.ws + off) + idx, e, RLX_AGENT);
}

constexpr int G1_STAGE = 40960, G1_G = 16384, G1_U = 16384 + 12288;
constexpr int PR_K = 3 * G1_STAGE;
constexpr int PR_V = PR_K + 11264;
constexpr int PR_S = PR_V + 10240;
constexpr int PR_D = PR_S + 1024;
constexpr int CV_XR = 0, CV_XRS = 4608;
constexpr int CV_CS = 4160, CV_HP = 3 * CV_XRS, CV_HPS = 4 * CV_CS;
constexpr int G2_Y = CV_HP + 3 * CV_HPS;
constexpr int CV_RAW = 2 * G1_STAGE;
constexpr int DUMMY_W = 2 * G1_STAGE;
constexpr int DUMMY_X = 2 * G1_STAGE + G1_G;
constexpr int FLG_SCR = 102400;
constexpr int G2_RED = 98304;
constexpr int LC_B = 163808, LC_C = 163824;
constexpr int LDS_BYTES = 163840;
static_assert(G2_Y + 80 * 128 <= CV_RAW && G2_RED + 60 * 1024 <= LC_B, "lds");

__global__ void __launch_bounds__(256, 1) k_fused(Params p) {
    extern __shared__ __attribute__((aligned(16))) unsigned char smem[];
    const int tid = threadIdx.x, lane = tid & 63, w = __builtin_amdgcn_readfirstlane(tid >> 6);
    const int bid = blockIdx.x;
    const int xcd = bid & 7, bi = bid >> 3, mq = xcd >> 1, half = xcd & 1;
    STAMP(p, 0);
    {
        unsigned long long pc;
        asm volatile("s_getpc_b64 %0" : "=s"(pc));
        const rsrc_t rsC = mkrs((const void*)(pc & ~255ull), 24576 - 256);
        dma16(rsC, (unsigned)(((bi + 32 * w) << 10) + lane * 16), 0, 160000 - 1024);
    }
    rsrc_t rsW = mkrs(p.ws, WS_BYTES);
    rsrc_t rsQ = mkrs(p.q, SEQ * DM * 2);

    const int ptt = bid & 31, pd8 = bid >> 5;
    {
        const rsrc_t rsK = mkrs(p.k, SEQ * DM * 2), rsV = mkrs(p.v, SEQ * DM * 2), rsS = mkrs(p.sk, DM * 4 * 2), rsD = mkrs(p.wd, FF * DM * 2);
        auto kdma = [&](int i) {
            const int c = 64 * i + lane, row = c / 10, col = c - 10 * row, t = 64 * ptt - 3 + row;
            dma16(rsK, (t >= 0 && c < 670) ? (unsigned)((t * DM + 80 * pd8 + 8 * col) * 2) : OOB, 0, PR_K + 1024 * i);
        };
        auto vdma = [&](int i) {
            const int c = 64 * i + lane, row = c / 10, col = c - 10 * row;
            dma16(rsV, (unsigned)(((64 * ptt + row) * DM + 80 * pd8 + 8 * col) * 2), 0, PR_V + 1024 * i);
        };
        auto ddma = [&](int j, int i) {
            const int g = 2 * bid + j, gdt = g / 48, gks = g - 48 * gdt;
            const int c = 64 * i + lane, row = c / 10, col = c - 10 * row;
            dma16(rsD, g < 384 ? (unsigned)(((32 * gks + row) * DM + 80 * gdt + 8 * col) * 2) : OOB, 0, PR_D + 5120 * j + 1024 * i);
        };
        if (w == 0) {
#pragma unroll
            for (int i = 0; i < 8; ++i) kdma(i);
        } else if (w == 1) {
#pragma unroll
            for (int i = 8; i < 11; ++i) kdma(i);
#pragma unroll
            for (int i = 0; i < 5; ++i) vdma(i);
        } else if (w == 2) {
#pragma unroll
            for (int i = 5; i < 10; ++i) vdma(i);
#pragma unroll
            for (int i = 0; i < 5; ++i) ddma(0, i);
        } else {
            dma16(rsS, lane < 40 ? (unsigned)(80 * pd8 * 8 + 16 * lane) : OOB, 0, PR_S);
#pragma unroll
            for (int i = 0; i < 5; ++i) ddma(1, i);
        }
    }
    STAMP(p, 14);

    const int mt1 = 4 * mq + (bi & 3), ft = 8 * half + (bi >> 2);
    const int wm = w & 1, wf = w >> 1;
    const int rot1 = ((ft & 7) + ((5 * (mt1 & 3)) >> 1)) % 9;
    rsrc_t rsWg = mkrs(p.wg, DM * FF * 2), rsWu = mkrs(p.wu, DM * FF * 2), rsX = rsQ;
    PIN_S(rsW); PIN_S(rsWg); PIN_S(rsWu); PIN_S(rsX);
    unsigned xoff[4], goff[3], uoff[3];
#pragma unroll
    for (int n = 0; n < 4; ++n) {
        const int row = 32 * w + 8 * n + (lane >> 3);
        xoff[n] = (unsigned)((128 * mt1 + row) * 1280 + (((lane & 7) ^ ((4 * n + (lane >> 4)) & 7)) << 4));
    }
#pragma unroll
    for (int n = 0; n < 3; ++n) {
        const int Lc = 64 * (3 * w + n) + lane, k = Lc / 12, c = Lc - 12 * k;
        goff[n] = (unsigned)(k * 3072 + 192 * ft + 16 * c);
        uoff[n] = (unsigned)(k * 3072 + 192 * ft + 16 * (c ^ 2));
    }
    auto g1_issue = [&](int kt, int stg) {
#pragma unroll
        for (int n = 0; n < 4; ++n) dma16_sc1(rsX, xoff[n], kt * 128, stg + (4 * w + n) * 1024);
#pragma unroll
        for (int n = 0; n < 3; ++n) {
            dma16(rsWg, goff[n], kt * 64 * 3072, stg + G1_G + (3 * w + n) * 1024);
            dma16(rsWu, uoff[n], kt * 64 * 3072, stg + G1_U + (3 * w + n) * 1024);
        }
    };
    g1_issue(0, 0);
    const unsigned epoch = *(const __attribute__((address_space(4))) unsigned*)(uintptr_t)(p.ws + OFF_EPOCH) + 1u;
    f32x16 acc[3][2];
#pragma unroll
    for (int a = 0; a < 3; ++a)
#pragma unroll
        for (int b = 0; b < 2; ++b)
#pragma unroll
            for (int r = 0; r < 16; ++r) acc[a][b][r] = 0.f;
    STAMP(p, 1);
    WAIT_V(10);
    BAR();
    STAMP(p, 15);
    int kap = 1 + rot1;
    g1_issue(kap, G1_STAGE);
    kap = kap == 9 ? 1 : kap + 1;
    STAMP(p, 12);

    if (bid < 24 && tid == 0) set_flag(p, bid < 16 ? OFF_CB + 256 * bid : OFF_CC + 256 * (bid - 16), 0, 0u);
    {
        const int tg = tid / 10, cc = tid - 10 * tg;
        v2u ov[8];
        if (tid < 160) {
            v4u kr[7], vr[4], tw[4];
#pragma unroll
            for (int i = 0; i < 7; ++i) kr[i] = lds_ld<v4u>((unsigned)(PR_K + (4 * tg + i) * 160 + 16 * cc));
#pragma unroll
            for (int j = 0; j < 4; ++j) vr[j] = lds_ld<v4u>((unsigned)(PR_V + (4 * tg + j) * 160 + 16 * cc));
#pragma unroll
            for (int i = 0; i < 4; ++i) tw[i] = lds_ld<v4u>((unsigned)(PR_S + 64 * cc + 16 * i));
#pragma unroll
            for (int e2 = 0; e2 < 4; ++e2) {
                const unsigned a01 = tw[e2][0], a23 = tw[e2][1], b01 = tw[e2][2], b23 = tw[e2][3];
                const f32x2 w0 = {blo(a01), blo(b01)}, w1 = {bhi(a01), bhi(b01)}, w2 = {blo(a23), blo(b23)}, w3 = {bhi(a23), bhi(b23)};
                f32x2 kf[7], r[4];
#pragma unroll
                for (int i = 0; i < 7; ++i) kf[i] = f32x2{blo(kr[i][e2]), bhi(kr[i][e2])};
#pragma unroll
                for (int j = 0; j < 4; ++j) {
                    f32x2 ks = kf[j] * w3;
                    ks = __builtin_elementwise_fma(kf[j + 1], w2, ks);
                    ks = __builtin_elementwise_fma(kf[j + 2], w1, ks);
                    ks = __builtin_elementwise_fma(kf[j + 3], w0, ks);
                    r[j] = ks * f32x2{blo(vr[j][e2]), bhi(vr[j][e2])};
                }
                ov[2 * e2] = v2u{pk2(r[3].x, r[2].x), pk2(r[1].x, r[0].x)};
                ov[2 * e2 + 1] = v2u{pk2(r[3].y, r[2].y), pk2(r[1].y, r[0].y)};
            }
        }
        if (w >= 2) {
            const int g = 2 * bid + (w - 2), gdt = g / 48, gks = g - 48 * gdt;
            const unsigned T = (unsigned)(PR_D + 5120 * (w - 2));
            const int gq = lane >> 4, q = (lane >> 2) & 3, pp = lane & 3;
            const int row = 16 * (gq >> 1) + 4 * (gq & 1) + q;
#pragma unroll
            for (int at = 0; at < 5; ++at) {
                const v2u lo = tr_read(T + row * 160 + (16 * at + 4 * pp) * 2);
                const v2u hi = tr_read(T + (row + 8) * 160 + (16 * at + 4 * pp) * 2);
                bst16<SC1>(rsW, g < 384 ? (unsigned)(OFF_WDT + ((gdt * 48 + gks) * 5 + at) * 1024 + lane * 16) : OOB, 0, v4u{lo.x, lo.y, hi.x, hi.y});
            }
        }
        STAMP(p, 13);
        WAIT_L(0);
        BAR();
        if (tid < 160) {
            const unsigned wo = (unsigned)(PR_V + cc * 1024 + (((7 - (tg >> 1)) ^ (cc & 7)) << 4) + ((tg & 1) ^ 1) * 8);
#pragma unroll
            for (int e = 0; e < 8; ++e) lds_st<v2u>(wo + e * 128, ov[e]);
        }
        WAIT_L(0);
        BAR();
        STAMP(p, 17);
#pragma unroll
        for (int i = 0; i < 3; ++i) {
            const int idx = tid + 256 * i, c = idx & 7, chl = min(idx >> 3, 79);
            const v4u val = lds_ld<v4u>((unsigned)(PR_V + chl * 128 + ((c ^ ((chl >> 3) & 7)) << 4)));
            bst16<SC1>(rsW, idx < 640 ? (unsigned)(OFF_KVR + ((80 * pd8 + chl) * SEQ + (SEQ - 64 - 64 * ptt) + 8 * c) * 2) : OOB, 0, val);
        }
    }
    STAMP(p, 16);

    const int nch = bid < 128 ? 3 : 2, ch = (bid < 128 ? 3 * bid : 384 + 2 * (bid - 128)) + w;
    const bool has_ch = w < nch;
    const int chs = has_ch ? ch : 0;
    const int l31 = lane & 31, hh = lane >> 5;
    const int mt2 = 8 * mq + (bi & 7), dt = 4 * half + (bi >> 3);
    unsigned favo;
    {
        const int c0 = ch - w, g0 = c0 / 80;
        int who = 24 * dt + min(max(lane - 32, 0), 23);
        if (lane < 32 && has_ch) who = 32 * (ch / 80) + lane;
        if (lane == 56) who = mt1;
        if (lane == 57) who = 16 + g0;
        if (lane == 58) who = 16 + min(g0 + 1, 7);
        favo = (unsigned)(OFF_FA + 128 * who);
    }
    {
        const int brow = 64 * wm + l31;
        unsigned ba[4];
#pragma unroll
        for (int kk = 0; kk < 4; ++kk) ba[kk] = (unsigned)(brow * 128 + (((2 * kk + hh) ^ ((brow >> 1) & 7)) << 4));
        const int G = lane >> 4, q = (lane >> 2) & 3, pp = lane & 3;
        unsigned aa[3];
#pragma unroll
        for (int fb = 0; fb < 3; ++fb) {
            const int fbg = 3 * wf + fb;
            aa[fb] = (unsigned)(((G & 1) ? G1_U + (fbg ^ 1) * 32 : G1_G + fbg * 32) + (8 * (G >> 1) + q) * 192 + 8 * pp);
        }
        v4u fb0[2], fb1[2];
        v2u fa0[3][2], fa1[3][2];
#define RDB(d, kk, mt) d = lds_ld<v4u>(ba[kk] + (mt) * 4096)
#define RDA(d, kk, fb, hi) d[fb][hi] = tr_read(aa[fb] + (kk) * 16 * 192 + (hi) * 4 * 192)
#define MM(A, B, fb, mt) acc[fb][mt] = mfma32(v4u{A[fb][0].x, A[fb][0].y, A[fb][1].x, A[fb][1].y}, B[mt], acc[fb][mt])
        if (w < 2) WAIT_V(13); else WAIT_V(18);
        WAIT_L(0);
        BAR();
        STAMP(p, 2);
        RDB(fb0[0], 0, 0); RDA(fa0, 0, 0, 0); RDA(fa0, 0, 0, 1); RDB(fb0[1], 0, 1); RDA(fa0, 0, 1, 0); RDA(fa0, 0, 1, 1); RDA(fa0, 0, 2, 0); RDA(fa0, 0, 2, 1);
        int st = 0;
        unsigned mx = 2 * G1_STAGE + 4096u * w, mw = 2 * G1_STAGE + 3072u * w;
        unsigned sx = kap * 128, sw = kap * 64 * 3072;
#define STEP(FA, FB, GA, GB, kn, D0, D1, D2)                                                     \
            MM(FA, FB, 0, 0); SB(); RDB(GB[0], kn, 0); RDA(GA, kn, 0, 0); SB();                 \
            MM(FA, FB, 0, 1); SB(); RDA(GA, kn, 0, 1); RDB(GB[1], kn, 1); SB();                 \
            MM(FA, FB, 1, 0); SB(); RDA(GA, kn, 1, 0); RDA(GA, kn, 1, 1); D0##_M; SB();         \
            MM(FA, FB, 1, 1); SB(); D0##_L; RDA(GA, kn, 2, 0); D1##_M; SB();                    \
            MM(FA, FB, 2, 0); SB(); D1##_L; RDA(GA, kn, 2, 1); D2##_M; SB();                    \
            MM(FA, FB, 2, 1); SB(); D2##_L; SB();
#define X0_M SET_M0(mx, 0)
#define X0_L dma16_m0_sc1(rsX, xoff[0], sx)
#define X1_M SET_M0(mx, 1024)
#define X1_L dma16_m0_sc1(rsX, xoff[1], sx)
#define X2_M SET_M0(mx, 2048)
#define X2_L dma16_m0_sc1(rsX, xoff[2], sx)
#define X3_M SET_M0(mx, 3072)
#define X3_L dma16_m0_sc1(rsX, xoff[3], sx)
#define G0_M SET_M0(mw, G1_G)
#define G0_L dma16_m0(rsWg, goff[0], sw)
#define G1_M SET_M0(mw, G1_G + 1024)
#define G1_L dma16_m0(rsWg, goff[1], sw)
#define G2_M SET_M0(mw, G1_G + 2048)
#define G2_L dma16_m0(rsWg, goff[2], sw)
#define U0_M SET_M0(mw, G1_U)
#define U0_L dma16_m0(rsWu, uoff[0], sw)
#define U1_M SET_M0(mw, G1_U + 1024)
#define U1_L dma16_m0(rsWu, uoff[1], sw)
#define U2_M SET_M0(mw, G1_U + 2048)
#define U2_L dma16_m0(rsWu, uoff[2], sw)
#define KBODY(WAITV) { \
            const int dlt = (st == 3 * G1_STAGE) ? -3 * G1_STAGE : G1_STAGE; \
            SB(); \
            STEP(fa0, fb0, fa1, fb1, 1, X0, X1, X2) \
            STEP(fa1, fb1, fa0, fb0, 2, X3, G0, U0) \
            STEP(fa0, fb0, fa1, fb1, 3, G1, U1, G2) \
            U2_M; SB(); \
            MM(fa1, fb1, 0, 0); SB(); U2_L; ba[0] += dlt; aa[0] += dlt; SB(); \
            MM(fa1, fb1, 0, 1); SB(); aa[1] += dlt; aa[2] += dlt; \
            kap = kap == 9 ? 1 : kap + 1; \
            { const unsigned nx = (unsigned)(st >= G1_STAGE ? st - G1_STAGE : st + 3 * G1_STAGE); mx = nx + 4096u * w; mw = nx + 3072u * w; } sx = kap * 128; sw = kap * 64 * 3072; \
            SB(); \
            MM(fa1, fb1, 1, 0); SB(); \
            WAIT_L(0); \
            WAITV; \
            BAR(); \
            RDB(fb0[0], 0, 0); RDA(fa0, 0, 0, 0); RDA(fa0, 0, 0, 1); SB(); \
            MM(fa1, fb1, 1, 1); SB(); RDB(fb0[1], 0, 1); RDA(fa0, 0, 1, 0); RDA(fa0, 0, 1, 1); SB(); \
            MM(fa1, fb1, 2, 0); SB(); RDA(fa0, 0, 2, 0); RDA(fa0, 0, 2, 1); ba[1] += dlt; SB(); \
            MM(fa1, fb1, 2, 1); SB(); ba[2] += dlt; ba[3] += dlt; \
            st += dlt; \
            }
#pragma unroll 1
        for (int kt = 0; kt < 9; ++kt) {
            KBODY(if (__builtin_expect(kt == 0, 0)) { if (w < 2) WAIT_V(13); else WAIT_V(18); } else WAIT_V(10))
            if (__builtin_expect(kt == 1, 0)) __builtin_amdgcn_raw_buffer_store_b32(epoch, rsW, tid == 0 ? (unsigned)(OFF_FA + 128 * bid) : OOB, 0, SC1);
            STAMP(p, 18 + kt);
            if (__builtin_expect(kt == 7, 0)) {
                mw = DUMMY_W; sw = 0x40000000u;
                sx = has_ch ? 0u : 0x40000000u;
                mx = CV_RAW + 4096u * w;
                rsX = mkrs(p.hk, DM * SEQ * 2);
#pragma unroll
                for (int n = 0; n < 4; ++n) xoff[n] = (unsigned)(chs * SEQ * 2 + n * 1024 + lane * 16);
                dma4_sc1(rsW, favo, FLG_SCR + 256u * w);
            }
        }
        {
            const unsigned flv = lds_ld<unsigned>((unsigned)(FLG_SCR + 256 * w + lane * 4));
            if (__builtin_amdgcn_ballot_w64(flv != epoch) != 0ull) wait_words(rsW, favo, epoch);
            if (has_ch) {
#pragma unroll
                for (int n = 0; n < 4; ++n) dma16_sc1(rsW, (unsigned)(lane * 16), (unsigned)(OFF_KVR + ch * SEQ * 2 + n * 1024), (unsigned)(CV_XR + CV_XRS * w + n * 1024));
            }
#define N_M do {} while (0)
#define N_L do {} while (0)
            SB();
            STEP(fa0, fb0, fa1, fb1, 1, N, N, N)
            STEP(fa1, fb1, fa0, fb0, 2, N, N, N)
            STEP(fa0, fb0, fa1, fb1, 3, N, N, N)
            MM(fa1, fb1, 0, 0); MM(fa1, fb1, 0, 1); MM(fa1, fb1, 1, 0); MM(fa1, fb1, 1, 1); MM(fa1, fb1, 2, 0); MM(fa1, fb1, 2, 1);
        }
#undef MM
#undef STEP
#undef KBODY
    }
    STAMP(p, 3);
    if (tid == 0) { lds_st<unsigned>(LC_B, 0u); lds_st<unsigned>(LC_C, 0u); }
    WAIT_L(0);
    BAR();
    const int rot2 = (3 * (bi & 7) / 2 + 3 * (bi >> 3)) % 12;
    unsigned a2o[12];
#pragma unroll
    for (int s = 0; s < 12; ++s) { const int ks = s + rot2; a2o[s] = (unsigned)(12 * w + (ks >= 12 ? ks - 12 : ks)) * 1024u; }
    const unsigned a2base = (unsigned)(OFF_WDT + dt * 48 * 5 * 1024);
    v4u A2[12][5];
#pragma unroll
    for (int slot = 0; slot < 8; ++slot) A2[slot / 5][slot % 5] = bld16<SC1>(rsW, lane * 16, a2base + a2o[slot / 5] * 5 + (slot % 5) * 1024);
    SB();
#pragma unroll
    for (int fb = 0; fb < 3; ++fb)
#pragma unroll
        for (int mt = 0; mt < 2; ++mt) {
            const f32x16& a = acc[fb][mt];
            v4u o;
#pragma unroll
            for (int j = 0; j < 4; ++j) o[j] = pk2(a[2 * j] * a[8 + 2 * j], a[2 * j + 1] * a[8 + 2 * j + 1]);
            const int Ft = 6 * ft + 3 * wf + fb;
            const int mrow = 128 * mt1 + 64 * wm + 32 * mt + l31;
            const int pos = (lane & 15) + 16 * (2 * (Ft & 1) + hh);
            bst16<SC1>(rsW, (unsigned)(OFF_HF + ((mrow >> 4) * 48 + (Ft >> 1)) * 1024 + pos * 16), 0, o);
        }
    STAMP(p, 4);

    if (has_ch) {
        const unsigned HP = (unsigned)(CV_HP + w * CV_HPS), XR = (unsigned)(CV_XR + w * CV_XRS), RAW = (unsigned)(CV_RAW + w * 4096);
        const unsigned sdv = p.sd[ch];
        WAIT_V(14);
#pragma unroll
        for (int ps = 0; ps < 5; ++ps) {
            const int n = 64 * ps + lane;
            v4u c0 = {0, 0, 0, 0}, c1 = {0, 0, 0, 0};
            if (ps < 4) {
                c0 = lds_ld<v4u>(RAW + 16 * (ps == 0 ? max(n - 2, 0) : n - 2));
                c1 = lds_ld<v4u>(RAW + 16 * (ps == 0 ? max(n - 1, 0) : n - 1));
                if (ps == 0) { if (n < 2) c0 = v4u{0, 0, 0, 0}; if (n < 1) c1 = v4u{0, 0, 0, 0}; }
            } else {
                c0 = lds_ld<v4u>(RAW + 16 * (254 + (lane & 1)));
                c1 = lds_ld<v4u>(RAW + 16 * 255);
                if (lane & 1) c1 = v4u{0, 0, 0, 0};
            }
            const unsigned Dw[6] = {c0.x, c0.y, c0.z, c0.w, c1.x, c1.y};
            v4u o0, o1, o2, o3;
#pragma unroll
            for (int q = 0; q < 4; ++q) {
                o0[q] = __builtin_amdgcn_alignbit(Dw[q + 1], Dw[q], 16);
                o1[q] = Dw[q + 1];
                o2[q] = __builtin_amdgcn_alignbit(Dw[q + 2], Dw[q + 1], 16);
                o3[q] = Dw[q + 2];
            }
            if (ps < 4 || lane < 2) {
                lds_st<v4u>(HP + 0 * CV_CS + 16 * n, o0);
                lds_st<v4u>(HP + 1 * CV_CS + 16 * n, o1);
                lds_st<v4u>(HP + 2 * CV_CS + 16 * n, o2);
                lds_st<v4u>(HP + 3 * CV_CS + 16 * n, o3);
            }
        }
        if (lane < 32) lds_st<v4u>(XR + 4096 + 16 * lane, v4u{0, 0, 0, 0});
        asm volatile("" ::: "memory");
        STAMP(p, 5);

        const int r = lane & 15, g = lane >> 4;
        unsigned ap0 = HP + (unsigned)((r & 3) * CV_CS + 2 * ((r & ~3) + 8 * g));
        unsigned ap1 = ap0 + 8;
        PIN_V(ap0); PIN_V(ap1);
        const unsigned bp = XR + (unsigned)(2 * (240 - 16 * r + 8 * g));
        f32x4 cacc[8];
#pragma unroll
        for (int i = 0; i < 8; ++i) cacc[i] = f32x4{0.f, 0.f, 0.f, 0.f};
        v4u CA[2][8], CB[2][8];
        auto cvrd = [&](int set, int s, int i) {
            CB[set][i] = lds_ld<v4u>(bp + 512 * (7 - i) + 64 * s);
            const v2u lo = lds_ld<v2u>(ap0 + 512 * i + 64 * s), hi = lds_ld<v2u>(ap1 + 512 * i + 64 * s);
            CA[set][i] = v4u{lo.x, lo.y, hi.x, hi.y};
        };
        const unsigned long long cyc2 = __builtin_amdgcn_s_memtime();
#pragma unroll
        for (int i = 0; i < 8; ++i) cvrd(0, 0, i);
#pragma unroll
        for (int s = 0; s < 8; ++s) {
            const int cur = s & 1;
            SB();
#pragma unroll
            for (int gm = 0; gm < 8; ++gm) {
#pragma unroll
                for (int be = 0; be <= gm; ++be) cacc[gm] = mfma16(CA[cur][be], CB[cur][gm - be], cacc[gm]);
                SB();
                if (s < 7) cvrd(cur ^ 1, s + 1, gm);
                { const int slot = 8 + 8 * s + gm; if (slot < 60) A2[slot / 5][slot % 5] = bld16<SC1>(rsW, lane * 16, a2base + a2o[slot / 5] * 5 + (slot % 5) * 1024); }
                SB();
            }
            if (s == 1) {
                WAIT_V(16);
                lds_arrive(LC_B);
            }
        }
        STAMP(p, 6);
        { const unsigned long long cyc3 = __builtin_amdgcn_s_memtime(); if (tid == 0) ((GAS unsigned long long*)(p.ws + OFF_STAMP))[blockIdx.x * 32 + 30] = cyc3 - cyc2; }
        const float Dc = blo(sdv);
#pragma unroll
        for (int gm = 0; gm < 8; ++gm) {
            const int t0 = 256 * gm + 16 * r + 4 * g;
            const v2u kvp = lds_ld<v2u>(XR + 2 * (2044 - t0));
            const float y0 = cacc[gm][0] + bhi(kvp.y) * Dc, y1 = cacc[gm][1] + blo(kvp.y) * Dc, y2 = cacc[gm][2] + bhi(kvp.x) * Dc, y3 = cacc[gm][3] + blo(kvp.x) * Dc;
            lds_st<v2u>(HP + 2 * t0, v2u{pk2(y0, y1), pk2(y2, y3)});
        }
        asm volatile("" ::: "memory");
#pragma unroll
        for (int ps = 0; ps < 4; ++ps) {
            const int n = 64 * ps + lane;
            bst16<SC1>(rsW, (unsigned)(OFF_YT + (ch * SEQ + 8 * n) * 2), 0, lds_ld<v4u>(HP + 16 * n));
        }
    } else {
        if (w == 3) {
            const rsrc_t rsG = mkrs(p.gw, DM * 2);
            const int c16 = lane & 15, row0 = 128 * mt1 + 8 * ft + (lane >> 4);
            v4u gwv[5], xa[5], xb[5];
#pragma unroll
            for (int i = 0; i < 5; ++i) {
                gwv[i] = bld16<0>(rsG, (unsigned)(16 * (c16 + 16 * i)), 0);
                xa[i] = bld16<0>(rsQ, (unsigned)(row0 * DM * 2 + 16 * (c16 + 16 * i)), 0);
                xb[i] = bld16<0>(rsQ, (unsigned)((row0 + 4) * DM * 2 + 16 * (c16 + 16 * i)), 0);
            }
            float s0 = 0.f, s1 = 0.f;
#pragma unroll
            for (int i = 0; i < 5; ++i)
#pragma unroll
                for (int e = 0; e < 4; ++e) { s0 = dot2(xa[i][e], gwv[i][e], s0); s1 = dot2(xb[i][e], gwv[i][e], s1); }
#pragma unroll
            for (int m = 1; m < 16; m <<= 1) { s0 += __shfl_xor(s0, m); s1 += __shfl_xor(s1, m); }
            const float g0 = rb(1.0f / (1.0f + __expf(-rb(s0)))), g1 = rb(1.0f / (1.0f + __expf(-rb(s1))));
            __builtin_amdgcn_raw_buffer_store_b32(__builtin_bit_cast(unsigned, g0), rsW, c16 == 0 ? (unsigned)(OFF_GATE + row0 * 4) : OOB, 0, SC1);
            __builtin_amdgcn_raw_buffer_store_b32(__builtin_bit_cast(unsigned, g1), rsW, c16 == 0 ? (unsigned)(OFF_GATE + (row0 + 4) * 4) : OOB, 0, SC1);
        }
        WAIT_V(0);
        lds_arrive(LC_B);
        lds_wait(LC_B, 4);
        if (w == 3) {
            if (lane == 0) {
                const unsigned old = add_count(p, OFF_CB + 256 * mt1, 1u);
                if (old == 15u) set_flag(p, OFF_RB + 256 * mt1, 0, epoch);
            }
            if (bid == 0) {
#pragma unroll 1
                for (int i = 0; i < 4; ++i) wait_words(rsW, (unsigned)(OFF_FA + 128 * (64 * i + lane)), epoch);
                if (lane == 0) set_flag(p, OFF_EPOCH, 0, epoch);
            }
        }
#pragma unroll
        for (int slot = 8; slot < 60; ++slot) A2[slot / 5][slot % 5] = bld16<SC1>(rsW, lane * 16, a2base + a2o[slot / 5] * 5 + (slot % 5) * 1024);
    }
    STAMP(p, 7);

    wait_ready<4>(p, OFF_RB + 256 * (mt2 >> 1), epoch);
    STAMP(p, 8);
    constexpr int W2 = 8;
    unsigned b2off[4];
#pragma unroll
    for (int n = 0; n < 4; ++n) b2off[n] = (unsigned)(OFF_HF + (4 * mt2 + ((w + n) & 3)) * 48 * 1024);
    v4u B2[W2][4];
#pragma unroll
    for (int s = 0; s < W2; ++s)
#pragma unroll
        for (int n = 0; n < 4; ++n) B2[s][n] = bld16<SC1>(rsW, lane * 16, b2off[n] + a2o[s]);
    SB();
    if (has_ch) {
        WAIT_V(32);
        lds_arrive(LC_C);
    }
    f32x4 acc2[5][4];
#pragma unroll
    for (int a = 0; a < 5; ++a)
#pragma unroll
        for (int n = 0; n < 4; ++n) acc2[a][n] = f32x4{0.f, 0.f, 0.f, 0.f};
    const int t = 64 * mt2 + 16 * w + (lane & 15), g4 = lane >> 4;
    v2u qv[5];
    float gt;
#pragma unroll
    for (int s = 0; s < 12; ++s) {
        v4u cb[4];
#pragma unroll
        for (int n = 0; n < 4; ++n) cb[n] = B2[s % W2][n];
        if (s + W2 < 12) {
#pragma unroll
            for (int n = 0; n < 4; ++n) B2[s % W2][n] = bld16<SC1>(rsW, lane * 16, b2off[n] + a2o[s + W2]);
        }
        if (s == 6) {
#pragma unroll
            for (int a = 0; a < 5; ++a) qv[a] = *(const GAS v2u*)(p.q + (size_t)t * DM + 80 * dt + 16 * a + 4 * g4);
            gt = __builtin_bit_cast(float, __builtin_amdgcn_raw_buffer_load_b32(rsW, (unsigned)(OFF_GATE + t * 4), 0, SC1));
        }
#pragma unroll
        for (int n = 0; n < 4; ++n)
#pragma unroll
            for (int a = 0; a < 5; ++a) acc2[a][n] = mfma16(A2[s][a], cb[n], acc2[a][n]);
        SB();
    }
    STAMP(p, 9);
    lds_wait(LC_B, 4);
#pragma unroll
    for (int a = 0; a < 5; ++a)
#pragma unroll
        for (int j = 1; j < 4; ++j) lds_st<f32x4>((unsigned)(G2_RED + ((((w + j) & 3) * 5 + a) * 3 + (j - 1)) * 1024 + lane * 16), acc2[a][j]);
    if (w == 3) {
        lds_wait(LC_C, (unsigned)nch);
        {
            const int c0 = ch - 3, g0 = c0 / 80, n0 = min(nch, 80 * (g0 + 1) - c0), gi = g0 + lane, ni = lane == 0 ? n0 : nch - n0;
            if (lane < 2 && ni > 0) {
                const unsigned old = add_count(p, OFF_CC + 256 * gi, (unsigned)ni);
                if (old + (unsigned)ni == 80u) set_flag(p, OFF_RC + 256 * gi, 0, epoch);
            }
        }
        wait_ready<4>(p, OFF_RC + 256 * dt, epoch);
        v4u yv[10];
#pragma unroll
        for (int it = 0; it < 10; ++it) {
            const int c = lane + 64 * it;
            yv[it] = bld16<SC1>(rsW, (unsigned)(OFF_YT + ((80 * dt + (c >> 3)) * SEQ + 64 * mt2 + 8 * (c & 7)) * 2), 0);
        }
#pragma unroll
        for (int it = 0; it < 10; ++it) lds_st<v4u>((unsigned)(G2_Y + (lane + 64 * it) * 16), yv[it]);
    }
    WAIT_L(0);
    BAR();
    STAMP(p, 10);
    const float omg = rb(1.0f - gt);
    const int q = (lane >> 2) & 3, pp = lane & 3;
#pragma unroll
    for (int a = 0; a < 5; ++a) {
        f32x4 b = acc2[a][0];
#pragma unroll
        for (int j = 0; j < 3; ++j) b += lds_ld<f32x4>((unsigned)(G2_RED + ((w * 5 + a) * 3 + j) * 1024 + lane * 16));
        const v2u y = tr_read((unsigned)(G2_Y + (16 * a + 4 * g4 + q) * 128 + 32 * w + 8 * pp));
        const int d0 = 80 * dt + 16 * a + 4 * g4;
        const float o0 = gt * (blo(qv[a].x) * blo(y.x)) + omg * b[0], o1 = gt * (bhi(qv[a].x) * bhi(y.x)) + omg * b[1];
        const float o2 = gt * (blo(qv[a].y) * blo(y.y)) + omg * b[2], o3 = gt * (bhi(qv[a].y) * bhi(y.y)) + omg * b[3];
        *(GAS v2u*)(p.out + (size_t)t * DM + d0) = v2u{pk2(o0, o1), pk2(o2, o3)};
    }
    STAMP(p, 11);
    if (tid == 0 && (bid == 0 || bid == 77 || bid == 133 || bid == 255)) {
        WAIT_V(0);
        const GAS unsigned long long* st = (const GAS unsigned long long*)(p.ws + OFF_STAMP) + bid * 32;
        unsigned long long t0 = __hip_atomic_load(st, RLX_AGENT);
        int v[27];
        for (int i = 0; i < 27; ++i) v[i] = (int)(__hip_atomic_load(st + i, RLX_AGENT) - t0);
        printf("B%d t0=%llu: 14:%d 1:%d 15:%d 12:%d 13:%d 17:%d 16:%d 2:%d | 18:%d 19:%d 20:%d 21:%d 22:%d 23:%d 24:%d 25:%d 26:%d | 3:%d 4:%d 5:%d 6:%d 7:%d 8:%d 9:%d 10:%d 11:%d\n", bid, t0 % 1000000,
               v[14], v[1], v[15], v[12], v[13], v[17], v[16], v[2], v[18], v[19], v[20], v[21], v[22], v[23], v[24], v[25], v[26], v[3], v[4], v[5], v[6], v[7], v[8], v[9], v[10], v[11]);
    }
}

extern "C" void kernel_launch(void* const* d_in, const int* in_sizes, int n_in, void* d_out, int out_size, void* d_ws, size_t ws_size, hipStream_t stream) {
    Params p;
    p.q = (const u16*)d_in[0]; p.k = (const u16*)d_in[1]; p.v = (const u16*)d_in[2]; p.sk = (const u16*)d_in[3];
    p.hk = (const u16*)d_in[4]; p.sd = (const u16*)d_in[5];
    p.wg = (const u16*)d_in[7]; p.wu = (const u16*)d_in[8]; p.wd = (const u16*)d_in[9]; p.gw = (const u16*)d_in[10];
    p.out = (u16*)d_out; p.ws = (unsigned char*)d_ws;
    (void)hipFuncSetAttribute((const void*)k_fused, hipFuncAttributeMaxDynamicSharedMemorySize, LDS_BYTES);
    k_fused<<<256, 256, LDS_BYTES, stream>>>(p);
}
```

```cpp
#include <hip/hip_runtime.h>
#include <stdint.h>

#define LAS __attribute__((address_space(3)))
#define GAS __attribute__((address_space(1)))
typedef unsigned short u16;
typedef unsigned v4u __attribute__((ext_vector_type(4)));
typedef unsigned v2u __attribute__((ext_vector_type(2)));
typedef __bf16 bf8 __attribute__((ext_vector_type(8)));
typedef __bf16 bf2 __attribute__((ext_vector_type(2)));
typedef short s4 __attribute__((ext_vector_type(4)));
typedef float f32x2 __attribute__((ext_vector_type(2)));
typedef float f32x4 __attribute__((ext_vector_type(4)));
typedef float f32x16 __attribute__((ext_vector_type(16)));
typedef __amdgpu_buffer_rsrc_t rsrc_t;

constexpr int SEQ = 2048, DM = 640, FF = 1536;
constexpr int OFF_EPOCH = 0;
constexpr int OFF_FA = 32768;
constexpr int OFF_CB = 2048;
constexpr int OFF_CC = 8192;
constexpr int OFF_RB = 12288;
constexpr int OFF_RC = 16384;
constexpr int OFF_STAMP = 32 * 1024 * 1024;
constexpr int OFF_KVR = 65536;
constexpr int OFF_WDT = OFF_KVR + DM * SEQ * 2;
constexpr int OFF_HF = OFF_WDT + FF * DM * 2;
constexpr int OFF_YT = OFF_HF + SEQ * FF * 2;
constexpr int OFF_GATE = OFF_YT + DM * SEQ * 2;
constexpr int WS_BYTES = OFF_GATE + SEQ * 4;
constexpr int SC1 = 16;
constexpr unsigned OOB = 0x80000000u;
constexpr unsigned SPIN_MAX = 1u << 16;

struct Params {
    const u16 *q, *k, *v, *sk, *hk, *sd, *wg, *wu, *wd, *gw;
    u16* out;
    unsigned char* ws;
};

__device__ __forceinline__ unsigned pk2(float a, float b) {
    f32x2 x = {a, b};
    return __builtin_bit_cast(unsigned, __builtin_convertvector(x, bf2));
}
__device__ __forceinline__ float blo(unsigned u) { return __builtin_bit_cast(float, u << 16); }
__device__ __forceinline__ float bhi(unsigned u) { return __builtin_bit_cast(float, u & 0xffff0000u); }
__device__ __forceinline__ float rb(float x) { return blo(pk2(x, x)); }
__device__ __forceinline__ f32x4 mfma16(v4u a, v4u b, f32x4 c) {
    return __builtin_amdgcn_mfma_f32_16x16x32_bf16(__builtin_bit_cast(bf8, a), __builtin_bit_cast(bf8, b), c, 0, 0, 0);
}
__device__ __forceinline__ f32x16 mfma32(v4u a, v4u b, f32x16 c) {
    return __builtin_amdgcn_mfma_f32_32x32x16_bf16(__builtin_bit_cast(bf8, a), __builtin_bit_cast(bf8, b), c, 0, 0, 0);
}
__device__ __forceinline__ float dot2(unsigned a, unsigned b, float c) {
    return __builtin_amdgcn_fdot2_f32_bf16(__builtin_bit_cast(bf2, a), __builtin_bit_cast(bf2, b), c, false);
}
template <class T> __device__ __forceinline__ T lds_ld(unsigned a) { return *(LAS T*)(uintptr_t)a; }
template <class T> __device__ __forceinline__ void lds_st(unsigned a, T v) { *(LAS T*)(uintptr_t)a = v; }
__device__ __forceinline__ v2u tr_read(unsigned a) {
    return __builtin_bit_cast(v2u, __builtin_amdgcn_ds_read_tr16_b64_v4i16((LAS s4*)(uintptr_t)a));
}
__device__ __forceinline__ rsrc_t mkrs(const void* p, unsigned bytes) { return __builtin_amdgcn_make_buffer_rsrc((void*)p, 0, bytes, 0x00020000); }
template <int AUX> __device__ __forceinline__ v4u bld16(rsrc_t rs, unsigned voff, unsigned soff) { return __builtin_amdgcn_raw_buffer_load_b128(rs, voff, soff, AUX); }
template <int AUX> __device__ __forceinline__ void bst16(rsrc_t rs, unsigned voff, unsigned soff, v4u v) { __builtin_amdgcn_raw_buffer_store_b128(v, rs, voff, soff, AUX); }
#define WAIT_V(n) asm volatile("s_waitcnt vmcnt(" #n ")" ::: "memory")
#define WAIT_L(n) asm volatile("s_waitcnt lgkmcnt(" #n ")" ::: "memory")
#define BAR() do { asm volatile("" ::: "memory"); __builtin_amdgcn_s_barrier(); asm volatile("" ::: "memory"); } while (0)
#define SB() __builtin_amdgcn_sched_barrier(0)
#define PIN_S(x) asm volatile("" : "+s"(x))
#define PIN_V(x) asm volatile("" : "+v"(x))
#define RLX_AGENT __ATOMIC_RELAXED, __HIP_MEMORY_SCOPE_AGENT
#define STAMP(p, i) do {} while (0)

__device__ __forceinline__ void dma16(rsrc_t rs, unsigned voff, unsigned soff, unsigned lds_addr) {
    asm volatile("s_mov_b32 m0, %0\n\ts_nop 0\n\tbuffer_load_dwordx4 %1, %2, %3 offen lds" ::"s"(lds_addr), "v"(voff), "s"(rs), "s"(soff) : "memory", "m0");
}
__device__ __forceinline__ void dma16_sc1(rsrc_t rs, unsigned voff, unsigned soff, unsigned lds_addr) {
    asm volatile("s_mov_b32 m0, %0\n\ts_nop 0\n\tbuffer_load_dwordx4 %1, %2, %3 offen sc1 lds" ::"s"(lds_addr), "v"(voff), "s"(rs), "s"(soff) : "memory", "m0");
}
#define SET_M0(base, imm) asm volatile("s_add_u32 m0, %0, %1" ::"s"(base), "i"(imm) : "memory", "m0", "scc")
__device__ __forceinline__ void dma16_m0(rsrc_t rs, unsigned voff, unsigned soff) {
    asm volatile("buffer_load_dwordx4 %0, %1, %2 offen lds" ::"v"(voff), "s"(rs), "s"(soff) : "memory");
}
__device__ __forceinline__ void dma16_m0_sc1(rsrc_t rs, unsigned voff, unsigned soff) {
    asm volatile("buffer_load_dwordx4 %0, %1, %2 offen sc1 lds" ::"v"(voff), "s"(rs), "s"(soff) : "memory");
}
__device__ __forceinline__ void aload16(v4u& d, rsrc_t rs, unsigned voff) {
    asm volatile("buffer_load_dwordx4 %0, %1, %2, 0 offen" : "=v"(d) : "v"(voff), "s"(rs) : "memory");
}

__device__ __forceinline__ void wait_words(rsrc_t rsW, unsigned voff, unsigned e) {
    unsigned t0, left = SPIN_MAX;
    asm volatile(
        "1:\n\t"
        "buffer_load_dword %0, %2, %3, 0 offen sc1\n\t"
        "s_waitcnt vmcnt(0)\n\t"
        "v_cmp_ne_u32 vcc, %4, %0\n\t"
        "s_nop 4\n\t"
        "s_cbranch_vccz 2f\n\t"
        "s_sleep 8\n\t"
        "s_sub_u32 %1, %1, 1\n\t"
        "s_cbranch_scc0 1b\n\t"
        "2:"
        : "=&v"(t0), "+s"(left)
        : "v"(voff), "s"(rsW), "s"(e)
        : "memory", "vcc", "scc");
}
__device__ __forceinline__ void dma4_sc1(rsrc_t rs, unsigned voff, unsigned lds_addr) {
    asm volatile("s_mov_b32 m0, %0\n\ts_nop 0\n\tbuffer_load_dword %1, %2, 0 offen sc1 lds" ::"s"(lds_addr), "v"(voff), "s"(rs) : "memory", "m0");
}
__device__ __forceinline__ unsigned add_count(const Params& p, int off, unsigned n) {
    return __hip_atomic_fetch_add((GAS unsigned*)(p.ws + off), n, RLX_AGENT);
}
template <int SLEEP>
__device__ __forceinline__ void wait_ready(const Params& p, int off, unsigned e) {
    GAS unsigned* c = (GAS unsigned*)(p.ws + off);
    for (unsigned it = 0; it < SPIN_MAX; ++it) {
        if ((unsigned)__builtin_amdgcn_readfirstlane(__hip_atomic_load(c, RLX_AGENT)) == e) break;
        __builtin_amdgcn_s_sleep(SLEEP);
    }
    __builtin_amdgcn_fence(__ATOMIC_ACQUIRE, "wavefront");
}
__device__ __forceinline__ void lds_arrive(unsigned off) {
    if ((threadIdx.x & 63) == 0) (void)__hip_atomic_fetch_add((LAS unsigned*)(uintptr_t)off, 1u, __ATOMIC_RELAXED, __HIP_MEMORY_SCOPE_WORKGROUP);
}
__device__ __forceinline__ void lds_wait(unsigned off, unsigned n) {
    for (unsigned it = 0; it < (SPIN_MAX << 4); ++it) {
        if ((unsigned)__builtin_amdgcn_readfirstlane(__hip_atomic_load((LAS unsigned*)(uintptr_t)off, __ATOMIC_RELAXED, __HIP_MEMORY_SCOPE_WORKGROUP)) >= n) break;
        __builtin_amdgcn_s_sleep(1);
    }
    asm volatile("" ::: "memory");
}
__device__ __forceinline__ void set_flag(const Params& p, int off, int idx, unsigned e) {
    __hip_atomic_store((GAS unsigned*)(p.ws + off) + idx, e, RLX_AGENT);
}

constexpr int G1_STAGE = 40960, G1_G = 16384, G1_U = 16384 + 12288;
constexpr int PR_K = 3 * G1_STAGE;
constexpr int PR_V = PR_K + 11264;
constexpr int PR_S = PR_V + 10240;
constexpr int PR_D = PR_S + 1024;
constexpr int CV_XR = 0, CV_XRS = 4608;
constexpr int CV_CS = 4160, CV_HP = 3 * CV_XRS, CV_HPS = 4 * CV_CS;
constexpr int G2_Y = CV_HP + 3 * CV_HPS;
constexpr int CV_RAW = 2 * G1_STAGE;
constexpr int DUMMY_W = 2 * G1_STAGE;
constexpr int DUMMY_X = 2 * G1_STAGE + G1_G;
constexpr int FLG_SCR = 102400;
constexpr int G2_RED = 98304;
constexpr int LC_B = 163808, LC_C = 163824;
constexpr int LDS_BYTES = 163840;
static_assert(G2_Y + 80 * 128 <= CV_RAW && G2_RED + 60 * 1024 <= LC_B, "lds");

__global__ void __launch_bounds__(256, 1) k_fused(Params p) {
    extern __shared__ __attribute__((aligned(16))) unsigned char smem[];
    const int tid = threadIdx.x, lane = tid & 63, w = __builtin_amdgcn_readfirstlane(tid >> 6);
    const int bid = blockIdx.x;
    const int xcd = bid & 7, bi = bid >> 3, mq = xcd >> 1, half = xcd & 1;
    STAMP(p, 0);
    {
        unsigned long long pc;
        asm volatile("s_getpc_b64 %0" : "=s"(pc));
        const rsrc_t rsC = mkrs((const void*)(pc & ~255ull), 24576 - 256);
        dma16(rsC, (unsigned)(((bi + 32 * w) << 10) + lane * 16), 0, 160000 - 1024);
    }
    const int nch = bid < 128 ? 3 : 2, ch = (bid < 128 ? 3 * bid : 384 + 2 * (bid - 128)) + w;
    const bool has_ch = w < nch;
    unsigned sdv = *(const __attribute__((address_space(4))) unsigned*)(uintptr_t)(p.sd + (min(ch, DM - 1) & ~1));
    PIN_S(sdv);
    rsrc_t rsW = mkrs(p.ws, WS_BYTES);
    rsrc_t rsQ = mkrs(p.q, SEQ * DM * 2);

    const int ptt = bid & 31, pd8 = bid >> 5;
    {
        const rsrc_t rsK = mkrs(p.k, SEQ * DM * 2), rsV = mkrs(p.v, SEQ * DM * 2), rsS = mkrs(p.sk, DM * 4 * 2), rsD = mkrs(p.wd, FF * DM * 2);
        auto kdma = [&](int i) {
            const int c = 64 * i + lane, row = c / 10, col = c - 10 * row, t = 64 * ptt - 3 + row;
            dma16(rsK, (t >= 0 && c < 670) ? (unsigned)((t * DM + 80 * pd8 + 8 * col) * 2) : OOB, 0, PR_K + 1024 * i);
        };
        auto vdma = [&](int i) {
            const int c = 64 * i + lane, row = c / 10, col = c - 10 * row;
            dma16(rsV, (unsigned)(((64 * ptt + row) * DM + 80 * pd8 + 8 * col) * 2), 0, PR_V + 1024 * i);
        };
        auto ddma = [&](int j, int i) {
            const int g = 2 * bid + j, gdt = g / 48, gks = g - 48 * gdt;
            const int c = 64 * i + lane, row = c / 10, col = c - 10 * row;
            dma16(rsD, g < 384 ? (unsigned)(((32 * gks + row) * DM + 80 * gdt + 8 * col) * 2) : OOB, 0, PR_D + 5120 * j + 1024 * i);
        };
        if (w == 0) {
#pragma unroll
            for (int i = 0; i < 8; ++i) kdma(i);
        } else if (w == 1) {
#pragma unroll
            for (int i = 8; i < 11; ++i) kdma(i);
#pragma unroll
            for (int i = 0; i < 5; ++i) vdma(i);
        } else if (w == 2) {
#pragma unroll
            for (int i = 5; i < 10; ++i) vdma(i);
#pragma unroll
            for (int i = 0; i < 5; ++i) ddma(0, i);
        } else {
            dma16(rsS, lane < 40 ? (unsigned)(80 * pd8 * 8 + 16 * lane) : OOB, 0, PR_S);
#pragma unroll
            for (int i = 0; i < 5; ++i) ddma(1, i);
        }
    }
    STAMP(p, 14);

    const int mt1 = 4 * mq + (bi & 3), ft = 8 * half + (bi >> 2);
    const int wm = w & 1, wf = w >> 1;
    const int rot1 = ((ft & 7) + ((5 * (mt1 & 3)) >> 1)) % 9;
    rsrc_t rsWg = mkrs(p.wg, DM * FF * 2), rsWu = mkrs(p.wu, DM * FF * 2), rsX = rsQ;
    PIN_S(rsW); PIN_S(rsWg); PIN_S(rsWu); PIN_S(rsX);
    unsigned xoff[4], goff[3], uoff[3];
#pragma unroll
    for (int n = 0; n < 4; ++n) {
        const int row = 32 * w + 8 * n + (lane >> 3);
        xoff[n] = (unsigned)((128 * mt1 + row) * 1280 + (((lane & 7) ^ ((4 * n + (lane >> 4)) & 7)) << 4));
    }
#pragma unroll
    for (int n = 0; n < 3; ++n) {
        const int Lc = 64 * (3 * w + n) + lane, k = Lc / 12, c = Lc - 12 * k;
        goff[n] = (unsigned)(k * 3072 + 192 * ft + 16 * c);
        uoff[n] = (unsigned)(k * 3072 + 192 * ft + 16 * (c ^ 2));
    }
    auto g1_issue = [&](int kt, int stg) {
#pragma unroll
        for (int n = 0; n < 4; ++n) dma16_sc1(rsX, xoff[n], kt * 128, stg + (4 * w + n) * 1024);
#pragma unroll
        for (int n = 0; n < 3; ++n) {
            dma16(rsWg, goff[n], kt * 64 * 3072, stg + G1_G + (3 * w + n) * 1024);
            dma16(rsWu, uoff[n], kt * 64 * 3072, stg + G1_U + (3 * w + n) * 1024);
        }
    };
    g1_issue(0, 0);
    const unsigned epoch = *(const __attribute__((address_space(4))) unsigned*)(uintptr_t)(p.ws + OFF_EPOCH) + 1u;
    f32x16 acc[3][2];
#pragma unroll
    for (int a = 0; a < 3; ++a)
#pragma unroll
        for (int b = 0; b < 2; ++b)
#pragma unroll
            for (int r = 0; r < 16; ++r) acc[a][b][r] = 0.f;
    STAMP(p, 1);
    WAIT_V(10);
    BAR();
    STAMP(p, 15);
    int kap = 1 + rot1;
    g1_issue(kap, G1_STAGE);
    kap = kap == 9 ? 1 : kap + 1;
    STAMP(p, 12);

    if (bid < 24 && tid == 0) set_flag(p, bid < 16 ? OFF_CB + 256 * bid : OFF_CC + 256 * (bid - 16), 0, 0u);
    {
        const int tg = tid / 10, cc = tid - 10 * tg;
        v2u ov[8];
        if (tid < 160) {
            v4u kr[7], vr[4], tw[4];
#pragma unroll
            for (int i = 0; i < 7; ++i) kr[i] = lds_ld<v4u>((unsigned)(PR_K + (4 * tg + i) * 160 + 16 * cc));
#pragma unroll
            for (int j = 0; j < 4; ++j) vr[j] = lds_ld<v4u>((unsigned)(PR_V + (4 * tg + j) * 160 + 16 * cc));
#pragma unroll
            for (int i = 0; i < 4; ++i) tw[i] = lds_ld<v4u>((unsigned)(PR_S + 64 * cc + 16 * i));
#pragma unroll
            for (int e2 = 0; e2 < 4; ++e2) {
                const unsigned a01 = tw[e2][0], a23 = tw[e2][1], b01 = tw[e2][2], b23 = tw[e2][3];
                const f32x2 w0 = {blo(a01), blo(b01)}, w1 = {bhi(a01), bhi(b01)}, w2 = {blo(a23), blo(b23)}, w3 = {bhi(a23), bhi(b23)};
                f32x2 kf[7], r[4];
#pragma unroll
                for (int i = 0; i < 7; ++i) kf[i] = f32x2{blo(kr[i][e2]), bhi(kr[i][e2])};
#pragma unroll
                for (int j = 0; j < 4; ++j) {
                    f32x2 ks = kf[j] * w3;
                    ks = __builtin_elementwise_fma(kf[j + 1], w2, ks);
                    ks = __builtin_elementwise_fma(kf[j + 2], w1, ks);
                    ks = __builtin_elementwise_fma(kf[j + 3], w0, ks);
                    r[j] = ks * f32x2{blo(vr[j][e2]), bhi(vr[j][e2])};
                }
                ov[2 * e2] = v2u{pk2(r[3].x, r[2].x), pk2(r[1].x, r[0].x)};
                ov[2 * e2 + 1] = v2u{pk2(r[3].y, r[2].y), pk2(r[1].y, r[0].y)};
            }
        }
        if (w >= 2) {
            const int g = 2 * bid + (w - 2), gdt = g / 48, gks = g - 48 * gdt;
            const unsigned T = (unsigned)(PR_D + 5120 * (w - 2));
            const int gq = lane >> 4, q = (lane >> 2) & 3, pp = lane & 3;
            const int row = 16 * (gq >> 1) + 4 * (gq & 1) + q;
#pragma unroll
            for (int at = 0; at < 5; ++at) {
                const v2u lo = tr_read(T + row * 160 + (16 * at + 4 * pp) * 2);
                const v2u hi = tr_read(T + (row + 8) * 160 + (16 * at + 4 * pp) * 2);
                bst16<SC1>(rsW, g < 384 ? (unsigned)(OFF_WDT + ((gdt * 48 + gks) * 5 + at) * 1024 + lane * 16) : OOB, 0, v4u{lo.x, lo.y, hi.x, hi.y});
            }
        }
        STAMP(p, 13);
        WAIT_L(0);
        BAR();
        if (tid < 160) {
            const unsigned wo = (unsigned)(PR_V + cc * 1024 + (((7 - (tg >> 1)) ^ (cc & 7)) << 4) + ((tg & 1) ^ 1) * 8);
#pragma unroll
            for (int e = 0; e < 8; ++e) lds_st<v2u>(wo + e * 128, ov[e]);
        }
        WAIT_L(0);
        BAR();
        STAMP(p, 17);
#pragma unroll
        for (int i = 0; i < 3; ++i) {
            const int idx = tid + 256 * i, c = idx & 7, chl = min(idx >> 3, 79);
            const v4u val = lds_ld<v4u>((unsigned)(PR_V + chl * 128 + ((c ^ ((chl >> 3) & 7)) << 4)));
            bst16<SC1>(rsW, idx < 640 ? (unsigned)(OFF_KVR + ((80 * pd8 + chl) * SEQ + (SEQ - 64 - 64 * ptt) + 8 * c) * 2) : OOB, 0, val);
        }
    }
    STAMP(p, 16);

    const int chs = has_ch ? ch : 0;
    const int l31 = lane & 31, hh = lane >> 5;
    const int mt2 = 8 * mq + (bi & 7), dt = 4 * half + (bi >> 3);
    unsigned favo;
    {
        const int c0 = ch - w, g0 = c0 / 80;
        int who = 24 * dt + min(max(lane - 32, 0), 23);
        if (lane < 32 && has_ch) who = 32 * (ch / 80) + lane;
        if (lane == 56) who = mt1;
        if (lane == 57) who = 16 + g0;
        if (lane == 58) who = 16 + min(g0 + 1, 7);
        favo = (unsigned)(OFF_FA + 128 * who);
    }
    {
        const int brow = 64 * wm + l31;
        unsigned ba[4];
#pragma unroll
        for (int kk = 0; kk < 4; ++kk) ba[kk] = (unsigned)(brow * 128 + (((2 * kk + hh) ^ ((brow >> 1) & 7)) << 4));
        const int G = lane >> 4, q = (lane >> 2) & 3, pp = lane & 3;
        unsigned aa[3];
#pragma unroll
        for (int fb = 0; fb < 3; ++fb) {
            const int fbg = 3 * wf + fb;
            aa[fb] = (unsigned)(((G & 1) ? G1_U + (fbg ^ 1) * 32 : G1_G + fbg * 32) + (8 * (G >> 1) + q) * 192 + 8 * pp);
        }
        v4u fb0[2], fb1[2];
        v2u fa0[3][2], fa1[3][2];
#define RDB(d, kk, mt) d = lds_ld<v4u>(ba[kk] + (mt) * 4096)
#define RDA(d, kk, fb, hi) d[fb][hi] = tr_read(aa[fb] + (kk) * 16 * 192 + (hi) * 4 * 192)
#define MM(A, B, fb, mt) acc[fb][mt] = mfma32(v4u{A[fb][0].x, A[fb][0].y, A[fb][1].x, A[fb][1].y}, B[mt], acc[fb][mt])
        if (w < 2) WAIT_V(13); else WAIT_V(18);
        WAIT_L(0);
        BAR();
        STAMP(p, 2);
        RDB(fb0[0], 0, 0); RDA(fa0, 0, 0, 0); RDA(fa0, 0, 0, 1); RDB(fb0[1], 0, 1); RDA(fa0, 0, 1, 0); RDA(fa0, 0, 1, 1); RDA(fa0, 0, 2, 0); RDA(fa0, 0, 2, 1);
        int st = 0;
        unsigned mx = 2 * G1_STAGE + 4096u * w, mw = 2 * G1_STAGE + 3072u * w;
        unsigned sx = kap * 128, sw = kap * 64 * 3072;
#define STEP(FA, FB, GA, GB, kn, D0, D1, D2)                                                     \
            MM(FA, FB, 0, 0); SB(); RDB(GB[0], kn, 0); RDA(GA, kn, 0, 0); SB();                 \
            MM(FA, FB, 0, 1); SB(); RDA(GA, kn, 0, 1); RDB(GB[1], kn, 1); SB();                 \
            MM(FA, FB, 1, 0); SB(); RDA(GA, kn, 1, 0); RDA(GA, kn, 1, 1); D0##_M; SB();         \
            MM(FA, FB, 1, 1); SB(); D0##_L; RDA(GA, kn, 2, 0); D1##_M; SB();                    \
            MM(FA, FB, 2, 0); SB(); D1##_L; RDA(GA, kn, 2, 1); D2##_M; SB();                    \
            MM(FA, FB, 2, 1); SB(); D2##_L; SB();
#define X0_M SET_M0(mx, 0)
#define X0_L dma16_m0_sc1(rsX, xoff[0], sx)
#define X1_M SET_M0(mx, 1024)
#define X1_L dma16_m0_sc1(rsX, xoff[1], sx)
#define X2_M SET_M0(mx, 2048)
#define X2_L dma16_m0_sc1(rsX, xoff[2], sx)
#define X3_M SET_M0(mx, 3072)
#define X3_L dma16_m0_sc1(rsX, xoff[3], sx)
#define G0_M SET_M0(mw, G1_G)
#define G0_L dma16_m0(rsWg, goff[0], sw)
#define G1_M SET_M0(mw, G1_G + 1024)
#define G1_L dma16_m0(rsWg, goff[1], sw)
#define G2_M SET_M0(mw, G1_G + 2048)
#define G2_L dma16_m0(rsWg, goff[2], sw)
#define U0_M SET_M0(mw, G1_U)
#define U0_L dma16_m0(rsWu, uoff[0], sw)
#define U1_M SET_M0(mw, G1_U + 1024)
#define U1_L dma16_m0(rsWu, uoff[1], sw)
#define U2_M SET_M0(mw, G1_U + 2048)
#define U2_L dma16_m0(rsWu, uoff[2], sw)
#define KBODY(WAITV) { \
            const int dlt = (st == 3 * G1_STAGE) ? -3 * G1_STAGE : G1_STAGE; \
            SB(); \
            STEP(fa0, fb0, fa1, fb1, 1, X0, X1, X2) \
            STEP(fa1, fb1, fa0, fb0, 2, X3, G0, U0) \
            STEP(fa0, fb0, fa1, fb1, 3, G1, U1, G2) \
            U2_M; SB(); \
            MM(fa1, fb1, 0, 0); SB(); U2_L; ba[0] += dlt; aa[0] += dlt; SB(); \
            MM(fa1, fb1, 0, 1); SB(); aa[1] += dlt; aa[2] += dlt; \
            kap = kap == 9 ? 1 : kap + 1; \
            { const unsigned nx = (unsigned)(st >= G1_STAGE ? st - G1_STAGE : st + 3 * G1_STAGE); mx = nx + 4096u * w; mw = nx + 3072u * w; } sx = kap * 128; sw = kap * 64 * 3072; \
            SB(); \
            MM(fa1, fb1, 1, 0); SB(); \
            WAIT_L(0); \
            WAITV; \
            BAR(); \
            RDB(fb0[0], 0, 0); RDA(fa0, 0, 0, 0); RDA(fa0, 0, 0, 1); SB(); \
            MM(fa1, fb1, 1, 1); SB(); RDB(fb0[1], 0, 1); RDA(fa0, 0, 1, 0); RDA(fa0, 0, 1, 1); SB(); \
            MM(fa1, fb1, 2, 0); SB(); RDA(fa0, 0, 2, 0); RDA(fa0, 0, 2, 1); ba[1] += dlt; SB(); \
            MM(fa1, fb1, 2, 1); SB(); ba[2] += dlt; ba[3] += dlt; \
            st += dlt; \
            }
#pragma unroll 1
        for (int kt = 0; kt < 9; ++kt) {
            KBODY(if (__builtin_expect(kt == 0, 0)) { if (w < 2) WAIT_V(13); else WAIT_V(18); } else WAIT_V(10))
            if (__builtin_expect(kt == 1, 0)) __builtin_amdgcn_raw_buffer_store_b32(epoch, rsW, tid == 0 ? (unsigned)(OFF_FA + 128 * bid) : OOB, 0, SC1);
            if (__builtin_expect(kt == 7, 0)) {
                mw = DUMMY_W; sw = 0x40000000u;
                sx = has_ch ? 0u : 0x40000000u;
                mx = CV_RAW + 4096u * w;
                rsX = mkrs(p.hk, DM * SEQ * 2);
#pragma unroll
                for (int n = 0; n < 4; ++n) xoff[n] = (unsigned)(chs * SEQ * 2 + n * 1024 + lane * 16);
                dma4_sc1(rsW, favo, FLG_SCR + 256u * w);
            }
        }
        {
            const unsigned flv = lds_ld<unsigned>((unsigned)(FLG_SCR + 256 * w + lane * 4));
            if (__builtin_amdgcn_ballot_w64(flv != epoch) != 0ull) wait_words(rsW, favo, epoch);
            if (has_ch) {
#pragma unroll
                for (int n = 0; n < 4; ++n) dma16_sc1(rsW, (unsigned)(lane * 16), (unsigned)(OFF_KVR + ch * SEQ * 2 + n * 1024), (unsigned)(CV_XR + CV_XRS * w + n * 1024));
            }
#define N_M do {} while (0)
#define N_L do {} while (0)
            SB();
            STEP(fa0, fb0, fa1, fb1, 1, N, N, N)
            STEP(fa1, fb1, fa0, fb0, 2, N, N, N)
            STEP(fa0, fb0, fa1, fb1, 3, N, N, N)
            MM(fa1, fb1, 0, 0); MM(fa1, fb1, 0, 1); MM(fa1, fb1, 1, 0); MM(fa1, fb1, 1, 1); MM(fa1, fb1, 2, 0); MM(fa1, fb1, 2, 1);
        }
#undef MM
#undef STEP
#undef KBODY
    }
    STAMP(p, 3);
    if (tid == 0) { lds_st<unsigned>(LC_B, 0u); lds_st<unsigned>(LC_C, 0u); }
    WAIT_L(0);
    BAR();
    const int rot2 = (3 * (bi & 7) / 2 + 3 * (bi >> 3)) % 12;
    unsigned a2o[12];
#pragma unroll
    for (int s = 0; s < 12; ++s) { const int ks = s + rot2; a2o[s] = (unsigned)(12 * w + (ks >= 12 ? ks - 12 : ks)) * 1024u; }
    const unsigned a2base = (unsigned)(OFF_WDT + dt * 48 * 5 * 1024);
    v4u A2[12][5];
#pragma unroll
    for (int slot = 0; slot < 8; ++slot) A2[slot / 5][slot % 5] = bld16<SC1>(rsW, lane * 16, a2base + a2o[slot / 5] * 5 + (slot % 5) * 1024);
    SB();
#pragma unroll
    for (int fb = 0; fb < 3; ++fb)
#pragma unroll
        for (int mt = 0; mt < 2; ++mt) {
            const f32x16& a = acc[fb][mt];
            v4u o;
#pragma unroll
            for (int j = 0; j < 4; ++j) o[j] = pk2(a[2 * j] * a[8 + 2 * j], a[2 * j + 1] * a[8 + 2 * j + 1]);
            const int Ft = 6 * ft + 3 * wf + fb;
            const int mrow = 128 * mt1 + 64 * wm + 32 * mt + l31;
            const int pos = (lane & 15) + 16 * (2 * (Ft & 1) + hh);
            bst16<SC1>(rsW, (unsigned)(OFF_HF + ((mrow >> 4) * 48 + (Ft >> 1)) * 1024 + pos * 16), 0, o);
        }
    STAMP(p, 4);

    if (has_ch) {
        const unsigned HP = (unsigned)(CV_HP + w * CV_HPS), XR = (unsigned)(CV_XR + w * CV_XRS), RAW = (unsigned)(CV_RAW + w * 4096);
        WAIT_V(14);
#pragma unroll
        for (int ps = 0; ps < 5; ++ps) {
            const int n = 64 * ps + lane;
            v4u c0 = {0, 0, 0, 0}, c1 = {0, 0, 0, 0};
            if (ps < 4) {
                c0 = lds_ld<v4u>(RAW + 16 * (ps == 0 ? max(n - 2, 0) : n - 2));
                c1 = lds_ld<v4u>(RAW + 16 * (ps == 0 ? max(n - 1, 0) : n - 1));
                if (ps == 0) { if (n < 2) c0 = v4u{0, 0, 0, 0}; if (n < 1) c1 = v4u{0, 0, 0, 0}; }
            } else {
                c0 = lds_ld<v4u>(RAW + 16 * (254 + (lane & 1)));
                c1 = lds_ld<v4u>(RAW + 16 * 255);
                if (lane & 1) c1 = v4u{0, 0, 0, 0};
            }
            const unsigned Dw[6] = {c0.x, c0.y, c0.z, c0.w, c1.x, c1.y};
            v4u o0, o1, o2, o3;
#pragma unroll
            for (int q = 0; q < 4; ++q) {
                o0[q] = __builtin_amdgcn_alignbit(Dw[q + 1], Dw[q], 16);
                o1[q] = Dw[q + 1];
                o2[q] = __builtin_amdgcn_alignbit(Dw[q + 2], Dw[q + 1], 16);
                o3[q] = Dw[q + 2];
            }
            if (ps < 4 || lane < 2) {
                lds_st<v4u>(HP + 0 * CV_CS + 16 * n, o0);
                lds_st<v4u>(HP + 1 * CV_CS + 16 * n, o1);
                lds_st<v4u>(HP + 2 * CV_CS + 16 * n, o2);
                lds_st<v4u>(HP + 3 * CV_CS + 16 * n, o3);
            }
        }
        if (lane < 32) lds_st<v4u>(XR + 4096 + 16 * lane, v4u{0, 0, 0, 0});
        asm volatile("" ::: "memory");
        STAMP(p, 5);

        const int r = lane & 15, g = lane >> 4;
        unsigned ap0 = HP + (unsigned)((r & 3) * CV_CS + 2 * ((r & ~3) + 8 * g));
        unsigned ap1 = ap0 + 8;
        PIN_V(ap0); PIN_V(ap1);
        const unsigned bp = XR + (unsigned)(2 * (240 - 16 * r + 8 * g));
        f32x4 cacc[8];
#pragma unroll
        for (int i = 0; i < 8; ++i) cacc[i] = f32x4{0.f, 0.f, 0.f, 0.f};
        v4u CA[2][8], CB[2][8];
        auto cvrd = [&](int set, int s, int i) {
            CB[set][i] = lds_ld<v4u>(bp + 512 * (7 - i) + 64 * s);
            const v2u lo = lds_ld<v2u>(ap0 + 512 * i + 64 * s), hi = lds_ld<v2u>(ap1 + 512 * i + 64 * s);
            CA[set][i] = v4u{lo.x, lo.y, hi.x, hi.y};
        };
#pragma unroll
        for (int i = 0; i < 8; ++i) cvrd(0, 0, i);
#pragma unroll
        for (int s = 0; s < 8; ++s) {
            const int cur = s & 1;
            SB();
#pragma unroll
            for (int gm = 0; gm < 8; ++gm) {
#pragma unroll
                for (int be = 0; be <= gm; ++be) cacc[gm] = mfma16(CA[cur][be], CB[cur][gm - be], cacc[gm]);
                SB();
                if (s < 7) cvrd(cur ^ 1, s + 1, gm);
                { const int slot = 8 + 8 * s + gm; if (slot < 60) A2[slot / 5][slot % 5] = bld16<SC1>(rsW, lane * 16, a2base + a2o[slot / 5] * 5 + (slot % 5) * 1024); }
                SB();
            }
            if (s == 1) {
                WAIT_V(16);
                lds_arrive(LC_B);
            }
        }
        STAMP(p, 6);
        const float Dc = (ch & 1) ? bhi(sdv) : blo(sdv);
#pragma unroll
        for (int gm = 0; gm < 8; ++gm) {
            const int t0 = 256 * gm + 16 * r + 4 * g;
            const v2u kvp = lds_ld<v2u>(XR + 2 * (2044 - t0));
            const float y0 = cacc[gm][0] + bhi(kvp.y) * Dc, y1 = cacc[gm][1] + blo(kvp.y) * Dc, y2 = cacc[gm][2] + bhi(kvp.x) * Dc, y3 = cacc[gm][3] + blo(kvp.x) * Dc;
            lds_st<v2u>(HP + 2 * t0, v2u{pk2(y0, y1), pk2(y2, y3)});
        }
        asm volatile("" ::: "memory");
#pragma unroll
        for (int ps = 0; ps < 4; ++ps) {
            const int n = 64 * ps + lane;
            bst16<SC1>(rsW, (unsigned)(OFF_YT + (ch * SEQ + 8 * n) * 2), 0, lds_ld<v4u>(HP + 16 * n));
        }
    } else {
        if (w == 3) {
            const rsrc_t rsG = mkrs(p.gw, DM * 2);
            const int c16 = lane & 15, row0 = 128 * mt1 + 8 * ft + (lane >> 4);
            v4u gwv[5], xa[5], xb[5];
#pragma unroll
            for (int i = 0; i < 5; ++i) {
                gwv[i] = bld16<0>(rsG, (unsigned)(16 * (c16 + 16 * i)), 0);
                xa[i] = bld16<0>(rsQ, (unsigned)(row0 * DM * 2 + 16 * (c16 + 16 * i)), 0);
                xb[i] = bld16<0>(rsQ, (unsigned)((row0 + 4) * DM * 2 + 16 * (c16 + 16 * i)), 0);
            }
            float s0 = 0.f, s1 = 0.f;
#pragma unroll
            for (int i = 0; i < 5; ++i)
#pragma unroll
                for (int e = 0; e < 4; ++e) { s0 = dot2(xa[i][e], gwv[i][e], s0); s1 = dot2(xb[i][e], gwv[i][e], s1); }
#pragma unroll
            for (int m = 1; m < 16; m <<= 1) { s0 += __shfl_xor(s0, m); s1 += __shfl_xor(s1, m); }
            const float g0 = rb(1.0f / (1.0f + __expf(-rb(s0)))), g1 = rb(1.0f / (1.0f + __expf(-rb(s1))));
            __builtin_amdgcn_raw_buffer_store_b32(__builtin_bit_cast(unsigned, g0), rsW, c16 == 0 ? (unsigned)(OFF_GATE + row0 * 4) : OOB, 0, SC1);
            __builtin_amdgcn_raw_buffer_store_b32(__builtin_bit_cast(unsigned, g1), rsW, c16 == 0 ? (unsigned)(OFF_GATE + (row0 + 4) * 4) : OOB, 0, SC1);
        }
        WAIT_V(0);
        lds_arrive(LC_B);
        lds_wait(LC_B, 4);
        if (w == 3) {
            if (lane == 0) {
                const unsigned old = add_count(p, OFF_CB + 256 * mt1, 1u);
                if (old == 15u) set_flag(p, OFF_RB + 256 * mt1, 0, epoch);
            }
            if (bid == 0) {
#pragma unroll 1
                for (int i = 0; i < 4; ++i) wait_words(rsW, (unsigned)(OFF_FA + 128 * (64 * i + lane)), epoch);
                if (lane == 0) set_flag(p, OFF_EPOCH, 0, epoch);
            }
        }
#pragma unroll
        for (int slot = 8; slot < 60; ++slot) A2[slot / 5][slot % 5] = bld16<SC1>(rsW, lane * 16, a2base + a2o[slot / 5] * 5 + (slot % 5) * 1024);
    }
    STAMP(p, 7);

    wait_ready<4>(p, OFF_RB + 256 * (mt2 >> 1), epoch);
    STAMP(p, 8);
    constexpr int W2 = 8;
    unsigned b2off[4];
#pragma unroll
    for (int n = 0; n < 4; ++n) b2off[n] = (unsigned)(OFF_HF + (4 * mt2 + ((w + n) & 3)) * 48 * 1024);
    v4u B2[W2][4];
#pragma unroll
    for (int s = 0; s < W2; ++s)
#pragma unroll
        for (int n = 0; n < 4; ++n) B2[s][n] = bld16<SC1>(rsW, lane * 16, b2off[n] + a2o[s]);
    SB();
    if (has_ch) {
        WAIT_V(32);
        lds_arrive(LC_C);
    }
    f32x4 acc2[5][4];
#pragma unroll
    for (int a = 0; a < 5; ++a)
#pragma unroll
        for (int n = 0; n < 4; ++n) acc2[a][n] = f32x4{0.f, 0.f, 0.f, 0.f};
    const int t = 64 * mt2 + 16 * w + (lane & 15), g4 = lane >> 4;
    v2u qv[5];
    float gt;
#pragma unroll
    for (int s = 0; s < 12; ++s) {
        v4u cb[4];
#pragma unroll
        for (int n = 0; n < 4; ++n) cb[n] = B2[s % W2][n];
        if (s + W2 < 12) {
#pragma unroll
            for (int n = 0; n < 4; ++n) B2[s % W2][n] = bld16<SC1>(rsW, lane * 16, b2off[n] + a2o[s + W2]);
        }
        if (s == 6) {
#pragma unroll
            for (int a = 0; a < 5; ++a) qv[a] = *(const GAS v2u*)(p.q + (size_t)t * DM + 80 * dt + 16 * a + 4 * g4);
            gt = __builtin_bit_cast(float, __builtin_amdgcn_raw_buffer_load_b32(rsW, (unsigned)(OFF_GATE + t * 4), 0, SC1));
        }
#pragma unroll
        for (int n = 0; n < 4; ++n)
#pragma unroll
            for (int a = 0; a < 5; ++a) acc2[a][n] = mfma16(A2[s][a], cb[n], acc2[a][n]);
        SB();
    }
    STAMP(p, 9);
    lds_wait(LC_B, 4);
#pragma unroll
    for (int a = 0; a < 5; ++a)
#pragma unroll
        for (int j = 1; j < 4; ++j) lds_st<f32x4>((unsigned)(G2_RED + ((((w + j) & 3) * 5 + a) * 3 + (j - 1)) * 1024 + lane * 16), acc2[a][j]);
    if (w == 3) {
        lds_wait(LC_C, (unsigned)nch);
        {
            const int c0 = ch - 3, g0 = c0 / 80, n0 = min(nch, 80 * (g0 + 1) - c0), gi = g0 + lane, ni = lane == 0 ? n0 : nch - n0;
            if (lane < 2 && ni > 0) {
                const unsigned old = add_count(p, OFF_CC + 256 * gi, (unsigned)ni);
                if (old + (unsigned)ni == 80u) set_flag(p, OFF_RC + 256 * gi, 0, epoch);
            }
        }
        wait_ready<4>(p, OFF_RC + 256 * dt, epoch);
        v4u yv[10];
#pragma unroll
        for (int it = 0; it < 10; ++it) {
            const int c = lane + 64 * it;
            yv[it] = bld16<SC1>(rsW, (unsigned)(OFF_YT + ((80 * dt + (c >> 3)) * SEQ + 64 * mt2 + 8 * (c & 7)) * 2), 0);
        }
#pragma unroll
        for (int it = 0; it < 10; ++it) lds_st<v4u>((unsigned)(G2_Y + (lane + 64 * it) * 16), yv[it]);
    }
    WAIT_L(0);
    BAR();
    STAMP(p, 10);
    const float omg = rb(1.0f - gt);
    const int q = (lane >> 2) & 3, pp = lane & 3;
#pragma unroll
    for (int a = 0; a < 5; ++a) {
        f32x4 b = acc2[a][0];
#pragma unroll
        for (int j = 0; j < 3; ++j) b += lds_ld<f32x4>((unsigned)(G2_RED + ((w * 5 + a) * 3 + j) * 1024 + lane * 16));
        const v2u y = tr_read((unsigned)(G2_Y + (16 * a + 4 * g4 + q) * 128 + 32 * w + 8 * pp));
        const int d0 = 80 * dt + 16 * a + 4 * g4;
        const float o0 = gt * (blo(qv[a].x) * blo(y.x)) + omg * b[0], o1 = gt * (bhi(qv[a].x) * bhi(y.x)) + omg * b[1];
        const float o2 = gt * (blo(qv[a].y) * blo(y.y)) + omg * b[2], o3 = gt * (bhi(qv[a].y) * bhi(y.y)) + omg * b[3];
        *(GAS v2u*)(p.out + (size_t)t * DM + d0) = v2u{pk2(o0, o1), pk2(o2, o3)};
    }
    STAMP(p, 11);
}

extern "C" void kernel_launch(void* const* d_in, const int* in_sizes, int n_in, void* d_out, int out_size, void* d_ws, size_t ws_size, hipStream_t stream) {
    Params p;
    p.q = (const u16*)d_in[0]; p.k = (const u16*)d_in[1]; p.v = (const u16*)d_in[2]; p.sk = (const u16*)d_in[3];
    p.hk = (const u16*)d_in[4]; p.sd = (const u16*)d_in[5];
    p.wg = (const u16*)d_in[7]; p.wu = (const u16*)d_in[8]; p.wd = (const u16*)d_in[9]; p.gw = (const u16*)d_in[10];
    p.out = (u16*)d_out; p.ws = (unsigned char*)d_ws;
    (void)hipFuncSetAttribute((const void*)k_fused, hipFuncAttributeMaxDynamicSharedMemorySize, LDS_BYTES);
    k_fused<<<256, 256, LDS_BYTES, stream>>>(p);
}
```

```cpp
#include <hip/hip_runtime.h>
#include <stdint.h>

#define LAS __attribute__((address_space(3)))
#define GAS __attribute__((address_space(1)))
typedef unsigned short u16;
typedef unsigned v4u __attribute__((ext_vector_type(4)));
typedef unsigned v2u __attribute__((ext_vector_type(2)));
typedef __bf16 bf8 __attribute__((ext_vector_type(8)));
typedef __bf16 bf2 __attribute__((ext_vector_type(2)));
typedef short s4 __attribute__((ext_vector_type(4)));
typedef float f32x2 __attribute__((ext_vector_type(2)));
typedef float f32x4 __attribute__((ext_vector_type(4)));
typedef float f32x16 __attribute__((ext_vector_type(16)));
typedef __amdgpu_buffer_rsrc_t rsrc_t;

constexpr int SEQ = 2048, DM = 640, FF = 1536;
constexpr int OFF_EPOCH = 0;
constexpr int OFF_FA = 32768;
constexpr int OFF_CB = 2048;
constexpr int OFF_CC = 8192;
constexpr int OFF_RB = 12288;
constexpr int OFF_RC = 16384;
constexpr int OFF_STAMP = 32 * 1024 * 1024;
constexpr int OFF_KVR = 65536;
constexpr int OFF_WDT = OFF_KVR + DM * SEQ * 2;
constexpr int OFF_HF = OFF_WDT + FF * DM * 2;
constexpr int OFF_YT = OFF_HF + SEQ * FF * 2;
constexpr int OFF_GATE = OFF_YT + DM * SEQ * 2;
constexpr int WS_BYTES = OFF_GATE + SEQ * 4;
constexpr int SC1 = 16;
constexpr unsigned OOB = 0x80000000u;
constexpr unsigned SPIN_MAX = 1u << 16;

struct Params {
    const u16 *q, *k, *v, *sk, *hk, *sd, *wg, *wu, *wd, *gw;
    u16* out;
    unsigned char* ws;
};

__device__ __forceinline__ unsigned pk2(float a, float b) {
    f32x2 x = {a, b};
    return __builtin_bit_cast(unsigned, __builtin_convertvector(x, bf2));
}
__device__ __forceinline__ float blo(unsigned u) { return __builtin_bit_cast(float, u << 16); }
__device__ __forceinline__ float bhi(unsigned u) { return __builtin_bit_cast(float, u & 0xffff0000u); }
__device__ __forceinline__ float rb(float x) { return blo(pk2(x, x)); }
__device__ __forceinline__ f32x4 mfma16(v4u a, v4u b, f32x4 c) {
    return __builtin_amdgcn_mfma_f32_16x16x32_bf16(__builtin_bit_cast(bf8, a), __builtin_bit_cast(bf8, b), c, 0, 0, 0);
}
__device__ __forceinline__ f32x16 mfma32(v4u a, v4u b, f32x16 c) {
    return __builtin_amdgcn_mfma_f32_32x32x16_bf16(__builtin_bit_cast(bf8, a), __builtin_bit_cast(bf8, b), c, 0, 0, 0);
}
__device__ __forceinline__ float dot2(unsigned a, unsigned b, float c) {
    return __builtin_amdgcn_fdot2_f32_bf16(__builtin_bit_cast(bf2, a), __builtin_bit_cast(bf2, b), c, false);
}
template <class T> __device__ __forceinline__ T lds_ld(unsigned a) { return *(LAS T*)(uintptr_t)a; }
template <class T> __device__ __forceinline__ void lds_st(unsigned a, T v) { *(LAS T*)(uintptr_t)a = v; }
__device__ __forceinline__ v2u tr_read(unsigned a) {
    return __builtin_bit_cast(v2u, __builtin_amdgcn_ds_read_tr16_b64_v4i16((LAS s4*)(uintptr_t)a));
}
__device__ __forceinline__ rsrc_t mkrs(const void* p, unsigned bytes) { return __builtin_amdgcn_make_buffer_rsrc((void*)p, 0, bytes, 0x00020000); }
template <int AUX> __device__ __forceinline__ v4u bld16(rsrc_t rs, unsigned voff, unsigned soff) { return __builtin_amdgcn_raw_buffer_load_b128(rs, voff, soff, AUX); }
template <int AUX> __device__ __forceinline__ void bst16(rsrc_t rs, unsigned voff, unsigned soff, v4u v) { __builtin_amdgcn_raw_buffer_store_b128(v, rs, voff, soff, AUX); }
#define WAIT_V(n) asm volatile("s_waitcnt vmcnt(" #n ")" ::: "memory")
#define WAIT_L(n) asm volatile("s_waitcnt lgkmcnt(" #n ")" ::: "memory")
#define BAR() do { asm volatile("" ::: "memory"); __builtin_amdgcn_s_barrier(); asm volatile("" ::: "memory"); } while (0)
#define SB() __builtin_amdgcn_sched_barrier(0)
#define PIN_S(x) asm volatile("" : "+s"(x))
#define PIN_V(x) asm volatile("" : "+v"(x))
#define RLX_AGENT __ATOMIC_RELAXED, __HIP_MEMORY_SCOPE_AGENT
#define STAMP(p, i) do {} while (0)

__device__ __forceinline__ void dma16(rsrc_t rs, unsigned voff, unsigned soff, unsigned lds_addr) {
    asm volatile("s_mov_b32 m0, %0\n\ts_nop 0\n\tbuffer_load_dwordx4 %1, %2, %3 offen lds" ::"s"(lds_addr), "v"(voff), "s"(rs), "s"(soff) : "memory", "m0");
}
__device__ __forceinline__ void dma16_sc1(rsrc_t rs, unsigned voff, unsigned soff, unsigned lds_addr) {
    asm volatile("s_mov_b32 m0, %0\n\ts_nop 0\n\tbuffer_load_dwordx4 %1, %2, %3 offen sc1 lds" ::"s"(lds_addr), "v"(voff), "s"(rs), "s"(soff) : "memory", "m0");
}
#define SET_M0(base, imm) asm volatile("s_add_u32 m0, %0, %1" ::"s"(base), "i"(imm) : "memory", "m0", "scc")
__device__ __forceinline__ void dma16_m0(rsrc_t rs, unsigned voff, unsigned soff) {
    asm volatile("buffer_load_dwordx4 %0, %1, %2 offen lds" ::"v"(voff), "s"(rs), "s"(soff) : "memory");
}
__device__ __forceinline__ void dma16_m0_sc1(rsrc_t rs, unsigned voff, unsigned soff) {
    asm volatile("buffer_load_dwordx4 %0, %1, %2 offen sc1 lds" ::"v"(voff), "s"(rs), "s"(soff) : "memory");
}
__device__ __forceinline__ void aload16(v4u& d, rsrc_t rs, unsigned voff) {
    asm volatile("buffer_load_dwordx4 %0, %1, %2, 0 offen" : "=v"(d) : "v"(voff), "s"(rs) : "memory");
}

__device__ __forceinline__ void wait_words(rsrc_t rsW, unsigned voff, unsigned e) {
    unsigned t0, left = SPIN_MAX;
    asm volatile(
        "1:\n\t"
        "buffer_load_dword %0, %2, %3, 0 offen sc1\n\t"
        "s_waitcnt vmcnt(0)\n\t"
        "v_cmp_ne_u32 vcc, %4, %0\n\t"
        "s_nop 4\n\t"
        "s_cbranch_vccz 2f\n\t"
        "s_sleep 8\n\t"
        "s_sub_u32 %1, %1, 1\n\t"
        "s_cbranch_scc0 1b\n\t"
        "2:"
        : "=&v"(t0), "+s"(left)
        : "v"(voff), "s"(rsW), "s"(e)
        : "memory", "vcc", "scc");
}
__device__ __forceinline__ void dma4_sc1(rsrc_t rs, unsigned voff, unsigned lds_addr) {
    asm volatile("s_mov_b32 m0, %0\n\ts_nop 0\n\tbuffer_load_dword %1, %2, 0 offen sc1 lds" ::"s"(lds_addr), "v"(voff), "s"(rs) : "memory", "m0");
}
__device__ __forceinline__ unsigned add_count(const Params& p, int off, unsigned n) {
    return __hip_atomic_fetch_add((GAS unsigned*)(p.ws + off), n, RLX_AGENT);
}
template <int SLEEP>
__device__ __forceinline__ void wait_ready(const Params& p, int off, unsigned e) {
    GAS unsigned* c = (GAS unsigned*)(p.ws + off);
    for (unsigned it = 0; it < SPIN_MAX; ++it) {
        if ((unsigned)__builtin_amdgcn_readfirstlane(__hip_atomic_load(c, RLX_AGENT)) == e) break;
        __builtin_amdgcn_s_sleep(SLEEP);
    }
    __builtin_amdgcn_fence(__ATOMIC_ACQUIRE, "wavefront");
}
__device__ __forceinline__ void lds_arrive(unsigned off) {
    if ((threadIdx.x & 63) == 0) (void)__hip_atomic_fetch_add((LAS unsigned*)(uintptr_t)off, 1u, __ATOMIC_RELAXED, __HIP_MEMORY_SCOPE_WORKGROUP);
}
__device__ __forceinline__ void lds_wait(unsigned off, unsigned n) {
    for (unsigned it = 0; it < (SPIN_MAX << 4); ++it) {
        if ((unsigned)__builtin_amdgcn_readfirstlane(__hip_atomic_load((LAS unsigned*)(uintptr_t)off, __ATOMIC_RELAXED, __HIP_MEMORY_SCOPE_WORKGROUP)) >= n) break;
        __builtin_amdgcn_s_sleep(1);
    }
    asm volatile("" ::: "memory");
}
__device__ __forceinline__ void set_flag(const Params& p, int off, int idx, unsigned e) {
    __hip_atomic_store((GAS unsigned*)(p.ws + off) + idx, e, RLX_AGENT);
}

constexpr int G1_STAGE = 40960, G1_G = 16384, G1_U = 16384 + 12288;
constexpr int SCR = 3 * G1_STAGE;
constexpr int PR_K = SCR;
constexpr int PR_V = PR_K + 11264;
constexpr int PR_S = PR_V + 10240;
constexpr int PR_D = PR_S + 1024;
constexpr int CV_RAW = SCR;
constexpr int CV_XR = SCR + 16384, CV_XRS = 4608;
constexpr int FLG_SCR = CV_XR + 3 * CV_XRS;
constexpr int DUMMY = PR_D + 10240;
constexpr int LC_B = 163808, LC_C = 163824;
constexpr int CV_CS = 4160, CV_HP = 0, CV_HPS = 4 * CV_CS;
constexpr int G2_Y = 3 * CV_HPS;
constexpr int G2_RED = 61440;
constexpr int LDS_BYTES = 163840;
static_assert(FLG_SCR + 1024 <= DUMMY && DUMMY + 4096 <= LC_B && G2_Y + 80 * 128 <= G2_RED && G2_RED + 60 * 1024 <= SCR, "lds");
constexpr int CODE_BYTES = 24576;

__global__ void __launch_bounds__(256, 1) k_fused(Params p) {
    extern __shared__ __attribute__((aligned(16))) unsigned char smem[];
    const int tid = threadIdx.x, lane = tid & 63, w = __builtin_amdgcn_readfirstlane(tid >> 6);
    const int bid = blockIdx.x;
    const int xcd = bid & 7, bi = bid >> 3, mq = xcd >> 1, half = xcd & 1;
    STAMP(p, 0);
    {
        unsigned long long pc;
        asm volatile("s_getpc_b64 %0" : "=s"(pc));
        const rsrc_t rsC = mkrs((const void*)(pc & ~255ull), CODE_BYTES - 256);
        dma16(rsC, (unsigned)(((bi + 32 * w) << 10) + lane * 16), 0, DUMMY);
    }
    const int nch = bid < 128 ? 3 : 2, ch = (bid < 128 ? 3 * bid : 384 + 2 * (bid - 128)) + w;
    const bool has_ch = w < nch;
    unsigned sdv = *(const __attribute__((address_space(4))) unsigned*)(uintptr_t)(p.sd + (min(ch, DM - 1) & ~1));
    PIN_S(sdv);
    rsrc_t rsW = mkrs(p.ws, WS_BYTES);
    rsrc_t rsQ = mkrs(p.q, SEQ * DM * 2);

    const int ptt = bid & 31, pd8 = bid >> 5;
    auto prep_loads = [&]() {
        const rsrc_t rsK = mkrs(p.k, SEQ * DM * 2), rsV = mkrs(p.v, SEQ * DM * 2), rsS = mkrs(p.sk, DM * 4 * 2), rsD = mkrs(p.wd, FF * DM * 2);
        auto kdma = [&](int i) {
            const int c = 64 * i + lane, row = c / 10, col = c - 10 * row, t = 64 * ptt - 4 + row;
            dma16(rsK, (t >= 0 && c < 680) ? (unsigned)((t * DM + 80 * pd8 + 8 * col) * 2) : OOB, 0, PR_K + 1024 * i);
        };
        auto vdma = [&](int i) {
            const int c = 64 * i + lane, row = c / 10, col = c - 10 * row;
            dma16(rsV, (unsigned)(((64 * ptt + row) * DM + 80 * pd8 + 8 * col) * 2), 0, PR_V + 1024 * i);
        };
        auto ddma = [&](int j, int i) {
            const int g = 2 * bid + j, gdt = g / 48, gks = g - 48 * gdt;
            const int c = 64 * i + lane, row = c / 10, col = c - 10 * row;
            dma16(rsD, g < 384 ? (unsigned)(((32 * gks + row) * DM + 80 * gdt + 8 * col) * 2) : OOB, 0, PR_D + 5120 * j + 1024 * i);
        };
        if (w == 0) {
#pragma unroll
            for (int i = 0; i < 8; ++i) kdma(i);
        } else if (w == 1) {
#pragma unroll
            for (int i = 8; i < 11; ++i) kdma(i);
#pragma unroll
            for (int i = 0; i < 5; ++i) vdma(i);
        } else if (w == 2) {
#pragma unroll
            for (int i = 5; i < 10; ++i) vdma(i);
#pragma unroll
            for (int i = 0; i < 5; ++i) ddma(0, i);
        } else {
            dma16(rsS, lane < 40 ? (unsigned)(80 * pd8 * 8 + 16 * lane) : OOB, 0, PR_S);
#pragma unroll
            for (int i = 0; i < 5; ++i) ddma(1, i);
        }
    };
    auto prep_compute = [&]() {
        if (bid < 24 && tid == 0) set_flag(p, bid < 16 ? OFF_CB + 256 * bid : OFF_CC + 256 * (bid - 16), 0, 0u);
        const int i16 = lane & 15, q = (lane >> 2) & 3, pp = lane & 3;
#pragma unroll
        for (int ps = 0; ps < 3; ++ps) {
            const int quad = min(4 * ps + w, 9), cg = quad >> 1, tgp = 4 * (quad & 1) + (lane >> 4);
            const unsigned ka = (unsigned)(PR_K + (8 * tgp + q) * 160 + (16 * cg + 4 * pp) * 2), va = ka + (PR_V - PR_K);
            const v2u k0 = tr_read(ka), k1 = tr_read(ka + 640), k2 = tr_read(ka + 1280), v0 = tr_read(va), v1 = tr_read(va + 640);
            const v2u tw = lds_ld<v2u>((unsigned)(PR_S + (16 * cg + i16) * 8));
            const unsigned W10 = __builtin_amdgcn_alignbit(tw.x, tw.x, 16), W32 = __builtin_amdgcn_alignbit(tw.y, tw.y, 16);
            const unsigned P[6] = {k0.x, k0.y, k1.x, k1.y, k2.x, k2.y};
            unsigned Q[5];
#pragma unroll
            for (int m = 0; m < 5; ++m) Q[m] = __builtin_amdgcn_alignbit(P[m + 1], P[m], 16);
            const unsigned V[4] = {v0.x, v0.y, v1.x, v1.y};
            float r[8];
#pragma unroll
            for (int m = 0; m < 4; ++m) {
                r[2 * m] = dot2(Q[m], W32, dot2(Q[m + 1], W10, 0.f)) * blo(V[m]);
                r[2 * m + 1] = dot2(P[m + 1], W32, dot2(P[m + 2], W10, 0.f)) * bhi(V[m]);
            }
            bst16<SC1>(rsW, 4 * ps + w < 10 ? (unsigned)(OFF_KVR + ((80 * pd8 + 16 * cg + i16) * SEQ + 2040 - 64 * ptt - 8 * tgp) * 2) : OOB, 0,
                       v4u{pk2(r[7], r[6]), pk2(r[5], r[4]), pk2(r[3], r[2]), pk2(r[1], r[0])});
        }
        if (w >= 2) {
            const int g = 2 * bid + (w - 2), gdt = g / 48, gks = g - 48 * gdt;
            const unsigned T = (unsigned)(PR_D + 5120 * (w - 2));
            const int gq = lane >> 4;
            const int row = 16 * (gq >> 1) + 4 * (gq & 1) + q;
#pragma unroll
            for (int at = 0; at < 5; ++at) {
                const v2u lo = tr_read(T + row * 160 + (16 * at + 4 * pp) * 2);
                const v2u hi = tr_read(T + (row + 8) * 160 + (16 * at + 4 * pp) * 2);
                bst16<SC1>(rsW, g < 384 ? (unsigned)(OFF_WDT + ((gdt * 48 + gks) * 5 + at) * 1024 + lane * 16) : OOB, 0, v4u{lo.x, lo.y, hi.x, hi.y});
            }
        }
    };

    prep_loads();
    STAMP(p, 14);

    const int mt1 = 4 * mq + (bi & 3), ft = 8 * half + (bi >> 2);
    const int wm = w & 1, wf = w >> 1;
    const int rot1 = ((ft & 7) + ((5 * (mt1 & 3)) >> 1)) % 9;
    rsrc_t rsWg = mkrs(p.wg, DM * FF * 2), rsWu = mkrs(p.wu, DM * FF * 2);
    PIN_S(rsW); PIN_S(rsWg); PIN_S(rsWu); PIN_S(rsQ);
    unsigned xoff[4], goff[3], uoff[3];
#pragma unroll
    for (int n = 0; n < 4; ++n) {
        const int row = 32 * w + 8 * n + (lane >> 3);
        xoff[n] = (unsigned)((128 * mt1 + row) * 1280 + (((lane & 7) ^ ((4 * n + (lane >> 4)) & 7)) << 4));
    }
#pragma unroll
    for (int n = 0; n < 3; ++n) {
        const int Lc = 64 * (3 * w + n) + lane, k = Lc / 12, c = Lc - 12 * k;
        goff[n] = (unsigned)(k * 3072 + 192 * ft + 16 * c);
        uoff[n] = (unsigned)(k * 3072 + 192 * ft + 16 * (c ^ 2));
    }
    auto g1_issue = [&](int kt, int stg) {
#pragma unroll
        for (int n = 0; n < 4; ++n) dma16(rsQ, xoff[n], kt * 128, stg + (4 * w + n) * 1024);
#pragma unroll
        for (int n = 0; n < 3; ++n) {
            dma16(rsWg, goff[n], kt * 64 * 3072, stg + G1_G + (3 * w + n) * 1024);
            dma16(rsWu, uoff[n], kt * 64 * 3072, stg + G1_U + (3 * w + n) * 1024);
        }
    };
    g1_issue(0, 0);
    const unsigned epoch = *(const __attribute__((address_space(4))) unsigned*)(uintptr_t)(p.ws + OFF_EPOCH) + 1u;
    f32x16 acc[3][2];
#pragma unroll
    for (int a = 0; a < 3; ++a)
#pragma unroll
        for (int b = 0; b < 2; ++b)
#pragma unroll
            for (int r = 0; r < 16; ++r) acc[a][b][r] = 0.f;
    STAMP(p, 1);
    WAIT_V(10);
    BAR();
    STAMP(p, 15);
    int kap = 1 + rot1;
    g1_issue(kap, G1_STAGE);
    kap = kap == 9 ? 1 : kap + 1;
    prep_compute();
    STAMP(p, 16);

    const int l31 = lane & 31, hh = lane >> 5;
    const int mt2 = 8 * mq + (bi & 7), dt = 4 * half + (bi >> 3);
    unsigned favo;
    {
        const int c0 = ch - w, g0 = c0 / 80;
        int who = 24 * dt + min(max(lane - 32, 0), 23);
        if (lane < 32 && has_ch) who = 32 * (ch / 80) + lane;
        if (lane == 56) who = mt1;
        if (lane == 57) who = 16 + g0;
        if (lane == 58) who = 16 + min(g0 + 1, 7);
        favo = (unsigned)(OFF_FA + 128 * who);
    }
    const int rot2 = (3 * (bi & 7) / 2 + 3 * (bi >> 3)) % 12;
    unsigned a2o[12];
#pragma unroll
    for (int s = 0; s < 12; ++s) { const int ks = s + rot2; a2o[s] = (unsigned)(12 * w + (ks >= 12 ? ks - 12 : ks)) * 1024u; }
    const unsigned a2base = (unsigned)(OFF_WDT + dt * 48 * 5 * 1024);
    {
        const int brow = 64 * wm + l31;
        unsigned ba[4];
#pragma unroll
        for (int kk = 0; kk < 4; ++kk) ba[kk] = (unsigned)(brow * 128 + (((2 * kk + hh) ^ ((brow >> 1) & 7)) << 4));
        const int G = lane >> 4, q = (lane >> 2) & 3, pp = lane & 3;
        unsigned aa[3];
#pragma unroll
        for (int fb = 0; fb < 3; ++fb) {
            const int fbg = 3 * wf + fb;
            aa[fb] = (unsigned)(((G & 1) ? G1_U + (fbg ^ 1) * 32 : G1_G + fbg * 32) + (8 * (G >> 1) + q) * 192 + 8 * pp);
        }
        v4u fb0[2], fb1[2];
        v2u fa0[3][2], fa1[3][2];
#define RDB(d, kk, mt) d = lds_ld<v4u>(ba[kk] + (mt) * 4096)
#define RDA(d, kk, fb, hi) d[fb][hi] = tr_read(aa[fb] + (kk) * 16 * 192 + (hi) * 4 * 192)
#define MM(A, B, fb, mt) acc[fb][mt] = mfma32(v4u{A[fb][0].x, A[fb][0].y, A[fb][1].x, A[fb][1].y}, B[mt], acc[fb][mt])
        if (w < 2) WAIT_V(13); else WAIT_V(18);
        WAIT_L(0);
        BAR();
        STAMP(p, 2);
        RDB(fb0[0], 0, 0); RDA(fa0, 0, 0, 0); RDA(fa0, 0, 0, 1); RDB(fb0[1], 0, 1); RDA(fa0, 0, 1, 0); RDA(fa0, 0, 1, 1); RDA(fa0, 0, 2, 0); RDA(fa0, 0, 2, 1);
        int st = 0;
        unsigned mx = 2 * G1_STAGE + 4096u * w, mg = 2 * G1_STAGE + G1_G + 3072u * w, mu = 2 * G1_STAGE + G1_U + 3072u * w;
        unsigned sx = kap * 128, sw = kap * 64 * 3072;
#define STEP(FA, FB, GA, GB, kn, D0, D1, D2)                                                     \
            MM(FA, FB, 0, 0); SB(); RDB(GB[0], kn, 0); RDA(GA, kn, 0, 0); SB();                 \
            MM(FA, FB, 0, 1); SB(); RDA(GA, kn, 0, 1); RDB(GB[1], kn, 1); SB();                 \
            MM(FA, FB, 1, 0); SB(); RDA(GA, kn, 1, 0); RDA(GA, kn, 1, 1); D0##_M; SB();         \
            MM(FA, FB, 1, 1); SB(); D0##_L; RDA(GA, kn, 2, 0); D1##_M; SB();                    \
            MM(FA, FB, 2, 0); SB(); D1##_L; RDA(GA, kn, 2, 1); D2##_M; SB();                    \
            MM(FA, FB, 2, 1); SB(); D2##_L; SB();
#define X0_M SET_M0(mx, 0)
#define X0_L dma16_m0(rsQ, xoff[0], sx)
#define X1_M SET_M0(mx, 1024)
#define X1_L dma16_m0(rsQ, xoff[1], sx)
#define X2_M SET_M0(mx, 2048)
#define X2_L dma16_m0(rsQ, xoff[2], sx)
#define X3_M SET_M0(mx, 3072)
#define X3_L dma16_m0(rsQ, xoff[3], sx)
#define G0_M SET_M0(mg, 0)
#define G0_L dma16_m0(rsWg, goff[0], sw)
#define G1_M SET_M0(mg, 1024)
#define G1_L dma16_m0(rsWg, goff[1], sw)
#define G2_M SET_M0(mg, 2048)
#define G2_L dma16_m0(rsWg, goff[2], sw)
#define U0_M SET_M0(mu, 0)
#define U0_L dma16_m0(rsWu, uoff[0], sw)
#define U1_M SET_M0(mu, 1024)
#define U1_L dma16_m0(rsWu, uoff[1], sw)
#define U2_M SET_M0(mu, 2048)
#define U2_L dma16_m0(rsWu, uoff[2], sw)
#define KBODY(WAITV) { \
            const int dlt = (st == 2 * G1_STAGE) ? -2 * G1_STAGE : G1_STAGE; \
            SB(); \
            STEP(fa0, fb0, fa1, fb1, 1, X0, X1, X2) \
            STEP(fa1, fb1, fa0, fb0, 2, X3, G0, U0) \
            STEP(fa0, fb0, fa1, fb1, 3, G1, U1, G2) \
            U2_M; SB(); \
            MM(fa1, fb1, 0, 0); SB(); U2_L; ba[0] += dlt; aa[0] += dlt; SB(); \
            MM(fa1, fb1, 0, 1); SB(); aa[1] += dlt; aa[2] += dlt; \
            kap = kap == 9 ? 1 : kap + 1; \
            mx = (unsigned)st + 4096u * w; mg = (unsigned)st + G1_G + 3072u * w; mu = (unsigned)st + G1_U + 3072u * w; sx = kap * 128; sw = kap * 64 * 3072; \
            SB(); \
            MM(fa1, fb1, 1, 0); SB(); \
            WAIT_L(0); \
            WAITV; \
            BAR(); \
            RDB(fb0[0], 0, 0); RDA(fa0, 0, 0, 0); RDA(fa0, 0, 0, 1); SB(); \
            MM(fa1, fb1, 1, 1); SB(); RDB(fb0[1], 0, 1); RDA(fa0, 0, 1, 0); RDA(fa0, 0, 1, 1); SB(); \
            MM(fa1, fb1, 2, 0); SB(); RDA(fa0, 0, 2, 0); RDA(fa0, 0, 2, 1); ba[1] += dlt; SB(); \
            MM(fa1, fb1, 2, 1); SB(); ba[2] += dlt; ba[3] += dlt; \
            st += dlt; \
            }
#pragma unroll 1
        for (int kt = 0; kt < 10; ++kt) {
            KBODY(if (__builtin_expect((0x245 >> kt) & 1, 0)) { if (kt == 0) { if (w < 2) WAIT_V(13); else WAIT_V(18); } else if (kt == 2) WAIT_V(15); else if (kt == 6) WAIT_V(14); else WAIT_V(20); } else WAIT_V(10))
            if (__builtin_expect(kt == 7, 0)) {
                sx = 0x40000000u; mx = DUMMY; mg = DUMMY; mu = DUMMY; sw = a2base;
                rsWg = rsW; rsWu = rsW;
#pragma unroll
                for (int n = 0; n < 3; ++n) { goff[n] = a2o[0] * 5 + (unsigned)(lane * 16 + 2048 * n); uoff[n] = goff[n] + 1024; }
            }
            if (__builtin_expect(kt == 8, 0)) {
#pragma unroll
                for (int n = 0; n < 3; ++n) { goff[n] = (n == 2 ? a2o[2] * 5 : a2o[1] * 5 + 1024 + 2048 * n) + (unsigned)(lane * 16); uoff[n] = (n == 0 ? a2o[1] * 5 + 2048 : n == 1 ? a2o[1] * 5 + 4096 : a2o[2] * 5 + 1024) + (unsigned)(lane * 16); }
                sx = 0x40000000u; mx = DUMMY; mg = DUMMY; mu = DUMMY; sw = a2base;
            }
            if (__builtin_expect((0x132 >> kt) & 1, 0)) {
                if (kt == 1) {
                    __builtin_amdgcn_raw_buffer_store_b32(epoch, rsW, tid == 0 ? (unsigned)(OFF_FA + 128 * bid) : OOB, 0, SC1);
                    const rsrc_t rsH = mkrs(p.hk, DM * SEQ * 2);
#pragma unroll
                    for (int n = 0; n < 4; ++n) dma16(rsH, has_ch ? (unsigned)(ch * SEQ * 2 + n * 1024 + lane * 16) : OOB, 0, CV_RAW + 4096u * w + 1024u * n);
                } else if (kt == 4) dma4_sc1(rsW, favo, FLG_SCR + 256u * w);
                else if (kt == 5) {
                    const unsigned flv = lds_ld<unsigned>((unsigned)(FLG_SCR + 256 * w + lane * 4));
                    if (__builtin_amdgcn_ballot_w64(flv != epoch) != 0ull) wait_words(rsW, favo, epoch);
#pragma unroll
                    for (int n = 0; n < 4; ++n) dma16_sc1(rsW, has_ch ? (unsigned)(OFF_KVR + ch * SEQ * 2 + n * 1024 + lane * 16) : OOB, 0, has_ch ? (unsigned)(CV_XR + CV_XRS * w + n * 1024) : (unsigned)DUMMY);
                } else {
                    if (tid == 0) { lds_st<unsigned>(LC_B, 0u); lds_st<unsigned>(LC_C, 0u); }
                }
            }
        }
#undef MM
#undef STEP
#undef KBODY
    }
    STAMP(p, 3);
    v4u A2[12][5];
#pragma unroll
    for (int fb = 0; fb < 3; ++fb)
#pragma unroll
        for (int mt = 0; mt < 2; ++mt) {
            const f32x16& a = acc[fb][mt];
            v4u o;
#pragma unroll
            for (int j = 0; j < 4; ++j) o[j] = pk2(a[2 * j] * a[8 + 2 * j], a[2 * j + 1] * a[8 + 2 * j + 1]);
            const int Ft = 6 * ft + 3 * wf + fb;
            const int mrow = 128 * mt1 + 64 * wm + 32 * mt + l31;
            const int pos = (lane & 15) + 16 * (2 * (Ft & 1) + hh);
            bst16<SC1>(rsW, (unsigned)(OFF_HF + ((mrow >> 4) * 48 + (Ft >> 1)) * 1024 + pos * 16), 0, o);
        }
    STAMP(p, 4);

    if (has_ch) {
        const unsigned HP = (unsigned)(CV_HP + w * CV_HPS), XR = (unsigned)(CV_XR + w * CV_XRS), RAW = (unsigned)(CV_RAW + w * 4096);
#pragma unroll
        for (int ps = 0; ps < 5; ++ps) {
            const int n = 64 * ps + lane;
            v4u c0 = {0, 0, 0, 0}, c1 = {0, 0, 0, 0};
            if (ps < 4) {
                c0 = lds_ld<v4u>(RAW + 16 * (ps == 0 ? max(n - 2, 0) : n - 2));
                c1 = lds_ld<v4u>(RAW + 16 * (ps == 0 ? max(n - 1, 0) : n - 1));
                if (ps == 0) { if (n < 2) c0 = v4u{0, 0, 0, 0}; if (n < 1) c1 = v4u{0, 0, 0, 0}; }
            } else {
                c0 = lds_ld<v4u>(RAW + 16 * (254 + (lane & 1)));
                c1 = lds_ld<v4u>(RAW + 16 * 255);
                if (lane & 1) c1 = v4u{0, 0, 0, 0};
            }
            const unsigned Dw[6] = {c0.x, c0.y, c0.z, c0.w, c1.x, c1.y};
            v4u o0, o1, o2, o3;
#pragma unroll
            for (int q = 0; q < 4; ++q) {
                o0[q] = __builtin_amdgcn_alignbit(Dw[q + 1], Dw[q], 16);
                o1[q] = Dw[q + 1];
                o2[q] = __builtin_amdgcn_alignbit(Dw[q + 2], Dw[q + 1], 16);
                o3[q] = Dw[q + 2];
            }
            if (ps < 4 || lane < 2) {
                lds_st<v4u>(HP + 0 * CV_CS + 16 * n, o0);
                lds_st<v4u>(HP + 1 * CV_CS + 16 * n, o1);
                lds_st<v4u>(HP + 2 * CV_CS + 16 * n, o2);
                lds_st<v4u>(HP + 3 * CV_CS + 16 * n, o3);
            }
        }
        if (lane < 32) lds_st<v4u>(XR + 4096 + 16 * lane, v4u{0, 0, 0, 0});
        asm volatile("" ::: "memory");
        STAMP(p, 5);

        const int r = lane & 15, g = lane >> 4;
        unsigned ap0 = HP + (unsigned)((r & 3) * CV_CS + 2 * ((r & ~3) + 8 * g));
        unsigned ap1 = ap0 + 8;
        PIN_V(ap0); PIN_V(ap1);
        const unsigned bp = XR + (unsigned)(2 * (240 - 16 * r + 8 * g));
        f32x4 cacc[8];
#pragma unroll
        for (int i = 0; i < 8; ++i) cacc[i] = f32x4{0.f, 0.f, 0.f, 0.f};
        v4u CA[2][8], CB[2][8];
        auto cvrd = [&](int set, int s, int i) {
            CB[set][i] = lds_ld<v4u>(bp + 512 * (7 - i) + 64 * s);
            const v2u lo = lds_ld<v2u>(ap0 + 512 * i + 64 * s), hi = lds_ld<v2u>(ap1 + 512 * i + 64 * s);
            CA[set][i] = v4u{lo.x, lo.y, hi.x, hi.y};
        };
#pragma unroll
        for (int i = 0; i < 8; ++i) cvrd(0, 0, i);
#pragma unroll
        for (int s = 0; s < 8; ++s) {
            const int cur = s & 1;
            SB();
#pragma unroll
            for (int gm = 0; gm < 8; ++gm) {
#pragma unroll
                for (int be = 0; be <= gm; ++be) cacc[gm] = mfma16(CA[cur][be], CB[cur][gm - be], cacc[gm]);
                SB();
                if (s < 7) cvrd(cur ^ 1, s + 1, gm);
                { const int slot = 8 * s + gm; if (slot < 60) A2[slot / 5][slot % 5] = bld16<SC1>(rsW, lane * 16, a2base + a2o[slot / 5] * 5 + (slot % 5) * 1024); }
                SB();
            }
            if (s == 1) {
                WAIT_V(16);
                lds_arrive(LC_B);
            }
        }
        STAMP(p, 6);
        const float Dc = (ch & 1) ? bhi(sdv) : blo(sdv);
#pragma unroll
        for (int gm = 0; gm < 8; ++gm) {
            const int t0 = 256 * gm + 16 * r + 4 * g;
            const v2u kvp = lds_ld<v2u>(XR + 2 * (2044 - t0));
            const float y0 = cacc[gm][0] + bhi(kvp.y) * Dc, y1 = cacc[gm][1] + blo(kvp.y) * Dc, y2 = cacc[gm][2] + bhi(kvp.x) * Dc, y3 = cacc[gm][3] + blo(kvp.x) * Dc;
            lds_st<v2u>(HP + 2 * t0, v2u{pk2(y0, y1), pk2(y2, y3)});
        }
        asm volatile("" ::: "memory");
#pragma unroll
        for (int ps = 0; ps < 4; ++ps) {
            const int n = 64 * ps + lane;
            bst16<SC1>(rsW, (unsigned)(OFF_YT + (ch * SEQ + 8 * n) * 2), 0, lds_ld<v4u>(HP + 16 * n));
        }
    } else {
        if (w == 3) {
            const rsrc_t rsG = mkrs(p.gw, DM * 2);
            const int c16 = lane & 15, row0 = 128 * mt1 + 8 * ft + (lane >> 4);
            v4u gwv[5], xa[5], xb[5];
#pragma unroll
            for (int i = 0; i < 5; ++i) {
                gwv[i] = bld16<0>(rsG, (unsigned)(16 * (c16 + 16 * i)), 0);
                xa[i] = bld16<0>(rsQ, (unsigned)(row0 * DM * 2 + 16 * (c16 + 16 * i)), 0);
                xb[i] = bld16<0>(rsQ, (unsigned)((row0 + 4) * DM * 2 + 16 * (c16 + 16 * i)), 0);
            }
            float s0 = 0.f, s1 = 0.f;
#pragma unroll
            for (int i = 0; i < 5; ++i)
#pragma unroll
                for (int e = 0; e < 4; ++e) { s0 = dot2(xa[i][e], gwv[i][e], s0); s1 = dot2(xb[i][e], gwv[i][e], s1); }
#pragma unroll
            for (int m = 1; m < 16; m <<= 1) { s0 += __shfl_xor(s0, m); s1 += __shfl_xor(s1, m); }
            const float g0 = rb(1.0f / (1.0f + __expf(-rb(s0)))), g1 = rb(1.0f / (1.0f + __expf(-rb(s1))));
            __builtin_amdgcn_raw_buffer_store_b32(__builtin_bit_cast(unsigned, g0), rsW, c16 == 0 ? (unsigned)(OFF_GATE + row0 * 4) : OOB, 0, SC1);
            __builtin_amdgcn_raw_buffer_store_b32(__builtin_bit_cast(unsigned, g1), rsW, c16 == 0 ? (unsigned)(OFF_GATE + (row0 + 4) * 4) : OOB, 0, SC1);
        }
        WAIT_V(0);
        lds_arrive(LC_B);
        lds_wait(LC_B, 4);
        if (w == 3) {
            if (lane == 0) {
                const unsigned old = add_count(p, OFF_CB + 256 * mt1, 1u);
                if (old == 15u) set_flag(p, OFF_RB + 256 * mt1, 0, epoch);
            }
            if (bid == 0) {
#pragma unroll 1
                for (int i = 0; i < 4; ++i) wait_words(rsW, (unsigned)(OFF_FA + 128 * (64 * i + lane)), epoch);
                if (lane == 0) set_flag(p, OFF_EPOCH, 0, epoch);
            }
        }
#pragma unroll
        for (int slot = 0; slot < 60; ++slot) A2[slot / 5][slot % 5] = bld16<SC1>(rsW, lane * 16, a2base + a2o[slot / 5] * 5 + (slot % 5) * 1024);
    }
    STAMP(p, 7);

    wait_ready<4>(p, OFF_RB + 256 * (mt2 >> 1), epoch);
    STAMP(p, 8);
    constexpr int W2 = 8;
    unsigned b2off[4];
#pragma unroll
    for (int n = 0; n < 4; ++n) b2off[n] = (unsigned)(OFF_HF + (4 * mt2 + ((w + n) & 3)) * 48 * 1024);
    v4u B2[W2][4];
#pragma unroll
    for (int s = 0; s < W2; ++s)
#pragma unroll
        for (int n = 0; n < 4; ++n) B2[s][n] = bld16<SC1>(rsW, lane * 16, b2off[n] + a2o[s]);
    SB();
    if (has_ch) {
        WAIT_V(32);
        lds_arrive(LC_C);
    }
    f32x4 acc2[5][4];
#pragma unroll
    for (int a = 0; a < 5; ++a)
#pragma unroll
        for (int n = 0; n < 4; ++n) acc2[a][n] = f32x4{0.f, 0.f, 0.f, 0.f};
    const int t = 64 * mt2 + 16 * w + (lane & 15), g4 = lane >> 4;
    v2u qv[5];
    float gt;
#pragma unroll
    for (int s = 0; s < 12; ++s) {
        v4u cb[4];
#pragma unroll
        for (int n = 0; n < 4; ++n) cb[n] = B2[s % W2][n];
        if (s + W2 < 12) {
#pragma unroll
            for (int n = 0; n < 4; ++n) B2[s % W2][n] = bld16<SC1>(rsW, lane * 16, b2off[n] + a2o[s + W2]);
        }
        if (s == 6) {
#pragma unroll
            for (int a = 0; a < 5; ++a) qv[a] = *(const GAS v2u*)(p.q + (size_t)t * DM + 80 * dt + 16 * a + 4 * g4);
            gt = __builtin_bit_cast(float, __builtin_amdgcn_raw_buffer_load_b32(rsW, (unsigned)(OFF_GATE + t * 4), 0, SC1));
        }
#pragma unroll
        for (int n = 0; n < 4; ++n)
#pragma unroll
            for (int a = 0; a < 5; ++a) acc2[a][n] = mfma16(A2[s][a], cb[n], acc2[a][n]);
        SB();
    }
    STAMP(p, 9);
    lds_wait(LC_B, 4);
#pragma unroll
    for (int a = 0; a < 5; ++a)
#pragma unroll
        for (int j = 1; j < 4; ++j) lds_st<f32x4>((unsigned)(G2_RED + ((((w + j) & 3) * 5 + a) * 3 + (j - 1)) * 1024 + lane * 16), acc2[a][j]);
    if (w == 3) {
        lds_wait(LC_C, (unsigned)nch);
        {
            const int c0 = ch - 3, g0 = c0 / 80, n0 = min(nch, 80 * (g0 + 1) - c0), gi = g0 + lane, ni = lane == 0 ? n0 : nch - n0;
            if (lane < 2 && ni > 0) {
                const unsigned old = add_count(p, OFF_CC + 256 * gi, (unsigned)ni);
                if (old + (unsigned)ni == 80u) set_flag(p, OFF_RC + 256 * gi, 0, epoch);
            }
        }
        wait_ready<4>(p, OFF_RC + 256 * dt, epoch);
        v4u yv[10];
#pragma unroll
        for (int it = 0; it < 10; ++it) {
            const int c = lane + 64 * it;
            yv[it] = bld16<SC1>(rsW, (unsigned)(OFF_YT + ((80 * dt + (c >> 3)) * SEQ + 64 * mt2 + 8 * (c & 7)) * 2), 0);
        }
#pragma unroll
        for (int it = 0; it < 10; ++it) lds_st<v4u>((unsigned)(G2_Y + (lane + 64 * it) * 16), yv[it]);
    }
    WAIT_L(0);
    BAR();
    STAMP(p, 10);
    const float omg = rb(1.0f - gt);
    const int q = (lane >> 2) & 3, pp = lane & 3;
#pragma unroll
    for (int a = 0; a < 5; ++a) {
        f32x4 b = acc2[a][0];
#pragma unroll
        for (int j = 0; j < 3; ++j) b += lds_ld<f32x4>((unsigned)(G2_RED + ((w * 5 + a) * 3 + j) * 1024 + lane * 16));
        const v2u y = tr_read((unsigned)(G2_Y + (16 * a + 4 * g4 + q) * 128 + 32 * w + 8 * pp));
        const int d0 = 80 * dt + 16 * a + 4 * g4;
        const float o0 = gt * (blo(qv[a].x) * blo(y.x)) + omg * b[0], o1 = gt * (bhi(qv[a].x) * bhi(y.x)) + omg * b[1];
        const float o2 = gt * (blo(qv[a].y) * blo(y.y)) + omg * b[2], o3 = gt * (bhi(qv[a].y) * bhi(y.y)) + omg * b[3];
        *(GAS v2u*)(p.out + (size_t)t * DM + d0) = v2u{pk2(o0, o1), pk2(o2, o3)};
    }
    STAMP(p, 11);
}

extern "C" void kernel_launch(void* const* d_in, const int* in_sizes, int n_in, void* d_out, int out_size, void* d_ws, size_t ws_size, hipStream_t stream) {
    Params p;
    p.q = (const u16*)d_in[0]; p.k = (const u16*)d_in[1]; p.v = (const u16*)d_in[2]; p.sk = (const u16*)d_in[3];
    p.hk = (const u16*)d_in[4]; p.sd = (const u16*)d_in[5];
    p.wg = (const u16*)d_in[7]; p.wu = (const u16*)d_in[8]; p.wd = (const u16*)d_in[9]; p.gw = (const u16*)d_in[10];
    p.out = (u16*)d_out; p.ws = (unsigned char*)d_ws;
    (void)hipFuncSetAttribute((const void*)k_fused, hipFuncAttributeMaxDynamicSharedMemorySize, LDS_BYTES);
    k_fused<<<256, 256, LDS_BYTES, stream>>>(p);
}
```

```cpp
#include <hip/hip_runtime.h>
#include <stdint.h>

#define LAS __attribute__((address_space(3)))
#define GAS __attribute__((address_space(1)))
typedef unsigned short u16;
typedef unsigned v4u __attribute__((ext_vector_type(4)));
typedef unsigned v2u __attribute__((ext_vector_type(2)));
typedef __bf16 bf8 __attribute__((ext_vector_type(8)));
typedef __bf16 bf2 __attribute__((ext_vector_type(2)));
typedef short s4 __attribute__((ext_vector_type(4)));
typedef float f32x2 __attribute__((ext_vector_type(2)));
typedef float f32x4 __attribute__((ext_vector_type(4)));
typedef float f32x16 __attribute__((ext_vector_type(16)));
typedef __amdgpu_buffer_rsrc_t rsrc_t;

constexpr int SEQ = 2048, DM = 640, FF = 1536;
constexpr int OFF_EPOCH = 0;
constexpr int OFF_FA = 32768;
constexpr int OFF_CB = 2048;
constexpr int OFF_CC = 8192;
constexpr int OFF_RB = 12288;
constexpr int OFF_RC = 16384;
constexpr int OFF_STAMP = 32 * 1024 * 1024;
constexpr int OFF_KVR = 65536;
constexpr int OFF_WDT = OFF_KVR + DM * SEQ * 2;
constexpr int OFF_HF = OFF_WDT + FF * DM * 2;
constexpr int OFF_YT = OFF_HF + SEQ * FF * 2;
constexpr int OFF_GATE = OFF_YT + DM * SEQ * 2;
constexpr int WS_BYTES = OFF_GATE + SEQ * 4;
constexpr int SC1 = 16;
constexpr unsigned OOB = 0x80000000u;
constexpr unsigned SPIN_MAX = 1u << 16;

struct Params {
    const u16 *q, *k, *v, *sk, *hk, *sd, *wg, *wu, *wd, *gw;
    u16* out;
    unsigned char* ws;
};

__device__ __forceinline__ unsigned pk2(float a, float b) {
    f32x2 x = {a, b};
    return __builtin_bit_cast(unsigned, __builtin_convertvector(x, bf2));
}
__device__ __forceinline__ float blo(unsigned u) { return __builtin_bit_cast(float, u << 16); }
__device__ __forceinline__ float bhi(unsigned u) { return __builtin_bit_cast(float, u & 0xffff0000u); }
__device__ __forceinline__ float rb(float x) { return blo(pk2(x, x)); }
__device__ __forceinline__ f32x4 mfma16(v4u a, v4u b, f32x4 c) {
    return __builtin_amdgcn_mfma_f32_16x16x32_bf16(__builtin_bit_cast(bf8, a), __builtin_bit_cast(bf8, b), c, 0, 0, 0);
}
__device__ __forceinline__ f32x16 mfma32(v4u a, v4u b, f32x16 c) {
    return __builtin_amdgcn_mfma_f32_32x32x16_bf16(__builtin_bit_cast(bf8, a), __builtin_bit_cast(bf8, b), c, 0, 0, 0);
}
__device__ __forceinline__ float dot2(unsigned a, unsigned b, float c) {
    return __builtin_amdgcn_fdot2_f32_bf16(__builtin_bit_cast(bf2, a), __builtin_bit_cast(bf2, b), c, false);
}
template <class T> __device__ __forceinline__ T lds_ld(unsigned a) { return *(LAS T*)(uintptr_t)a; }
template <class T> __device__ __forceinline__ void lds_st(unsigned a, T v) { *(LAS T*)(uintptr_t)a = v; }
__device__ __forceinline__ v2u tr_read(unsigned a) {
    return __builtin_bit_cast(v2u, __builtin_amdgcn_ds_read_tr16_b64_v4i16((LAS s4*)(uintptr_t)a));
}
__device__ __forceinline__ rsrc_t mkrs(const void* p, unsigned bytes) { return __builtin_amdgcn_make_buffer_rsrc((void*)p, 0, bytes, 0x00020000); }
template <int AUX> __device__ __forceinline__ v4u bld16(rsrc_t rs, unsigned voff, unsigned soff) { return __builtin_amdgcn_raw_buffer_load_b128(rs, voff, soff, AUX); }
template <int AUX> __device__ __forceinline__ void bst16(rsrc_t rs, unsigned voff, unsigned soff, v4u v) { __builtin_amdgcn_raw_buffer_store_b128(v, rs, voff, soff, AUX); }
#define WAIT_V(n) asm volatile("s_waitcnt vmcnt(" #n ")" ::: "memory")
#define WAIT_L(n) asm volatile("s_waitcnt lgkmcnt(" #n ")" ::: "memory")
#define BAR() do { asm volatile("" ::: "memory"); __builtin_amdgcn_s_barrier(); asm volatile("" ::: "memory"); } while (0)
#define SB() __builtin_amdgcn_sched_barrier(0)
#define PIN_S(x) asm volatile("" : "+s"(x))
#define PIN_V(x) asm volatile("" : "+v"(x))
#define RLX_AGENT __ATOMIC_RELAXED, __HIP_MEMORY_SCOPE_AGENT
#define STAMP(p, i) do {} while (0)

__device__ __forceinline__ void dma16(rsrc_t rs, unsigned voff, unsigned soff, unsigned lds_addr) {
    asm volatile("s_mov_b32 m0, %0\n\ts_nop 0\n\tbuffer_load_dwordx4 %1, %2, %3 offen lds" ::"s"(lds_addr), "v"(voff), "s"(rs), "s"(soff) : "memory", "m0");
}
__device__ __forceinline__ void dma16_sc1(rsrc_t rs, unsigned voff, unsigned soff, unsigned lds_addr) {
    asm volatile("s_mov_b32 m0, %0\n\ts_nop 0\n\tbuffer_load_dwordx4 %1, %2, %3 offen sc1 lds" ::"s"(lds_addr), "v"(voff), "s"(rs), "s"(soff) : "memory", "m0");
}
#define SET_M0(base, imm) asm volatile("s_add_u32 m0, %0, %1" ::"s"(base), "i"(imm) : "memory", "m0", "scc")
__device__ __forceinline__ void dma16_m0(rsrc_t rs, unsigned voff, unsigned soff) {
    asm volatile("buffer_load_dwordx4 %0, %1, %2 offen lds" ::"v"(voff), "s"(rs), "s"(soff) : "memory");
}
__device__ __forceinline__ void dma16_m0_sc1(rsrc_t rs, unsigned voff, unsigned soff) {
    asm volatile("buffer_load_dwordx4 %0, %1, %2 offen sc1 lds" ::"v"(voff), "s"(rs), "s"(soff) : "memory");
}
__device__ __forceinline__ void aload16(v4u& d, rsrc_t rs, unsigned voff) {
    asm volatile("buffer_load_dwordx4 %0, %1, %2, 0 offen" : "=v"(d) : "v"(voff), "s"(rs) : "memory");
}

__device__ __forceinline__ void wait_words(rsrc_t rsW, unsigned voff, unsigned e) {
    unsigned t0, left = SPIN_MAX;
    asm volatile(
        "1:\n\t"
        "buffer_load_dword %0, %2, %3, 0 offen sc1\n\t"
        "s_waitcnt vmcnt(0)\n\t"
        "v_cmp_ne_u32 vcc, %4, %0\n\t"
        "s_nop 4\n\t"
        "s_cbranch_vccz 2f\n\t"
        "s_sleep 8\n\t"
        "s_sub_u32 %1, %1, 1\n\t"
        "s_cbranch_scc0 1b\n\t"
        "2:"
        : "=&v"(t0), "+s"(left)
        : "v"(voff), "s"(rsW), "s"(e)
        : "memory", "vcc", "scc");
}
__device__ __forceinline__ void dma4(rsrc_t rs, unsigned voff, unsigned lds_addr) {
    asm volatile("s_mov_b32 m0, %0\n\ts_nop 0\n\tbuffer_load_dword %1, %2, 0 offen lds" ::"s"(lds_addr), "v"(voff), "s"(rs) : "memory", "m0");
}
__device__ __forceinline__ void dma4_sc1(rsrc_t rs, unsigned voff, unsigned lds_addr) {
    asm volatile("s_mov_b32 m0, %0\n\ts_nop 0\n\tbuffer_load_dword %1, %2, 0 offen sc1 lds" ::"s"(lds_addr), "v"(voff), "s"(rs) : "memory", "m0");
}
__device__ __forceinline__ unsigned add_count(const Params& p, int off, unsigned n) {
    return __hip_atomic_fetch_add((GAS unsigned*)(p.ws + off), n, RLX_AGENT);
}
template <int SLEEP>
__device__ __forceinline__ void wait_ready(const Params& p, int off, unsigned e) {
    GAS unsigned* c = (GAS unsigned*)(p.ws + off);
    for (unsigned it = 0; it < SPIN_MAX; ++it) {
        if ((unsigned)__builtin_amdgcn_readfirstlane(__hip_atomic_load(c, RLX_AGENT)) == e) break;
        __builtin_amdgcn_s_sleep(SLEEP);
    }
    __builtin_amdgcn_fence(__ATOMIC_ACQUIRE, "wavefront");
}
__device__ __forceinline__ void lds_arrive(unsigned off) {
    if ((threadIdx.x & 63) == 0) (void)__hip_atomic_fetch_add((LAS unsigned*)(uintptr_t)off, 1u, __ATOMIC_RELAXED, __HIP_MEMORY_SCOPE_WORKGROUP);
}
__device__ __forceinline__ void lds_wait(unsigned off, unsigned n) {
    for (unsigned it = 0; it < (SPIN_MAX << 4); ++it) {
        if ((unsigned)__builtin_amdgcn_readfirstlane(__hip_atomic_load((LAS unsigned*)(uintptr_t)off, __ATOMIC_RELAXED, __HIP_MEMORY_SCOPE_WORKGROUP)) >= n) break;
        __builtin_amdgcn_s_sleep(1);
    }
    asm volatile("" ::: "memory");
}
__device__ __forceinline__ void set_flag(const Params& p, int off, int idx, unsigned e) {
    __hip_atomic_store((GAS unsigned*)(p.ws + off) + idx, e, RLX_AGENT);
}

constexpr int G1_STAGE = 40960, G1_G = 16384, G1_U = 16384 + 12288;
constexpr int SCR = 3 * G1_STAGE;
constexpr int PR_K = SCR;
constexpr int PR_V = PR_K + 11264;
constexpr int PR_S = PR_V + 10240;
constexpr int PR_D = PR_S + 1024;
constexpr int CV_RAW = SCR;
constexpr int CV_XR = SCR + 16384, CV_XRS = 4608;
constexpr int FLG_SCR = CV_XR + 3 * CV_XRS;
constexpr int SD_SCR = FLG_SCR + 1024;
constexpr int DUMMY = PR_D + 10240;
constexpr int LC_B = 163808, LC_C = 163824;
constexpr int CV_CS = 4160, CV_HP = 0, CV_HPS = 4 * CV_CS;
constexpr int G2_Y = 3 * CV_HPS;
constexpr int G2_RED = 61440;
constexpr int LDS_BYTES = 163840;
static_assert(SD_SCR + 1024 <= DUMMY && DUMMY + 4096 <= LC_B && G2_Y + 80 * 128 <= G2_RED && G2_RED + 60 * 1024 <= SCR, "lds");
constexpr int CODE_BYTES = 24576;

__global__ void __launch_bounds__(256, 1) k_fused(Params p) {
    extern __shared__ __attribute__((aligned(16))) unsigned char smem[];
    const int tid = threadIdx.x, lane = tid & 63, w = __builtin_amdgcn_readfirstlane(tid >> 6);
    const int bid = blockIdx.x;
    const int xcd = bid & 7, bi = bid >> 3, mq = xcd >> 1, half = xcd & 1;
    STAMP(p, 0);
    {
        unsigned long long pc;
        asm volatile("s_getpc_b64 %0" : "=s"(pc));
        const rsrc_t rsC = mkrs((const void*)(pc & ~255ull), CODE_BYTES - 256);
        dma16(rsC, (unsigned)(((bi + 32 * w) << 10) + lane * 16), 0, DUMMY);
    }
    const int nch = bid < 128 ? 3 : 2, ch = (bid < 128 ? 3 * bid : 384 + 2 * (bid - 128)) + w;
    const bool has_ch = w < nch;
    asm volatile("" ::"s"(p.q), "s"(p.k), "s"(p.v), "s"(p.sk), "s"(p.hk), "s"(p.sd), "s"(p.wg), "s"(p.wu), "s"(p.wd), "s"(p.gw), "s"(p.out), "s"(p.ws));
    rsrc_t rsW = mkrs(p.ws, WS_BYTES);
    rsrc_t rsQ = mkrs(p.q, SEQ * DM * 2);

    const int ptt = bid & 31, pd8 = bid >> 5;
    auto prep_loads = [&]() {
        const rsrc_t rsK = mkrs(p.k, SEQ * DM * 2), rsV = mkrs(p.v, SEQ * DM * 2), rsS = mkrs(p.sk, DM * 4 * 2), rsD = mkrs(p.wd, FF * DM * 2);
        auto kdma = [&](int i) {
            const int c = 64 * i + lane, row = c / 10, col = c - 10 * row, t = 64 * ptt - 4 + row;
            dma16(rsK, (t >= 0 && c < 680) ? (unsigned)((t * DM + 80 * pd8 + 8 * col) * 2) : OOB, 0, PR_K + 1024 * i);
        };
        auto vdma = [&](int i) {
            const int c = 64 * i + lane, row = c / 10, col = c - 10 * row;
            dma16(rsV, (unsigned)(((64 * ptt + row) * DM + 80 * pd8 + 8 * col) * 2), 0, PR_V + 1024 * i);
        };
        auto ddma = [&](int j, int i) {
            const int g = 2 * bid + j, gdt = g / 48, gks = g - 48 * gdt;
            const int c = 64 * i + lane, row = c / 10, col = c - 10 * row;
            dma16(rsD, g < 384 ? (unsigned)(((32 * gks + row) * DM + 80 * gdt + 8 * col) * 2) : OOB, 0, PR_D + 5120 * j + 1024 * i);
        };
        if (w == 0) {
#pragma unroll
            for (int i = 0; i < 8; ++i) kdma(i);
        } else if (w == 1) {
#pragma unroll
            for (int i = 8; i < 11; ++i) kdma(i);
#pragma unroll
            for (int i = 0; i < 5; ++i) vdma(i);
        } else if (w == 2) {
#pragma unroll
            for (int i = 5; i < 10; ++i) vdma(i);
#pragma unroll
            for (int i = 0; i < 5; ++i) ddma(0, i);
        } else {
            dma16(rsS, lane < 40 ? (unsigned)(80 * pd8 * 8 + 16 * lane) : OOB, 0, PR_S);
#pragma unroll
            for (int i = 0; i < 5; ++i) ddma(1, i);
        }
    };
    auto prep_compute = [&]() {
        if (bid < 24 && tid == 0) set_flag(p, bid < 16 ? OFF_CB + 256 * bid : OFF_CC + 256 * (bid - 16), 0, 0u);
        const int i16 = lane & 15, q = (lane >> 2) & 3, pp = lane & 3;
#pragma unroll
        for (int ps = 0; ps < 3; ++ps) {
            const int quad = min(4 * ps + w, 9), cg = quad >> 1, tgp = 4 * (quad & 1) + (lane >> 4);
            const unsigned ka = (unsigned)(PR_K + (8 * tgp + q) * 160 + (16 * cg + 4 * pp) * 2), va = ka + (PR_V - PR_K);
            const v2u k0 = tr_read(ka), k1 = tr_read(ka + 640), k2 = tr_read(ka + 1280), v0 = tr_read(va), v1 = tr_read(va + 640);
            const v2u tw = lds_ld<v2u>((unsigned)(PR_S + (16 * cg + i16) * 8));
            const unsigned W10 = __builtin_amdgcn_alignbit(tw.x, tw.x, 16), W32 = __builtin_amdgcn_alignbit(tw.y, tw.y, 16);
            const unsigned P[6] = {k0.x, k0.y, k1.x, k1.y, k2.x, k2.y};
            unsigned Q[5];
#pragma unroll
            for (int m = 0; m < 5; ++m) Q[m] = __builtin_amdgcn_alignbit(P[m + 1], P[m], 16);
            const unsigned V[4] = {v0.x, v0.y, v1.x, v1.y};
            float r[8];
#pragma unroll
            for (int m = 0; m < 4; ++m) {
                r[2 * m] = dot2(Q[m], W32, dot2(Q[m + 1], W10, 0.f)) * blo(V[m]);
                r[2 * m + 1] = dot2(P[m + 1], W32, dot2(P[m + 2], W10, 0.f)) * bhi(V[m]);
            }
            bst16<SC1>(rsW, 4 * ps + w < 10 ? (unsigned)(OFF_KVR + ((80 * pd8 + 16 * cg + i16) * SEQ + 2040 - 64 * ptt - 8 * tgp) * 2) : OOB, 0,
                       v4u{pk2(r[7], r[6]), pk2(r[5], r[4]), pk2(r[3], r[2]), pk2(r[1], r[0])});
        }
        if (w >= 2) {
            const int g = 2 * bid + (w - 2), gdt = g / 48, gks = g - 48 * gdt;
            const unsigned T = (unsigned)(PR_D + 5120 * (w - 2));
            const int gq = lane >> 4;
            const int row = 16 * (gq >> 1) + 4 * (gq & 1) + q;
#pragma unroll
            for (int at = 0; at < 5; ++at) {
                const v2u lo = tr_read(T + row * 160 + (16 * at + 4 * pp) * 2);
                const v2u hi = tr_read(T + (row + 8) * 160 + (16 * at + 4 * pp) * 2);
                bst16<SC1>(rsW, g < 384 ? (unsigned)(OFF_WDT + ((gdt * 48 + gks) * 5 + at) * 1024 + lane * 16) : OOB, 0, v4u{lo.x, lo.y, hi.x, hi.y});
            }
        }
    };

    prep_loads();
    STAMP(p, 14);

    const int mt1 = 4 * mq + (bi & 3), ft = 8 * half + (bi >> 2);
    const int wm = w & 1, wf = w >> 1;
    const int rot1 = ((ft & 7) + ((5 * (mt1 & 3)) >> 1)) % 9;
    rsrc_t rsWg = mkrs(p.wg, DM * FF * 2), rsWu = mkrs(p.wu, DM * FF * 2);
    PIN_S(rsW); PIN_S(rsWg); PIN_S(rsWu); PIN_S(rsQ);
    unsigned xoff[4], goff[3], uoff[3];
#pragma unroll
    for (int n = 0; n < 4; ++n) {
        const int row = 32 * w + 8 * n + (lane >> 3);
        xoff[n] = (unsigned)((128 * mt1 + row) * 1280 + (((lane & 7) ^ ((4 * n + (lane >> 4)) & 7)) << 4));
    }
#pragma unroll
    for (int n = 0; n < 3; ++n) {
        const int Lc = 64 * (3 * w + n) + lane, k = Lc / 12, c = Lc - 12 * k;
        goff[n] = (unsigned)(k * 3072 + 192 * ft + 16 * c);
        uoff[n] = (unsigned)(k * 3072 + 192 * ft + 16 * (c ^ 2));
    }
    auto g1_issue = [&](int kt, int stg) {
#pragma unroll
        for (int n = 0; n < 4; ++n) dma16(rsQ, xoff[n], kt * 128, stg + (4 * w + n) * 1024);
#pragma unroll
        for (int n = 0; n < 3; ++n) {
            dma16(rsWg, goff[n], kt * 64 * 3072, stg + G1_G + (3 * w + n) * 1024);
            dma16(rsWu, uoff[n], kt * 64 * 3072, stg + G1_U + (3 * w + n) * 1024);
        }
    };
    g1_issue(0, 0);
    const unsigned epoch = *(const __attribute__((address_space(4))) unsigned*)(uintptr_t)(p.ws + OFF_EPOCH) + 1u;
    f32x16 acc[3][2];
#pragma unroll
    for (int a = 0; a < 3; ++a)
#pragma unroll
        for (int b = 0; b < 2; ++b)
#pragma unroll
            for (int r = 0; r < 16; ++r) acc[a][b][r] = 0.f;
    STAMP(p, 1);
    WAIT_V(10);
    BAR();
    STAMP(p, 15);
    int kap = 1 + rot1;
    g1_issue(kap, G1_STAGE);
    kap = kap == 9 ? 1 : kap + 1;
    prep_compute();
    STAMP(p, 16);

    const int l31 = lane & 31, hh = lane >> 5;
    const int mt2 = 8 * mq + (bi & 7), dt = 4 * half + (bi >> 3);
    unsigned favo;
    {
        const int c0 = ch - w, g0 = c0 / 80;
        int who = 24 * dt + min(max(lane - 32, 0), 23);
        if (lane < 32 && has_ch) who = 32 * (ch / 80) + lane;
        if (lane == 56) who = mt1;
        if (lane == 57) who = 16 + g0;
        if (lane == 58) who = 16 + min(g0 + 1, 7);
        favo = (unsigned)(OFF_FA + 128 * who);
    }
    const int rot2 = (3 * (bi & 7) / 2 + 3 * (bi >> 3)) % 12;
    unsigned a2o[12];
#pragma unroll
    for (int s = 0; s < 12; ++s) { const int ks = s + rot2; a2o[s] = (unsigned)(12 * w + (ks >= 12 ? ks - 12 : ks)) * 1024u; }
    const unsigned a2base = (unsigned)(OFF_WDT + dt * 48 * 5 * 1024);
    {
        const int brow = 64 * wm + l31;
        unsigned ba[4];
#pragma unroll
        for (int kk = 0; kk < 4; ++kk) ba[kk] = (unsigned)(brow * 128 + (((2 * kk + hh) ^ ((brow >> 1) & 7)) << 4));
        const int G = lane >> 4, q = (lane >> 2) & 3, pp = lane & 3;
        unsigned aa[3];
#pragma unroll
        for (int fb = 0; fb < 3; ++fb) {
            const int fbg = 3 * wf + fb;
            aa[fb] = (unsigned)(((G & 1) ? G1_U + (fbg ^ 1) * 32 : G1_G + fbg * 32) + (8 * (G >> 1) + q) * 192 + 8 * pp);
        }
        v4u fb0[2], fb1[2];
        v2u fa0[3][2], fa1[3][2];
#define RDB(d, kk, mt) d = lds_ld<v4u>(ba[kk] + (mt) * 4096)
#define RDA(d, kk, fb, hi) d[fb][hi] = tr_read(aa[fb] + (kk) * 16 * 192 + (hi) * 4 * 192)
#define MM(A, B, fb, mt) acc[fb][mt] = mfma32(v4u{A[fb][0].x, A[fb][0].y, A[fb][1].x, A[fb][1].y}, B[mt], acc[fb][mt])
        if (w < 2) WAIT_V(13); else WAIT_V(18);
        WAIT_L(0);
        BAR();
        STAMP(p, 2);
        RDB(fb0[0], 0, 0); RDA(fa0, 0, 0, 0); RDA(fa0, 0, 0, 1); RDB(fb0[1], 0, 1); RDA(fa0, 0, 1, 0); RDA(fa0, 0, 1, 1); RDA(fa0, 0, 2, 0); RDA(fa0, 0, 2, 1);
        int st = 0;
        unsigned mx = 2 * G1_STAGE + 4096u * w, mg = 2 * G1_STAGE + G1_G + 3072u * w, mu = 2 * G1_STAGE + G1_U + 3072u * w;
        unsigned sx = kap * 128, sw = kap * 64 * 3072;
#define STEP(FA, FB, GA, GB, kn, D0, D1, D2)                                                     \
            MM(FA, FB, 0, 0); SB(); RDB(GB[0], kn, 0); RDA(GA, kn, 0, 0); SB();                 \
            MM(FA, FB, 0, 1); SB(); RDA(GA, kn, 0, 1); RDB(GB[1], kn, 1); SB();                 \
            MM(FA, FB, 1, 0); SB(); RDA(GA, kn, 1, 0); RDA(GA, kn, 1, 1); D0##_M; SB();         \
            MM(FA, FB, 1, 1); SB(); D0##_L; RDA(GA, kn, 2, 0); D1##_M; SB();                    \
            MM(FA, FB, 2, 0); SB(); D1##_L; RDA(GA, kn, 2, 1); D2##_M; SB();                    \
            MM(FA, FB, 2, 1); SB(); D2##_L; SB();
#define X0_M SET_M0(mx, 0)
#define X0_L dma16_m0(rsQ, xoff[0], sx)
#define X1_M SET_M0(mx, 1024)
#define X1_L dma16_m0(rsQ, xoff[1], sx)
#define X2_M SET_M0(mx, 2048)
#define X2_L dma16_m0(rsQ, xoff[2], sx)
#define X3_M SET_M0(mx, 3072)
#define X3_L dma16_m0(rsQ, xoff[3], sx)
#define G0_M SET_M0(mg, 0)
#define G0_L dma16_m0(rsWg, goff[0], sw)
#define G1_M SET_M0(mg, 1024)
#define G1_L dma16_m0(rsWg, goff[1], sw)
#define G2_M SET_M0(mg, 2048)
#define G2_L dma16_m0(rsWg, goff[2], sw)
#define U0_M SET_M0(mu, 0)
#define U0_L dma16_m0(rsWu, uoff[0], sw)
#define U1_M SET_M0(mu, 1024)
#define U1_L dma16_m0(rsWu, uoff[1], sw)
#define U2_M SET_M0(mu, 2048)
#define U2_L dma16_m0(rsWu, uoff[2], sw)
#define KBODY(WAITV) { \
            const int dlt = (st == 2 * G1_STAGE) ? -2 * G1_STAGE : G1_STAGE; \
            SB(); \
            STEP(fa0, fb0, fa1, fb1, 1, X0, X1, X2) \
            STEP(fa1, fb1, fa0, fb0, 2, X3, G0, U0) \
            STEP(fa0, fb0, fa1, fb1, 3, G1, U1, G2) \
            U2_M; SB(); \
            MM(fa1, fb1, 0, 0); SB(); U2_L; ba[0] += dlt; aa[0] += dlt; SB(); \
            MM(fa1, fb1, 0, 1); SB(); aa[1] += dlt; aa[2] += dlt; \
            kap = kap == 9 ? 1 : kap + 1; \
            mx = (unsigned)st + 4096u * w; mg = (unsigned)st + G1_G + 3072u * w; mu = (unsigned)st + G1_U + 3072u * w; sx = kap * 128; sw = kap * 64 * 3072; \
            SB(); \
            MM(fa1, fb1, 1, 0); SB(); \
            WAIT_L(0); \
            WAITV; \
            BAR(); \
            RDB(fb0[0], 0, 0); RDA(fa0, 0, 0, 0); RDA(fa0, 0, 0, 1); SB(); \
            MM(fa1, fb1, 1, 1); SB(); RDB(fb0[1], 0, 1); RDA(fa0, 0, 1, 0); RDA(fa0, 0, 1, 1); SB(); \
            MM(fa1, fb1, 2, 0); SB(); RDA(fa0, 0, 2, 0); RDA(fa0, 0, 2, 1); ba[1] += dlt; SB(); \
            MM(fa1, fb1, 2, 1); SB(); ba[2] += dlt; ba[3] += dlt; \
            st += dlt; \
            }
#pragma unroll 1
        for (int kt = 0; kt < 10; ++kt) {
            KBODY(if (__builtin_expect((0x245 >> kt) & 1, 0)) { if (kt == 0) { if (w < 2) WAIT_V(13); else WAIT_V(18); } else if (kt == 2) WAIT_V(16); else if (kt == 6) WAIT_V(14); else WAIT_V(20); } else WAIT_V(10))
            if (__builtin_expect(kt == 7, 0)) {
                sx = 0x40000000u; mx = DUMMY; mg = DUMMY; mu = DUMMY; sw = a2base;
                rsWg = rsW; rsWu = rsW;
#pragma unroll
                for (int n = 0; n < 3; ++n) { goff[n] = a2o[0] * 5 + (unsigned)(lane * 16 + 2048 * n); uoff[n] = goff[n] + 1024; }
            }
            if (__builtin_expect(kt == 8, 0)) {
#pragma unroll
                for (int n = 0; n < 3; ++n) { goff[n] = (n == 2 ? a2o[2] * 5 : a2o[1] * 5 + 1024 + 2048 * n) + (unsigned)(lane * 16); uoff[n] = (n == 0 ? a2o[1] * 5 + 2048 : n == 1 ? a2o[1] * 5 + 4096 : a2o[2] * 5 + 1024) + (unsigned)(lane * 16); }
                sx = 0x40000000u; mx = DUMMY; mg = DUMMY; mu = DUMMY; sw = a2base;
            }
            if (__builtin_expect((0x132 >> kt) & 1, 0)) {
                if (kt == 1) {
                    __builtin_amdgcn_raw_buffer_store_b32(epoch, rsW, tid == 0 ? (unsigned)(OFF_FA + 128 * bid) : OOB, 0, SC1);
                    const rsrc_t rsH = mkrs(p.hk, DM * SEQ * 2);
#pragma unroll
                    for (int n = 0; n < 4; ++n) dma16(rsH, has_ch ? (unsigned)(ch * SEQ * 2 + n * 1024 + lane * 16) : OOB, 0, CV_RAW + 4096u * w + 1024u * n);
                    dma4(mkrs(p.sd, DM * 2), has_ch ? (unsigned)((ch & ~1) * 2) : OOB, SD_SCR + 256u * w);
                } else if (kt == 4) dma4_sc1(rsW, favo, FLG_SCR + 256u * w);
                else if (kt == 5) {
                    const unsigned flv = lds_ld<unsigned>((unsigned)(FLG_SCR + 256 * w + lane * 4));
                    if (__builtin_amdgcn_ballot_w64(flv != epoch) != 0ull) wait_words(rsW, favo, epoch);
#pragma unroll
                    for (int n = 0; n < 4; ++n) dma16_sc1(rsW, has_ch ? (unsigned)(OFF_KVR + ch * SEQ * 2 + n * 1024 + lane * 16) : OOB, 0, has_ch ? (unsigned)(CV_XR + CV_XRS * w + n * 1024) : (unsigned)DUMMY);
                } else {
                    if (tid == 0) { lds_st<unsigned>(LC_B, 0u); lds_st<unsigned>(LC_C, 0u); }
                }
            }
        }
#undef MM
#undef STEP
#undef KBODY
    }
    STAMP(p, 3);
    v4u A2[12][5];
    unsigned a2s[12], a2v0 = (unsigned)(lane * 16), a2v4 = a2v0 + 4096;
    PIN_V(a2v4);
#pragma unroll
    for (int s = 0; s < 12; ++s) a2s[s] = a2base + a2o[s] * 5;
#pragma unroll
    for (int fb = 0; fb < 3; ++fb)
#pragma unroll
        for (int mt = 0; mt < 2; ++mt) {
            const f32x16& a = acc[fb][mt];
            v4u o;
#pragma unroll
            for (int j = 0; j < 4; ++j) o[j] = pk2(a[2 * j] * a[8 + 2 * j], a[2 * j + 1] * a[8 + 2 * j + 1]);
            const int Ft = 6 * ft + 3 * wf + fb;
            const int mrow = 128 * mt1 + 64 * wm + 32 * mt + l31;
            const int pos = (lane & 15) + 16 * (2 * (Ft & 1) + hh);
            bst16<SC1>(rsW, (unsigned)(OFF_HF + ((mrow >> 4) * 48 + (Ft >> 1)) * 1024 + pos * 16), 0, o);
        }
    STAMP(p, 4);

    if (has_ch) {
        const unsigned HP = (unsigned)(CV_HP + w * CV_HPS), XR = (unsigned)(CV_XR + w * CV_XRS), RAW = (unsigned)(CV_RAW + w * 4096);
        v4u c0[5], c1[5];
#pragma unroll
        for (int ps = 0; ps < 5; ++ps) {
            const int n = 64 * ps + lane;
            c0[ps] = lds_ld<v4u>(RAW + 16 * (ps == 0 ? max(n - 2, 0) : ps < 4 ? n - 2 : 254 + (lane & 1)));
            c1[ps] = lds_ld<v4u>(RAW + 16 * (ps == 0 ? max(n - 1, 0) : ps < 4 ? n - 1 : 255));
        }
        if (lane < 2) c0[0] = v4u{0, 0, 0, 0};
        if (lane < 1) c1[0] = v4u{0, 0, 0, 0};
        if (lane & 1) c1[4] = v4u{0, 0, 0, 0};
#pragma unroll
        for (int ps = 0; ps < 5; ++ps) {
            const int n = 64 * ps + lane;
            const unsigned Dw[6] = {c0[ps].x, c0[ps].y, c0[ps].z, c0[ps].w, c1[ps].x, c1[ps].y};
            v4u o0, o1, o2, o3;
#pragma unroll
            for (int q = 0; q < 4; ++q) {
                o0[q] = __builtin_amdgcn_alignbit(Dw[q + 1], Dw[q], 16);
                o1[q] = Dw[q + 1];
                o2[q] = __builtin_amdgcn_alignbit(Dw[q + 2], Dw[q + 1], 16);
                o3[q] = Dw[q + 2];
            }
            if (ps < 4 || lane < 2) {
                lds_st<v4u>(HP + 0 * CV_CS + 16 * n, o0);
                lds_st<v4u>(HP + 1 * CV_CS + 16 * n, o1);
                lds_st<v4u>(HP + 2 * CV_CS + 16 * n, o2);
                lds_st<v4u>(HP + 3 * CV_CS + 16 * n, o3);
            }
        }
        if (lane < 32) lds_st<v4u>(XR + 4096 + 16 * lane, v4u{0, 0, 0, 0});
        asm volatile("" ::: "memory");
        STAMP(p, 5);

        const int r = lane & 15, g = lane >> 4;
        unsigned ap0 = HP + (unsigned)((r & 3) * CV_CS + 2 * ((r & ~3) + 8 * g));
        unsigned ap1 = ap0 + 8;
        PIN_V(ap0); PIN_V(ap1);
        unsigned bp = XR + (unsigned)(2 * (240 - 16 * r + 8 * g));
        PIN_V(bp);
        f32x4 cacc[8];
#pragma unroll
        for (int i = 0; i < 8; ++i) cacc[i] = f32x4{0.f, 0.f, 0.f, 0.f};
        v4u CA[2][8], CB[2][8];
        auto cvrd = [&](int set, int s, int i) {
            CB[set][i] = lds_ld<v4u>(bp + 512 * (7 - i) + 64 * s);
            const v2u lo = lds_ld<v2u>(ap0 + 512 * i + 64 * s), hi = lds_ld<v2u>(ap1 + 512 * i + 64 * s);
            CA[set][i] = v4u{lo.x, lo.y, hi.x, hi.y};
        };
#pragma unroll
        for (int i = 0; i < 8; ++i) cvrd(0, 0, i);
#pragma unroll
        for (int s = 0; s < 8; ++s) {
            const int cur = s & 1;
            SB();
#pragma unroll
            for (int gm = 0; gm < 8; ++gm) {
#pragma unroll
                for (int be = 0; be <= gm; ++be) cacc[gm] = mfma16(CA[cur][be], CB[cur][gm - be], cacc[gm]);
                SB();
                if (s < 7) cvrd(cur ^ 1, s + 1, gm);
                { const int slot = 8 * s + gm; if (slot < 60) A2[slot / 5][slot % 5] = bld16<SC1>(rsW, (slot % 5 == 4 ? a2v4 : a2v0 + (slot % 5) * 1024), a2s[slot / 5]); }
                SB();
            }
            if (s == 1) {
                WAIT_V(16);
                lds_arrive(LC_B);
            }
        }
        STAMP(p, 6);
        const unsigned sdv = lds_ld<unsigned>((unsigned)(SD_SCR + 256 * w));
        const float Dc = (ch & 1) ? bhi(sdv) : blo(sdv);
#pragma unroll
        for (int gm = 0; gm < 8; ++gm) {
            const int t0 = 256 * gm + 16 * r + 4 * g;
            const v2u kvp = lds_ld<v2u>(XR + 2 * (2044 - t0));
            const float y0 = cacc[gm][0] + bhi(kvp.y) * Dc, y1 = cacc[gm][1] + blo(kvp.y) * Dc, y2 = cacc[gm][2] + bhi(kvp.x) * Dc, y3 = cacc[gm][3] + blo(kvp.x) * Dc;
            lds_st<v2u>(HP + 2 * t0, v2u{pk2(y0, y1), pk2(y2, y3)});
        }
        asm volatile("" ::: "memory");
#pragma unroll
        for (int ps = 0; ps < 4; ++ps) {
            const int n = 64 * ps + lane;
            bst16<SC1>(rsW, (unsigned)(OFF_YT + (ch * SEQ + 8 * n) * 2), 0, lds_ld<v4u>(HP + 16 * n));
        }
    } else {
        if (w == 3) {
            const rsrc_t rsG = mkrs(p.gw, DM * 2);
            const int c16 = lane & 15, row0 = 128 * mt1 + 8 * ft + (lane >> 4);
            v4u gwv[5], xa[5], xb[5];
#pragma unroll
            for (int i = 0; i < 5; ++i) {
                gwv[i] = bld16<0>(rsG, (unsigned)(16 * (c16 + 16 * i)), 0);
                xa[i] = bld16<0>(rsQ, (unsigned)(row0 * DM * 2 + 16 * (c16 + 16 * i)), 0);
                xb[i] = bld16<0>(rsQ, (unsigned)((row0 + 4) * DM * 2 + 16 * (c16 + 16 * i)), 0);
            }
            float s0 = 0.f, s1 = 0.f;
#pragma unroll
            for (int i = 0; i < 5; ++i)
#pragma unroll
                for (int e = 0; e < 4; ++e) { s0 = dot2(xa[i][e], gwv[i][e], s0); s1 = dot2(xb[i][e], gwv[i][e], s1); }
#pragma unroll
            for (int m = 1; m < 16; m <<= 1) { s0 += __shfl_xor(s0, m); s1 += __shfl_xor(s1, m); }
            const float g0 = rb(1.0f / (1.0f + __expf(-rb(s0)))), g1 = rb(1.0f / (1.0f + __expf(-rb(s1))));
            __builtin_amdgcn_raw_buffer_store_b32(__builtin_bit_cast(unsigned, g0), rsW, c16 == 0 ? (unsigned)(OFF_GATE + row0 * 4) : OOB, 0, SC1);
            __builtin_amdgcn_raw_buffer_store_b32(__builtin_bit_cast(unsigned, g1), rsW, c16 == 0 ? (unsigned)(OFF_GATE + (row0 + 4) * 4) : OOB, 0, SC1);
        }
        WAIT_V(0);
        lds_arrive(LC_B);
        lds_wait(LC_B, 4);
        if (w == 3) {
            if (lane == 0) {
                const unsigned old = add_count(p, OFF_CB + 256 * mt1, 1u);
                if (old == 15u) set_flag(p, OFF_RB + 256 * mt1, 0, epoch);
            }
            if (bid == 0) {
#pragma unroll 1
                for (int i = 0; i < 4; ++i) wait_words(rsW, (unsigned)(OFF_FA + 128 * (64 * i + lane)), epoch);
                if (lane == 0) set_flag(p, OFF_EPOCH, 0, epoch);
            }
        }
#pragma unroll
        for (int slot = 0; slot < 60; ++slot) A2[slot / 5][slot % 5] = bld16<SC1>(rsW, (slot % 5 == 4 ? a2v4 : a2v0 + (slot % 5) * 1024), a2s[slot / 5]);
    }
    STAMP(p, 7);

    wait_ready<4>(p, OFF_RB + 256 * (mt2 >> 1), epoch);
    STAMP(p, 8);
    constexpr int W2 = 8;
    unsigned b2off[4];
#pragma unroll
    for (int n = 0; n < 4; ++n) b2off[n] = (unsigned)(OFF_HF + (4 * mt2 + ((w + n) & 3)) * 48 * 1024);
    v4u B2[W2][4];
#pragma unroll
    for (int s = 0; s < W2; ++s)
#pragma unroll
        for (int n = 0; n < 4; ++n) B2[s][n] = bld16<SC1>(rsW, lane * 16, b2off[n] + a2o[s]);
    SB();
    if (has_ch) {
        WAIT_V(32);
        lds_arrive(LC_C);
    }
    f32x4 acc2[5][4];
#pragma unroll
    for (int a = 0; a < 5; ++a)
#pragma unroll
        for (int n = 0; n < 4; ++n) acc2[a][n] = f32x4{0.f, 0.f, 0.f, 0.f};
    const int t = 64 * mt2 + 16 * w + (lane & 15), g4 = lane >> 4;
    v2u qv[5];
    float gt;
#pragma unroll
    for (int s = 0; s < 12; ++s) {
        v4u cb[4];
#pragma unroll
        for (int n = 0; n < 4; ++n) cb[n] = B2[s % W2][n];
        if (s + W2 < 12) {
#pragma unroll
            for (int n = 0; n < 4; ++n) B2[s % W2][n] = bld16<SC1>(rsW, lane * 16, b2off[n] + a2o[s + W2]);
        }
        if (s == 6) {
#pragma unroll
            for (int a = 0; a < 5; ++a) qv[a] = *(const GAS v2u*)(p.q + (size_t)t * DM + 80 * dt + 16 * a + 4 * g4);
            gt = __builtin_bit_cast(float, __builtin_amdgcn_raw_buffer_load_b32(rsW, (unsigned)(OFF_GATE + t * 4), 0, SC1));
        }
#pragma unroll
        for (int n = 0; n < 4; ++n)
#pragma unroll
            for (int a = 0; a < 5; ++a) acc2[a][n] = mfma16(A2[s][a], cb[n], acc2[a][n]);
        SB();
    }
    STAMP(p, 9);
    lds_wait(LC_B, 4);
#pragma unroll
    for (int a = 0; a < 5; ++a)
#pragma unroll
        for (int j = 1; j < 4; ++j) lds_st<f32x4>((unsigned)(G2_RED + ((((w + j) & 3) * 5 + a) * 3 + (j - 1)) * 1024 + lane * 16), acc2[a][j]);
    if (w == 3) {
        lds_wait(LC_C, (unsigned)nch);
        {
            const int c0 = ch - 3, g0 = c0 / 80, n0 = min(nch, 80 * (g0 + 1) - c0), gi = g0 + lane, ni = lane == 0 ? n0 : nch - n0;
            if (lane < 2 && ni > 0) {
                const unsigned old = add_count(p, OFF_CC + 256 * gi, (unsigned)ni);
                if (old + (unsigned)ni == 80u) set_flag(p, OFF_RC + 256 * gi, 0, epoch);
            }
        }
        wait_ready<4>(p, OFF_RC + 256 * dt, epoch);
        v4u yv[10];
#pragma unroll
        for (int it = 0; it < 10; ++it) {
            const int c = lane + 64 * it;
            yv[it] = bld16<SC1>(rsW, (unsigned)(OFF_YT + ((80 * dt + (c >> 3)) * SEQ + 64 * mt2 + 8 * (c & 7)) * 2), 0);
        }
#pragma unroll
        for (int it = 0; it < 10; ++it) lds_st<v4u>((unsigned)(G2_Y + (lane + 64 * it) * 16), yv[it]);
    }
    WAIT_L(0);
    BAR();
    STAMP(p, 10);
    const float omg = rb(1.0f - gt);
    const int q = (lane >> 2) & 3, pp = lane & 3;
#pragma unroll
    for (int a = 0; a < 5; ++a) {
        f32x4 b = acc2[a][0];
#pragma unroll
        for (int j = 0; j < 3; ++j) b += lds_ld<f32x4>((unsigned)(G2_RED + ((w * 5 + a) * 3 + j) * 1024 + lane * 16));
        const v2u y = tr_read((unsigned)(G2_Y + (16 * a + 4 * g4 + q) * 128 + 32 * w + 8 * pp));
        const int d0 = 80 * dt + 16 * a + 4 * g4;
        const float o0 = gt * (blo(qv[a].x) * blo(y.x)) + omg * b[0], o1 = gt * (bhi(qv[a].x) * bhi(y.x)) + omg * b[1];
        const float o2 = gt * (blo(qv[a].y) * blo(y.y)) + omg * b[2], o3 = gt * (bhi(qv[a].y) * bhi(y.y)) + omg * b[3];
        *(GAS v2u*)(p.out + (size_t)t * DM + d0) = v2u{pk2(o0, o1), pk2(o2, o3)};
    }
    STAMP(p, 11);
}

extern "C" void kernel_launch(void* const* d_in, const int* in_sizes, int n_in, void* d_out, int out_size, void* d_ws, size_t ws_size, hipStream_t stream) {
    Params p;
    p.q = (const u16*)d_in[0]; p.k = (const u16*)d_in[1]; p.v = (const u16*)d_in[2]; p.sk = (const u16*)d_in[3];
    p.hk = (const u16*)d_in[4]; p.sd = (const u16*)d_in[5];
    p.wg = (const u16*)d_in[7]; p.wu = (const u16*)d_in[8]; p.wd = (const u16*)d_in[9]; p.gw = (const u16*)d_in[10];
    p.out = (u16*)d_out; p.ws = (unsigned char*)d_ws;
    (void)hipFuncSetAttribute((const void*)k_fused, hipFuncAttributeMaxDynamicSharedMemorySize, LDS_BYTES);
    k_fused<<<256, 256, LDS_BYTES, stream>>>(p);
}
```

```cpp
#include <hip/hip_runtime.h>
#include <stdint.h>

#define LAS __attribute__((address_space(3)))
#define GAS __attribute__((address_space(1)))
typedef unsigned short u16;
typedef unsigned v4u __attribute__((ext_vector_type(4)));
typedef unsigned v2u __attribute__((ext_vector_type(2)));
typedef __bf16 bf8 __attribute__((ext_vector_type(8)));
typedef __bf16 bf2 __attribute__((ext_vector_type(2)));
typedef short s4 __attribute__((ext_vector_type(4)));
typedef float f32x2 __attribute__((ext_vector_type(2)));
typedef float f32x4 __attribute__((ext_vector_type(4)));
typedef float f32x16 __attribute__((ext_vector_type(16)));
typedef __amdgpu_buffer_rsrc_t rsrc_t;

constexpr int SEQ = 2048, DM = 640, FF = 1536;
constexpr int OFF_EPOCH = 0;
constexpr int OFF_FA = 32768;
constexpr int OFF_CB = 2048;
constexpr int OFF_CC = 8192;
constexpr int OFF_RB = 12288;
constexpr int OFF_RC = 16384;
constexpr int OFF_STAMP = 32 * 1024 * 1024;
constexpr int OFF_KVR = 65536;
constexpr int OFF_WDT = OFF_KVR + DM * SEQ * 2;
constexpr int OFF_HF = OFF_WDT + FF * DM * 2;
constexpr int OFF_YT = OFF_HF + SEQ * FF * 2;
constexpr int OFF_GATE = OFF_YT + DM * SEQ * 2;
constexpr int WS_BYTES = OFF_GATE + SEQ * 4;
constexpr int SC1 = 16;
constexpr unsigned OOB = 0x80000000u;
constexpr unsigned SPIN_MAX = 1u << 16;

struct Params {
    const u16 *q, *k, *v, *sk, *hk, *sd, *wg, *wu, *wd, *gw;
    u16* out;
    unsigned char* ws;
};

__device__ __forceinline__ unsigned pk2(float a, float b) {
    f32x2 x = {a, b};
    return __builtin_bit_cast(unsigned, __builtin_convertvector(x, bf2));
}
__device__ __forceinline__ float blo(unsigned u) { return __builtin_bit_cast(float, u << 16); }
__device__ __forceinline__ float bhi(unsigned u) { return __builtin_bit_cast(float, u & 0xffff0000u); }
__device__ __forceinline__ float rb(float x) { return blo(pk2(x, x)); }
__device__ __forceinline__ f32x4 mfma16(v4u a, v4u b, f32x4 c) {
    return __builtin_amdgcn_mfma_f32_16x16x32_bf16(__builtin_bit_cast(bf8, a), __builtin_bit_cast(bf8, b), c, 0, 0, 0);
}
__device__ __forceinline__ f32x16 mfma32(v4u a, v4u b, f32x16 c) {
    return __builtin_amdgcn_mfma_f32_32x32x16_bf16(__builtin_bit_cast(bf8, a), __builtin_bit_cast(bf8, b), c, 0, 0, 0);
}
__device__ __forceinline__ float dot2(unsigned a, unsigned b, float c) {
    return __builtin_amdgcn_fdot2_f32_bf16(__builtin_bit_cast(bf2, a), __builtin_bit_cast(bf2, b), c, false);
}
template <class T> __device__ __forceinline__ T lds_ld(unsigned a) { return *(LAS T*)(uintptr_t)a; }
template <class T> __device__ __forceinline__ void lds_st(unsigned a, T v) { *(LAS T*)(uintptr_t)a = v; }
__device__ __forceinline__ v2u tr_read(unsigned a) {
    return __builtin_bit_cast(v2u, __builtin_amdgcn_ds_read_tr16_b64_v4i16((LAS s4*)(uintptr_t)a));
}
__device__ __forceinline__ rsrc_t mkrs(const void* p, unsigned bytes) { return __builtin_amdgcn_make_buffer_rsrc((void*)p, 0, bytes, 0x00020000); }
template <int AUX> __device__ __forceinline__ v4u bld16(rsrc_t rs, unsigned voff, unsigned soff) { return __builtin_amdgcn_raw_buffer_load_b128(rs, voff, soff, AUX); }
template <int AUX> __device__ __forceinline__ void bst16(rsrc_t rs, unsigned voff, unsigned soff, v4u v) { __builtin_amdgcn_raw_buffer_store_b128(v, rs, voff, soff, AUX); }
#define WAIT_V(n) asm volatile("s_waitcnt vmcnt(" #n ")" ::: "memory")
#define WAIT_L(n) asm volatile("s_waitcnt lgkmcnt(" #n ")" ::: "memory")
#define BAR() do { asm volatile("" ::: "memory"); __builtin_amdgcn_s_barrier(); asm volatile("" ::: "memory"); } while (0)
#define SB() __builtin_amdgcn_sched_barrier(0)
#define PIN_S(x) asm volatile("" : "+s"(x))
#define PIN_V(x) asm volatile("" : "+v"(x))
#define RLX_AGENT __ATOMIC_RELAXED, __HIP_MEMORY_SCOPE_AGENT
#define STAMP(p, i) do {} while (0)

__device__ __forceinline__ void dma16(rsrc_t rs, unsigned voff, unsigned soff, unsigned lds_addr) {
    asm volatile("s_mov_b32 m0, %0\n\ts_nop 0\n\tbuffer_load_dwordx4 %1, %2, %3 offen lds" ::"s"(lds_addr), "v"(voff), "s"(rs), "s"(soff) : "memory", "m0");
}
__device__ __forceinline__ void dma16_sc1(rsrc_t rs, unsigned voff, unsigned soff, unsigned lds_addr) {
    asm volatile("s_mov_b32 m0, %0\n\ts_nop 0\n\tbuffer_load_dwordx4 %1, %2, %3 offen sc1 lds" ::"s"(lds_addr), "v"(voff), "s"(rs), "s"(soff) : "memory", "m0");
}
#define SET_M0(base, imm) asm volatile("s_add_u32 m0, %0, %1" ::"s"(base), "i"(imm) : "memory", "m0", "scc")
__device__ __forceinline__ void dma16_m0(rsrc_t rs, unsigned voff, unsigned soff) {
    asm volatile("buffer_load_dwordx4 %0, %1, %2 offen lds" ::"v"(voff), "s"(rs), "s"(soff) : "memory");
}
__device__ __forceinline__ void dma16_m0_sc1(rsrc_t rs, unsigned voff, unsigned soff) {
    asm volatile("buffer_load_dwordx4 %0, %1, %2 offen sc1 lds" ::"v"(voff), "s"(rs), "s"(soff) : "memory");
}
__device__ __forceinline__ void aload16(v4u& d, rsrc_t rs, unsigned voff) {
    asm volatile("buffer_load_dwordx4 %0, %1, %2, 0 offen" : "=v"(d) : "v"(voff), "s"(rs) : "memory");
}

__device__ __forceinline__ void wait_words(rsrc_t rsW, unsigned voff, unsigned e) {
    unsigned t0, left = SPIN_MAX;
    asm volatile(
        "1:\n\t"
        "buffer_load_dword %0, %2, %3, 0 offen sc1\n\t"
        "s_waitcnt vmcnt(0)\n\t"
        "v_cmp_ne_u32 vcc, %4, %0\n\t"
        "s_nop 4\n\t"
        "s_cbranch_vccz 2f\n\t"
        "s_sleep 8\n\t"
        "s_sub_u32 %1, %1, 1\n\t"
        "s_cbranch_scc0 1b\n\t"
        "2:"
        : "=&v"(t0), "+s"(left)
        : "v"(voff), "s"(rsW), "s"(e)
        : "memory", "vcc", "scc");
}
__device__ __forceinline__ void dma4(rsrc_t rs, unsigned voff, unsigned lds_addr) {
    asm volatile("s_mov_b32 m0, %0\n\ts_nop 0\n\tbuffer_load_dword %1, %2, 0 offen lds" ::"s"(lds_addr), "v"(voff), "s"(rs) : "memory", "m0");
}
__device__ __forceinline__ void dma4_sc1(rsrc_t rs, unsigned voff, unsigned lds_addr) {
    asm volatile("s_mov_b32 m0, %0\n\ts_nop 0\n\tbuffer_load_dword %1, %2, 0 offen sc1 lds" ::"s"(lds_addr), "v"(voff), "s"(rs) : "memory", "m0");
}
__device__ __forceinline__ unsigned add_count(const Params& p, int off, unsigned n) {
    return __hip_atomic_fetch_add((GAS unsigned*)(p.ws + off), n, RLX_AGENT);
}
template <int SLEEP>
__device__ __forceinline__ void wait_ready(const Params& p, int off, unsigned e) {
    GAS unsigned* c = (GAS unsigned*)(p.ws + off);
    for (unsigned it = 0; it < SPIN_MAX; ++it) {
        if ((unsigned)__builtin_amdgcn_readfirstlane(__hip_atomic_load(c, RLX_AGENT)) == e) break;
        __builtin_amdgcn_s_sleep(SLEEP);
    }
    __builtin_amdgcn_fence(__ATOMIC_ACQUIRE, "wavefront");
}
__device__ __forceinline__ void lds_arrive(unsigned off) {
    if ((threadIdx.x & 63) == 0) (void)__hip_atomic_fetch_add((LAS unsigned*)(uintptr_t)off, 1u, __ATOMIC_RELAXED, __HIP_MEMORY_SCOPE_WORKGROUP);
}
__device__ __forceinline__ void lds_wait(unsigned off, unsigned n) {
    for (unsigned it = 0; it < (SPIN_MAX << 4); ++it) {
        if ((unsigned)__builtin_amdgcn_readfirstlane(__hip_atomic_load((LAS unsigned*)(uintptr_t)off, __ATOMIC_RELAXED, __HIP_MEMORY_SCOPE_WORKGROUP)) >= n) break;
        __builtin_amdgcn_s_sleep(1);
    }
    asm volatile("" ::: "memory");
}
__device__ __forceinline__ void set_flag(const Params& p, int off, int idx, unsigned e) {
    __hip_atomic_store((GAS unsigned*)(p.ws + off) + idx, e, RLX_AGENT);
}

constexpr int G1_STAGE = 40960, G1_G = 16384, G1_U = 16384 + 12288;
constexpr int SCR = 3 * G1_STAGE;
constexpr int PR_K = SCR;
constexpr int PR_V = PR_K + 11264;
constexpr int PR_S = PR_V + 10240;
constexpr int PR_D = PR_S + 1024;
constexpr int CV_RAW = SCR;
constexpr int CV_XR = SCR + 16384, CV_XRS = 4608;
constexpr int FLG_SCR = CV_XR + 3 * CV_XRS;
constexpr int SD_SCR = FLG_SCR + 1024;
constexpr int DUMMY = PR_D + 10240;
constexpr int LC_B = 163808, LC_C = 163824;
constexpr int CV_CS = 4160, CV_HP = 0, CV_HPS = 4 * CV_CS;
constexpr int G2_Y = 3 * CV_HPS;
constexpr int G2_RED = 61440;
constexpr int LDS_BYTES = 163840;
static_assert(SD_SCR + 1024 <= DUMMY && DUMMY + 4096 <= LC_B && G2_Y + 80 * 128 <= G2_RED && G2_RED + 60 * 1024 <= SCR, "lds");
constexpr int CODE_BYTES = 24576;

__global__ void __launch_bounds__(256, 1) k_fused(Params p) {
    extern __shared__ __attribute__((aligned(16))) unsigned char smem[];
    const int tid = threadIdx.x, lane = tid & 63, w = __builtin_amdgcn_readfirstlane(tid >> 6);
    const int bid = blockIdx.x;
    const int xcd = bid & 7, bi = bid >> 3, mq = xcd >> 1, half = xcd & 1;
    STAMP(p, 0);
    {
        unsigned long long pc;
        asm volatile("s_getpc_b64 %0" : "=s"(pc));
        const rsrc_t rsC = mkrs((const void*)(pc & ~255ull), CODE_BYTES - 256);
        dma16(rsC, (unsigned)(((bi + 32 * w) << 10) + lane * 16), 0, DUMMY);
    }
    const int nch = bid < 128 ? 3 : 2, ch = (bid < 128 ? 3 * bid : 384 + 2 * (bid - 128)) + w;
    const bool has_ch = w < nch;
    asm volatile("" ::"s"(p.q), "s"(p.k), "s"(p.v), "s"(p.sk), "s"(p.hk), "s"(p.sd), "s"(p.wg), "s"(p.wu), "s"(p.wd), "s"(p.gw), "s"(p.out), "s"(p.ws));
    rsrc_t rsW = mkrs(p.ws, WS_BYTES);
    rsrc_t rsQ = mkrs(p.q, SEQ * DM * 2);

    const int ptt = bid & 31, pd8 = bid >> 5;
    auto prep_loads = [&]() {
        const rsrc_t rsK = mkrs(p.k, SEQ * DM * 2), rsV = mkrs(p.v, SEQ * DM * 2), rsS = mkrs(p.sk, DM * 4 * 2), rsD = mkrs(p.wd, FF * DM * 2);
        auto kdma = [&](int i) {
            const int c = 64 * i + lane, row = c / 10, col = c - 10 * row, t = 64 * ptt - 4 + row;
            dma16(rsK, (t >= 0 && c < 680) ? (unsigned)((t * DM + 80 * pd8 + 8 * col) * 2) : OOB, 0, PR_K + 1024 * i);
        };
        auto vdma = [&](int i) {
            const int c = 64 * i + lane, row = c / 10, col = c - 10 * row;
            dma16(rsV, (unsigned)(((64 * ptt + row) * DM + 80 * pd8 + 8 * col) * 2), 0, PR_V + 1024 * i);
        };
        auto ddma = [&](int j, int i) {
            const int g = 2 * bid + j, gdt = g / 48, gks = g - 48 * gdt;
            const int c = 64 * i + lane, row = c / 10, col = c - 10 * row;
            dma16(rsD, g < 384 ? (unsigned)(((32 * gks + row) * DM + 80 * gdt + 8 * col) * 2) : OOB, 0, PR_D + 5120 * j + 1024 * i);
        };
        if (w == 0) {
#pragma unroll
            for (int i = 0; i < 8; ++i) kdma(i);
        } else if (w == 1) {
#pragma unroll
            for (int i = 8; i < 11; ++i) kdma(i);
#pragma unroll
            for (int i = 0; i < 5; ++i) vdma(i);
        } else if (w == 2) {
#pragma unroll
            for (int i = 5; i < 10; ++i) vdma(i);
#pragma unroll
            for (int i = 0; i < 5; ++i) ddma(0, i);
        } else {
            dma16(rsS, lane < 40 ? (unsigned)(80 * pd8 * 8 + 16 * lane) : OOB, 0, PR_S);
#pragma unroll
            for (int i = 0; i < 5; ++i) ddma(1, i);
        }
    };
    v4u pkv[3], pwd[5];
    auto prep_compute = [&]() {
        const int i16 = lane & 15, q = (lane >> 2) & 3, pp = lane & 3;
#pragma unroll
        for (int ps = 0; ps < 3; ++ps) {
            const int quad = min(4 * ps + w, 9), cg = quad >> 1, tgp = 4 * (quad & 1) + (lane >> 4);
            const unsigned ka = (unsigned)(PR_K + (8 * tgp + q) * 160 + (16 * cg + 4 * pp) * 2), va = ka + (PR_V - PR_K);
            const v2u k0 = tr_read(ka), k1 = tr_read(ka + 640), k2 = tr_read(ka + 1280), v0 = tr_read(va), v1 = tr_read(va + 640);
            const v2u tw = lds_ld<v2u>((unsigned)(PR_S + (16 * cg + i16) * 8));
            const unsigned W10 = __builtin_amdgcn_alignbit(tw.x, tw.x, 16), W32 = __builtin_amdgcn_alignbit(tw.y, tw.y, 16);
            const unsigned P[6] = {k0.x, k0.y, k1.x, k1.y, k2.x, k2.y};
            unsigned Q[5];
#pragma unroll
            for (int m = 0; m < 5; ++m) Q[m] = __builtin_amdgcn_alignbit(P[m + 1], P[m], 16);
            const unsigned V[4] = {v0.x, v0.y, v1.x, v1.y};
            float r[8];
#pragma unroll
            for (int m = 0; m < 4; ++m) {
                r[2 * m] = dot2(Q[m], W32, dot2(Q[m + 1], W10, 0.f)) * blo(V[m]);
                r[2 * m + 1] = dot2(P[m + 1], W32, dot2(P[m + 2], W10, 0.f)) * bhi(V[m]);
            }
            pkv[ps] = v4u{pk2(r[7], r[6]), pk2(r[5], r[4]), pk2(r[3], r[2]), pk2(r[1], r[0])};
        }
        const unsigned T = (unsigned)(PR_D + 5120 * (w & 1));
        const int gq = lane >> 4, row = 16 * (gq >> 1) + 4 * (gq & 1) + q;
#pragma unroll
        for (int at = 0; at < 5; ++at) {
            const v2u lo = tr_read(T + row * 160 + (16 * at + 4 * pp) * 2);
            const v2u hi = tr_read(T + (row + 8) * 160 + (16 * at + 4 * pp) * 2);
            pwd[at] = v4u{lo.x, lo.y, hi.x, hi.y};
        }
    };
    auto prep_store = [&]() {
        if (bid < 24) __builtin_amdgcn_raw_buffer_store_b32(0u, rsW, tid == 0 ? (unsigned)(bid < 16 ? OFF_CB + 256 * bid : OFF_CC + 256 * (bid - 16)) : OOB, 0, SC1);
        const int i16 = lane & 15;
#pragma unroll
        for (int ps = 0; ps < 3; ++ps) {
            const int quad = min(4 * ps + w, 9), cg = quad >> 1, tgp = 4 * (quad & 1) + (lane >> 4);
            bst16<SC1>(rsW, 4 * ps + w < 10 ? (unsigned)(OFF_KVR + ((80 * pd8 + 16 * cg + i16) * SEQ + 2040 - 64 * ptt - 8 * tgp) * 2) : OOB, 0, pkv[ps]);
        }
        if (w >= 2) {
            const int g = 2 * bid + (w - 2), gdt = g / 48, gks = g - 48 * gdt;
#pragma unroll
            for (int at = 0; at < 5; ++at) bst16<SC1>(rsW, g < 384 ? (unsigned)(OFF_WDT + ((gdt * 48 + gks) * 5 + at) * 1024 + lane * 16) : OOB, 0, pwd[at]);
        }
    };

    prep_loads();
    STAMP(p, 14);

    const int mt1 = 4 * mq + (bi & 3), ft = 8 * half + (bi >> 2);
    const int wm = w & 1, wf = w >> 1;
#define NCOMMON 1
    constexpr int NC = NCOMMON, NR = 10 - NC;
    const int rot1 = ((ft & 7) + ((NR * (mt1 & 3)) >> 2)) % NR;
    rsrc_t rsWg = mkrs(p.wg, DM * FF * 2), rsWu = mkrs(p.wu, DM * FF * 2);
    PIN_S(rsW); PIN_S(rsWg); PIN_S(rsWu); PIN_S(rsQ);
    unsigned xoff[4], goff[3], uoff[3];
#pragma unroll
    for (int n = 0; n < 4; ++n) {
        const int row = 32 * w + 8 * n + (lane >> 3);
        xoff[n] = (unsigned)((128 * mt1 + row) * 1280 + (((lane & 7) ^ ((4 * n + (lane >> 4)) & 7)) << 4));
    }
#pragma unroll
    for (int n = 0; n < 3; ++n) {
        const int Lc = 64 * (3 * w + n) + lane, k = Lc / 12, c = Lc - 12 * k;
        goff[n] = (unsigned)(k * 3072 + 192 * ft + 16 * c);
        uoff[n] = (unsigned)(k * 3072 + 192 * ft + 16 * (c ^ 2));
    }
    auto g1_issue = [&](int kt, int stg) {
#pragma unroll
        for (int n = 0; n < 4; ++n) dma16(rsQ, xoff[n], kt * 128, stg + (4 * w + n) * 1024);
#pragma unroll
        for (int n = 0; n < 3; ++n) {
            dma16(rsWg, goff[n], kt * 64 * 3072, stg + G1_G + (3 * w + n) * 1024);
            dma16(rsWu, uoff[n], kt * 64 * 3072, stg + G1_U + (3 * w + n) * 1024);
        }
    };
    g1_issue(0, 0);
    const unsigned epoch = *(const __attribute__((address_space(4))) unsigned*)(uintptr_t)(p.ws + OFF_EPOCH) + 1u;
    f32x16 acc[3][2];
#pragma unroll
    for (int a = 0; a < 3; ++a)
#pragma unroll
        for (int b = 0; b < 2; ++b)
#pragma unroll
            for (int r = 0; r < 16; ++r) acc[a][b][r] = 0.f;
    STAMP(p, 1);
    WAIT_V(10);
    BAR();
    STAMP(p, 15);
    int kap = NC > 1 ? 1 : 1 + rot1;
    int kcnt = 1;
    auto knext = [&]() { ++kcnt; kap = kcnt < NC ? kcnt : kcnt == NC ? NC + rot1 : kap == 9 ? NC : kap + 1; };
    g1_issue(kap, G1_STAGE);
    knext();
    prep_compute();
    STAMP(p, 16);

    const int l31 = lane & 31, hh = lane >> 5;
    const int mt2 = 8 * mq + (bi & 7), dt = 4 * half + (bi >> 3);
    unsigned favo;
    {
        const int c0 = ch - w, g0 = c0 / 80;
        int who = 24 * dt + min(max(lane - 32, 0), 23);
        if (lane < 32 && has_ch) who = 32 * (ch / 80) + lane;
        if (lane == 56) who = mt1;
        if (lane == 57) who = 16 + g0;
        if (lane == 58) who = 16 + min(g0 + 1, 7);
        favo = (unsigned)(OFF_FA + 128 * who);
    }
    const int rot2 = (3 * (bi & 7) / 2 + 3 * (bi >> 3)) % 12;
    unsigned a2o[12];
#pragma unroll
    for (int s = 0; s < 12; ++s) { const int ks = s + rot2; a2o[s] = (unsigned)(12 * w + (ks >= 12 ? ks - 12 : ks)) * 1024u; }
    const unsigned a2base = (unsigned)(OFF_WDT + dt * 48 * 5 * 1024);
    {
        const int brow = 64 * wm + l31;
        unsigned ba[4];
#pragma unroll
        for (int kk = 0; kk < 4; ++kk) ba[kk] = (unsigned)(brow * 128 + (((2 * kk + hh) ^ ((brow >> 1) & 7)) << 4));
        const int G = lane >> 4, q = (lane >> 2) & 3, pp = lane & 3;
        unsigned aa[3];
#pragma unroll
        for (int fb = 0; fb < 3; ++fb) {
            const int fbg = 3 * wf + fb;
            aa[fb] = (unsigned)(((G & 1) ? G1_U + (fbg ^ 1) * 32 : G1_G + fbg * 32) + (8 * (G >> 1) + q) * 192 + 8 * pp);
        }
        v4u fb0[2], fb1[2];
        v2u fa0[3][2], fa1[3][2];
#define RDB(d, kk, mt) d = lds_ld<v4u>(ba[kk] + (mt) * 4096)
#define RDA(d, kk, fb, hi) d[fb][hi] = tr_read(aa[fb] + (kk) * 16 * 192 + (hi) * 4 * 192)
#define MM(A, B, fb, mt) acc[fb][mt] = mfma32(v4u{A[fb][0].x, A[fb][0].y, A[fb][1].x, A[fb][1].y}, B[mt], acc[fb][mt])
        WAIT_V(10);
        WAIT_L(0);
        BAR();
        STAMP(p, 2);
        RDB(fb0[0], 0, 0); RDA(fa0, 0, 0, 0); RDA(fa0, 0, 0, 1); RDB(fb0[1], 0, 1); RDA(fa0, 0, 1, 0); RDA(fa0, 0, 1, 1); RDA(fa0, 0, 2, 0); RDA(fa0, 0, 2, 1);
        int st = 0;
        unsigned mx = 2 * G1_STAGE + 4096u * w, mg = 2 * G1_STAGE + G1_G + 3072u * w, mu = 2 * G1_STAGE + G1_U + 3072u * w;
        unsigned sx = kap * 128, sw = kap * 64 * 3072;
#define STEP(FA, FB, GA, GB, kn, D0, D1, D2)                                                     \
            MM(FA, FB, 0, 0); SB(); RDB(GB[0], kn, 0); RDA(GA, kn, 0, 0); SB();                 \
            MM(FA, FB, 0, 1); SB(); RDA(GA, kn, 0, 1); RDB(GB[1], kn, 1); SB();                 \
            MM(FA, FB, 1, 0); SB(); RDA(GA, kn, 1, 0); RDA(GA, kn, 1, 1); D0##_M; SB();         \
            MM(FA, FB, 1, 1); SB(); D0##_L; RDA(GA, kn, 2, 0); D1##_M; SB();                    \
            MM(FA, FB, 2, 0); SB(); D1##_L; RDA(GA, kn, 2, 1); D2##_M; SB();                    \
            MM(FA, FB, 2, 1); SB(); D2##_L; SB();
#define X0_M SET_M0(mx, 0)
#define X0_L dma16_m0(rsQ, xoff[0], sx)
#define X1_M SET_M0(mx, 1024)
#define X1_L dma16_m0(rsQ, xoff[1], sx)
#define X2_M SET_M0(mx, 2048)
#define X2_L dma16_m0(rsQ, xoff[2], sx)
#define X3_M SET_M0(mx, 3072)
#define X3_L dma16_m0(rsQ, xoff[3], sx)
#define G0_M SET_M0(mg, 0)
#define G0_L dma16_m0(rsWg, goff[0], sw)
#define G1_M SET_M0(mg, 1024)
#define G1_L dma16_m0(rsWg, goff[1], sw)
#define G2_M SET_M0(mg, 2048)
#define G2_L dma16_m0(rsWg, goff[2], sw)
#define U0_M SET_M0(mu, 0)
#define U0_L dma16_m0(rsWu, uoff[0], sw)
#define U1_M SET_M0(mu, 1024)
#define U1_L dma16_m0(rsWu, uoff[1], sw)
#define U2_M SET_M0(mu, 2048)
#define U2_L dma16_m0(rsWu, uoff[2], sw)
#define KBODY(WAITV) { \
            const int dlt = (st == 2 * G1_STAGE) ? -2 * G1_STAGE : G1_STAGE; \
            SB(); \
            STEP(fa0, fb0, fa1, fb1, 1, X0, X1, X2) \
            STEP(fa1, fb1, fa0, fb0, 2, X3, G0, U0) \
            STEP(fa0, fb0, fa1, fb1, 3, G1, U1, G2) \
            U2_M; SB(); \
            MM(fa1, fb1, 0, 0); SB(); U2_L; ba[0] += dlt; aa[0] += dlt; SB(); \
            MM(fa1, fb1, 0, 1); SB(); aa[1] += dlt; aa[2] += dlt; \
            knext(); \
            mx = (unsigned)st + 4096u * w; mg = (unsigned)st + G1_G + 3072u * w; mu = (unsigned)st + G1_U + 3072u * w; sx = kap * 128; sw = kap * 64 * 3072; \
            SB(); \
            MM(fa1, fb1, 1, 0); SB(); \
            WAIT_L(0); \
            WAITV; \
            BAR(); \
            RDB(fb0[0], 0, 0); RDA(fa0, 0, 0, 0); RDA(fa0, 0, 0, 1); SB(); \
            MM(fa1, fb1, 1, 1); SB(); RDB(fb0[1], 0, 1); RDA(fa0, 0, 1, 0); RDA(fa0, 0, 1, 1); SB(); \
            MM(fa1, fb1, 2, 0); SB(); RDA(fa0, 0, 2, 0); RDA(fa0, 0, 2, 1); ba[1] += dlt; SB(); \
            MM(fa1, fb1, 2, 1); SB(); ba[2] += dlt; ba[3] += dlt; \
            st += dlt; \
            }
#pragma unroll 1
        for (int kt = 0; kt < 10; ++kt) {
            KBODY(if (__builtin_expect((0x24A >> kt) & 1, 0)) { if (kt == 1) { if (w < 2) WAIT_V(13); else WAIT_V(18); } else if (kt == 3) WAIT_V(16); else if (kt == 6) WAIT_V(14); else WAIT_V(20); } else WAIT_V(10))
            if (__builtin_expect(kt == 7, 0)) {
                sx = 0x40000000u; mx = DUMMY; mg = DUMMY; mu = DUMMY; sw = a2base;
                rsWg = rsW; rsWu = rsW;
#pragma unroll
                for (int n = 0; n < 3; ++n) { goff[n] = a2o[0] * 5 + (unsigned)(lane * 16 + 2048 * n); uoff[n] = goff[n] + 1024; }
            }
            if (__builtin_expect(kt == 8, 0)) {
#pragma unroll
                for (int n = 0; n < 3; ++n) { goff[n] = (n == 2 ? a2o[2] * 5 : a2o[1] * 5 + 1024 + 2048 * n) + (unsigned)(lane * 16); uoff[n] = (n == 0 ? a2o[1] * 5 + 2048 : n == 1 ? a2o[1] * 5 + 4096 : a2o[2] * 5 + 1024) + (unsigned)(lane * 16); }
                sx = 0x40000000u; mx = DUMMY; mg = DUMMY; mu = DUMMY; sw = a2base;
            }
            if (__builtin_expect((0x135 >> kt) & 1, 0)) {
                if (kt == 0) prep_store();
                else if (kt == 2) {
                    __builtin_amdgcn_raw_buffer_store_b32(epoch, rsW, tid == 0 ? (unsigned)(OFF_FA + 128 * bid) : OOB, 0, SC1);
                    const rsrc_t rsH = mkrs(p.hk, DM * SEQ * 2);
#pragma unroll
                    for (int n = 0; n < 4; ++n) dma16(rsH, has_ch ? (unsigned)(ch * SEQ * 2 + n * 1024 + lane * 16) : OOB, 0, CV_RAW + 4096u * w + 1024u * n);
                    dma4(mkrs(p.sd, DM * 2), has_ch ? (unsigned)((ch & ~1) * 2) : OOB, SD_SCR + 256u * w);
                } else if (kt == 4) dma4_sc1(rsW, favo, FLG_SCR + 256u * w);
                else if (kt == 5) {
                    const unsigned flv = lds_ld<unsigned>((unsigned)(FLG_SCR + 256 * w + lane * 4));
                    if (__builtin_amdgcn_ballot_w64(flv != epoch) != 0ull) wait_words(rsW, favo, epoch);
#pragma unroll
                    for (int n = 0; n < 4; ++n) dma16_sc1(rsW, has_ch ? (unsigned)(OFF_KVR + ch * SEQ * 2 + n * 1024 + lane * 16) : OOB, 0, has_ch ? (unsigned)(CV_XR + CV_XRS * w + n * 1024) : (unsigned)DUMMY);
                } else {
                    if (tid == 0) { lds_st<unsigned>(LC_B, 0u); lds_st<unsigned>(LC_C, 0u); }
                }
            }
        }
#undef MM
#undef STEP
#undef KBODY
    }
    STAMP(p, 3);
    v4u A2[12][5];
    unsigned a2s[12], a2v0 = (unsigned)(lane * 16), a2v4 = a2v0 + 4096;
    PIN_V(a2v4);
#pragma unroll
    for (int s = 0; s < 12; ++s) a2s[s] = a2base + a2o[s] * 5;
#pragma unroll
    for (int fb = 0; fb < 3; ++fb)
#pragma unroll
        for (int mt = 0; mt < 2; ++mt) {
            const f32x16& a = acc[fb][mt];
            v4u o;
#pragma unroll
            for (int j = 0; j < 4; ++j) o[j] = pk2(a[2 * j] * a[8 + 2 * j], a[2 * j + 1] * a[8 + 2 * j + 1]);
            const int Ft = 6 * ft + 3 * wf + fb;
            const int mrow = 128 * mt1 + 64 * wm + 32 * mt + l31;
            const int pos = (lane & 15) + 16 * (2 * (Ft & 1) + hh);
            bst16<SC1>(rsW, (unsigned)(OFF_HF + ((mrow >> 4) * 48 + (Ft >> 1)) * 1024 + pos * 16), 0, o);
        }
    STAMP(p, 4);

    if (has_ch) {
        const unsigned HP = (unsigned)(CV_HP + w * CV_HPS), XR = (unsigned)(CV_XR + w * CV_XRS), RAW = (unsigned)(CV_RAW + w * 4096);
        v4u c0[5], c1[5];
#pragma unroll
        for (int ps = 0; ps < 5; ++ps) {
            const int n = 64 * ps + lane;
            c0[ps] = lds_ld<v4u>(RAW + 16 * (ps == 0 ? max(n - 2, 0) : ps < 4 ? n - 2 : 254 + (lane & 1)));
            c1[ps] = lds_ld<v4u>(RAW + 16 * (ps == 0 ? max(n - 1, 0) : ps < 4 ? n - 1 : 255));
        }
        if (lane < 2) c0[0] = v4u{0, 0, 0, 0};
        if (lane < 1) c1[0] = v4u{0, 0, 0, 0};
        if (lane & 1) c1[4] = v4u{0, 0, 0, 0};
#pragma unroll
        for (int ps = 0; ps < 5; ++ps) {
            const int n = 64 * ps + lane;
            const unsigned Dw[6] = {c0[ps].x, c0[ps].y, c0[ps].z, c0[ps].w, c1[ps].x, c1[ps].y};
            v4u o0, o1, o2, o3;
#pragma unroll
            for (int q = 0; q < 4; ++q) {
                o0[q] = __builtin_amdgcn_alignbit(Dw[q + 1], Dw[q], 16);
                o1[q] = Dw[q + 1];
                o2[q] = __builtin_amdgcn_alignbit(Dw[q + 2], Dw[q + 1], 16);
                o3[q] = Dw[q + 2];
            }
            if (ps < 4 || lane < 2) {
                lds_st<v4u>(HP + 0 * CV_CS + 16 * n, o0);
                lds_st<v4u>(HP + 1 * CV_CS + 16 * n, o1);
                lds_st<v4u>(HP + 2 * CV_CS + 16 * n, o2);
                lds_st<v4u>(HP + 3 * CV_CS + 16 * n, o3);
            }
        }
        if (lane < 32) lds_st<v4u>(XR + 4096 + 16 * lane, v4u{0, 0, 0, 0});
        asm volatile("" ::: "memory");
        STAMP(p, 5);

        const int r = lane & 15, g = lane >> 4;
        unsigned ap0 = HP + (unsigned)((r & 3) * CV_CS + 2 * ((r & ~3) + 8 * g));
        unsigned ap1 = ap0 + 8;
        PIN_V(ap0); PIN_V(ap1);
        unsigned bp = XR + (unsigned)(2 * (240 - 16 * r + 8 * g));
        PIN_V(bp);
        f32x4 cacc[8];
#pragma unroll
        for (int i = 0; i < 8; ++i) cacc[i] = f32x4{0.f, 0.f, 0.f, 0.f};
        v4u CA[2][8], CB[2][8];
        auto cvrd = [&](int set, int s, int i) {
            CB[set][i] = lds_ld<v4u>(bp + 512 * (7 - i) + 64 * s);
            const v2u lo = lds_ld<v2u>(ap0 + 512 * i + 64 * s), hi = lds_ld<v2u>(ap1 + 512 * i + 64 * s);
            CA[set][i] = v4u{lo.x, lo.y, hi.x, hi.y};
        };
#pragma unroll
        for (int i = 0; i < 8; ++i) cvrd(0, 0, i);
#pragma unroll
        for (int s = 0; s < 8; ++s) {
            const int cur = s & 1;
            SB();
#pragma unroll
            for (int gm = 0; gm < 8; ++gm) {
#pragma unroll
                for (int be = 0; be <= gm; ++be) cacc[gm] = mfma16(CA[cur][be], CB[cur][gm - be], cacc[gm]);
                SB();
                if (s < 7) cvrd(cur ^ 1, s + 1, gm);
                { const int slot = 8 * s + gm; if (slot < 60) A2[slot / 5][slot % 5] = bld16<SC1>(rsW, (slot % 5 == 4 ? a2v4 : a2v0 + (slot % 5) * 1024), a2s[slot / 5]); }
                SB();
            }
            if (s == 1) {
                WAIT_V(16);
                lds_arrive(LC_B);
            }
        }
        STAMP(p, 6);
        const unsigned sdv = lds_ld<unsigned>((unsigned)(SD_SCR + 256 * w));
        const float Dc = (ch & 1) ? bhi(sdv) : blo(sdv);
#pragma unroll
        for (int gm = 0; gm < 8; ++gm) {
            const int t0 = 256 * gm + 16 * r + 4 * g;
            const v2u kvp = lds_ld<v2u>(XR + 2 * (2044 - t0));
            const float y0 = cacc[gm][0] + bhi(kvp.y) * Dc, y1 = cacc[gm][1] + blo(kvp.y) * Dc, y2 = cacc[gm][2] + bhi(kvp.x) * Dc, y3 = cacc[gm][3] + blo(kvp.x) * Dc;
            lds_st<v2u>(HP + 2 * t0, v2u{pk2(y0, y1), pk2(y2, y3)});
        }
        asm volatile("" ::: "memory");
#pragma unroll
        for (int ps = 0; ps < 4; ++ps) {
            const int n = 64 * ps + lane;
            bst16<SC1>(rsW, (unsigned)(OFF_YT + (ch * SEQ + 8 * n) * 2), 0, lds_ld<v4u>(HP + 16 * n));
        }
    } else {
        if (w == 3) {
            const rsrc_t rsG = mkrs(p.gw, DM * 2);
            const int c16 = lane & 15, row0 = 128 * mt1 + 8 * ft + (lane >> 4);
            v4u gwv[5], xa[5], xb[5];
#pragma unroll
            for (int i = 0; i < 5; ++i) {
                gwv[i] = bld16<0>(rsG, (unsigned)(16 * (c16 + 16 * i)), 0);
                xa[i] = bld16<0>(rsQ, (unsigned)(row0 * DM * 2 + 16 * (c16 + 16 * i)), 0);
                xb[i] = bld16<0>(rsQ, (unsigned)((row0 + 4) * DM * 2 + 16 * (c16 + 16 * i)), 0);
            }
            float s0 = 0.f, s1 = 0.f;
#pragma unroll
            for (int i = 0; i < 5; ++i)
#pragma unroll
                for (int e = 0; e < 4; ++e) { s0 = dot2(xa[i][e], gwv[i][e], s0); s1 = dot2(xb[i][e], gwv[i][e], s1); }
#pragma unroll
            for (int m = 1; m < 16; m <<= 1) { s0 += __shfl_xor(s0, m); s1 += __shfl_xor(s1, m); }
            const float g0 = rb(1.0f / (1.0f + __expf(-rb(s0)))), g1 = rb(1.0f / (1.0f + __expf(-rb(s1))));
            __builtin_amdgcn_raw_buffer_store_b32(__builtin_bit_cast(unsigned, g0), rsW, c16 == 0 ? (unsigned)(OFF_GATE + row0 * 4) : OOB, 0, SC1);
            __builtin_amdgcn_raw_buffer_store_b32(__builtin_bit_cast(unsigned, g1), rsW, c16 == 0 ? (unsigned)(OFF_GATE + (row0 + 4) * 4) : OOB, 0, SC1);
        }
        WAIT_V(0);
        lds_arrive(LC_B);
        lds_wait(LC_B, 4);
        if (w == 3) {
            if (lane == 0) {
                const unsigned old = add_count(p, OFF_CB + 256 * mt1, 1u);
                if (old == 15u) set_flag(p, OFF_RB + 256 * mt1, 0, epoch);
            }
            if (bid == 0) {
#pragma unroll 1
                for (int i = 0; i < 4; ++i) wait_words(rsW, (unsigned)(OFF_FA + 128 * (64 * i + lane)), epoch);
                if (lane == 0) set_flag(p, OFF_EPOCH, 0, epoch);
            }
        }
#pragma unroll
        for (int slot = 0; slot < 60; ++slot) A2[slot / 5][slot % 5] = bld16<SC1>(rsW, (slot % 5 == 4 ? a2v4 : a2v0 + (slot % 5) * 1024), a2s[slot / 5]);
    }
    STAMP(p, 7);

    wait_ready<4>(p, OFF_RB + 256 * (mt2 >> 1), epoch);
    STAMP(p, 8);
    constexpr int W2 = 8;
    unsigned b2off[4];
#pragma unroll
    for (int n = 0; n < 4; ++n) b2off[n] = (unsigned)(OFF_HF + (4 * mt2 + ((w + n) & 3)) * 48 * 1024);
    v4u B2[W2][4];
#pragma unroll
    for (int s = 0; s < W2; ++s)
#pragma unroll
        for (int n = 0; n < 4; ++n) B2[s][n] = bld16<SC1>(rsW, lane * 16, b2off[n] + a2o[s]);
    SB();
    if (has_ch) {
        WAIT_V(32);
        lds_arrive(LC_C);
    }
    f32x4 acc2[5][4];
#pragma unroll
    for (int a = 0; a < 5; ++a)
#pragma unroll
        for (int n = 0; n < 4; ++n) acc2[a][n] = f32x4{0.f, 0.f, 0.f, 0.f};
    const int t = 64 * mt2 + 16 * w + (lane & 15), g4 = lane >> 4;
    v2u qv[5];
    float gt;
#pragma unroll
    for (int s = 0; s < 12; ++s) {
        v4u cb[4];
#pragma unroll
        for (int n = 0; n < 4; ++n) cb[n] = B2[s % W2][n];
        if (s + W2 < 12) {
#pragma unroll
            for (int n = 0; n < 4; ++n) B2[s % W2][n] = bld16<SC1>(rsW, lane * 16, b2off[n] + a2o[s + W2]);
        }
        if (s == 6) {
#pragma unroll
            for (int a = 0; a < 5; ++a) qv[a] = *(const GAS v2u*)(p.q + (size_t)t * DM + 80 * dt + 16 * a + 4 * g4);
            gt = __builtin_bit_cast(float, __builtin_amdgcn_raw_buffer_load_b32(rsW, (unsigned)(OFF_GATE + t * 4), 0, SC1));
        }
#pragma unroll
        for (int n = 0; n < 4; ++n)
#pragma unroll
            for (int a = 0; a < 5; ++a) acc2[a][n] = mfma16(A2[s][a], cb[n], acc2[a][n]);
        SB();
    }
    STAMP(p, 9);
    lds_wait(LC_B, 4);
#pragma unroll
    for (int a = 0; a < 5; ++a)
#pragma unroll
        for (int j = 1; j < 4; ++j) lds_st<f32x4>((unsigned)(G2_RED + ((((w + j) & 3) * 5 + a) * 3 + (j - 1)) * 1024 + lane * 16), acc2[a][j]);
    if (w == 3) {
        lds_wait(LC_C, (unsigned)nch);
        {
            const int c0 = ch - 3, g0 = c0 / 80, n0 = min(nch, 80 * (g0 + 1) - c0), gi = g0 + lane, ni = lane == 0 ? n0 : nch - n0;
            if (lane < 2 && ni > 0) {
                const unsigned old = add_count(p, OFF_CC + 256 * gi, (unsigned)ni);
                if (old + (unsigned)ni == 80u) set_flag(p, OFF_RC + 256 * gi, 0, epoch);
            }
        }
        wait_ready<4>(p, OFF_RC + 256 * dt, epoch);
        v4u yv[10];
#pragma unroll
        for (int it = 0; it < 10; ++it) {
            const int c = lane + 64 * it;
            yv[it] = bld16<SC1>(rsW, (unsigned)(OFF_YT + ((80 * dt + (c >> 3)) * SEQ + 64 * mt2 + 8 * (c & 7)) * 2), 0);
        }
#pragma unroll
        for (int it = 0; it < 10; ++it) lds_st<v4u>((unsigned)(G2_Y + (lane + 64 * it) * 16), yv[it]);
    }
    WAIT_L(0);
    BAR();
    STAMP(p, 10);
    const float omg = rb(1.0f - gt);
    const int q = (lane >> 2) & 3, pp = lane & 3;
#pragma unroll
    for (int a = 0; a < 5; ++a) {
        f32x4 b = acc2[a][0];
#pragma unroll
        for (int j = 0; j < 3; ++j) b += lds_ld<f32x4>((unsigned)(G2_RED + ((w * 5 + a) * 3 + j) * 1024 + lane * 16));
        const v2u y = tr_read((unsigned)(G2_Y + (16 * a + 4 * g4 + q) * 128 + 32 * w + 8 * pp));
        const int d0 = 80 * dt + 16 * a + 4 * g4;
        const float o0 = gt * (blo(qv[a].x) * blo(y.x)) + omg * b[0], o1 = gt * (bhi(qv[a].x) * bhi(y.x)) + omg * b[1];
        const float o2 = gt * (blo(qv[a].y) * blo(y.y)) + omg * b[2], o3 = gt * (bhi(qv[a].y) * bhi(y.y)) + omg * b[3];
        *(GAS v2u*)(p.out + (size_t)t * DM + d0) = v2u{pk2(o0, o1), pk2(o2, o3)};
    }
    STAMP(p, 11);
}

extern "C" void kernel_launch(void* const* d_in, const int* in_sizes, int n_in, void* d_out, int out_size, void* d_ws, size_t ws_size, hipStream_t stream) {
    Params p;
    p.q = (const u16*)d_in[0]; p.k = (const u16*)d_in[1]; p.v = (const u16*)d_in[2]; p.sk = (const u16*)d_in[3];
    p.hk = (const u16*)d_in[4]; p.sd = (const u16*)d_in[5];
    p.wg = (const u16*)d_in[7]; p.wu = (const u16*)d_in[8]; p.wd = (const u16*)d_in[9]; p.gw = (const u16*)d_in[10];
    p.out = (u16*)d_out; p.ws = (unsigned char*)d_ws;
    (void)hipFuncSetAttribute((const void*)k_fused, hipFuncAttributeMaxDynamicSharedMemorySize, LDS_BYTES);
    k_fused<<<256, 256, LDS_BYTES, stream>>>(p);
}
```

```cpp
#define NCOMMON 10
#define MALLPF
#include <hip/hip_runtime.h>
#include <stdint.h>

#define LAS __attribute__((address_space(3)))
#define GAS __attribute__((address_space(1)))
typedef unsigned short u16;
typedef unsigned v4u __attribute__((ext_vector_type(4)));
typedef unsigned v2u __attribute__((ext_vector_type(2)));
typedef __bf16 bf8 __attribute__((ext_vector_type(8)));
typedef __bf16 bf2 __attribute__((ext_vector_type(2)));
typedef short s4 __attribute__((ext_vector_type(4)));
typedef float f32x2 __attribute__((ext_vector_type(2)));
typedef float f32x4 __attribute__((ext_vector_type(4)));
typedef float f32x16 __attribute__((ext_vector_type(16)));
typedef __amdgpu_buffer_rsrc_t rsrc_t;

constexpr int SEQ = 2048, DM = 640, FF = 1536;
constexpr int OFF_EPOCH = 0;
constexpr int OFF_FA = 32768;
constexpr int OFF_CB = 2048;
constexpr int OFF_CC = 8192;
constexpr int OFF_RB = 12288;
constexpr int OFF_RC = 16384;
constexpr int OFF_STAMP = 32 * 1024 * 1024;
constexpr int OFF_KVR = 65536;
constexpr int OFF_WDT = OFF_KVR + DM * SEQ * 2;
constexpr int OFF_HF = OFF_WDT + FF * DM * 2;
constexpr int OFF_YT = OFF_HF + SEQ * FF * 2;
constexpr int OFF_GATE = OFF_YT + DM * SEQ * 2;
constexpr int WS_BYTES = OFF_GATE + SEQ * 4;
constexpr int SC1 = 16;
constexpr unsigned OOB = 0x80000000u;
constexpr unsigned SPIN_MAX = 1u << 16;

struct Params {
    const u16 *q, *k, *v, *sk, *hk, *sd, *wg, *wu, *wd, *gw;
    u16* out;
    unsigned char* ws;
};

__device__ __forceinline__ unsigned pk2(float a, float b) {
    f32x2 x = {a, b};
    return __builtin_bit_cast(unsigned, __builtin_convertvector(x, bf2));
}
__device__ __forceinline__ float blo(unsigned u) { return __builtin_bit_cast(float, u << 16); }
__device__ __forceinline__ float bhi(unsigned u) { return __builtin_bit_cast(float, u & 0xffff0000u); }
__device__ __forceinline__ float rb(float x) { return blo(pk2(x, x)); }
__device__ __forceinline__ f32x4 mfma16(v4u a, v4u b, f32x4 c) {
    return __builtin_amdgcn_mfma_f32_16x16x32_bf16(__builtin_bit_cast(bf8, a), __builtin_bit_cast(bf8, b), c, 0, 0, 0);
}
__device__ __forceinline__ f32x16 mfma32(v4u a, v4u b, f32x16 c) {
    return __builtin_amdgcn_mfma_f32_32x32x16_bf16(__builtin_bit_cast(bf8, a), __builtin_bit_cast(bf8, b), c, 0, 0, 0);
}
__device__ __forceinline__ float dot2(unsigned a, unsigned b, float c) {
    return __builtin_amdgcn_fdot2_f32_bf16(__builtin_bit_cast(bf2, a), __builtin_bit_cast(bf2, b), c, false);
}
template <class T> __device__ __forceinline__ T lds_ld(unsigned a) { return *(LAS T*)(uintptr_t)a; }
template <class T> __device__ __forceinline__ void lds_st(unsigned a, T v) { *(LAS T*)(uintptr_t)a = v; }
__device__ __forceinline__ v2u tr_read(unsigned a) {
    return __builtin_bit_cast(v2u, __builtin_amdgcn_ds_read_tr16_b64_v4i16((LAS s4*)(uintptr_t)a));
}
__device__ __forceinline__ rsrc_t mkrs(const void* p, unsigned bytes) { return __builtin_amdgcn_make_buffer_rsrc((void*)p, 0, bytes, 0x00020000); }
template <int AUX> __device__ __forceinline__ v4u bld16(rsrc_t rs, unsigned voff, unsigned soff) { return __builtin_amdgcn_raw_buffer_load_b128(rs, voff, soff, AUX); }
template <int AUX> __device__ __forceinline__ void bst16(rsrc_t rs, unsigned voff, unsigned soff, v4u v) { __builtin_amdgcn_raw_buffer_store_b128(v, rs, voff, soff, AUX); }
#define WAIT_V(n) asm volatile("s_waitcnt vmcnt(" #n ")" ::: "memory")
#define WAIT_L(n) asm volatile("s_waitcnt lgkmcnt(" #n ")" ::: "memory")
#define BAR() do { asm volatile("" ::: "memory"); __builtin_amdgcn_s_barrier(); asm volatile("" ::: "memory"); } while (0)
#define SB() __builtin_amdgcn_sched_barrier(0)
#define PIN_S(x) asm volatile("" : "+s"(x))
#define PIN_V(x) asm volatile("" : "+v"(x))
#define RLX_AGENT __ATOMIC_RELAXED, __HIP_MEMORY_SCOPE_AGENT
#define STAMP(p, i) do {} while (0)

__device__ __forceinline__ void dma16(rsrc_t rs, unsigned voff, unsigned soff, unsigned lds_addr) {
    asm volatile("s_mov_b32 m0, %0\n\ts_nop 0\n\tbuffer_load_dwordx4 %1, %2, %3 offen lds" ::"s"(lds_addr), "v"(voff), "s"(rs), "s"(soff) : "memory", "m0");
}
__device__ __forceinline__ void dma16_sc1(rsrc_t rs, unsigned voff, unsigned soff, unsigned lds_addr) {
    asm volatile("s_mov_b32 m0, %0\n\ts_nop 0\n\tbuffer_load_dwordx4 %1, %2, %3 offen sc1 lds" ::"s"(lds_addr), "v"(voff), "s"(rs), "s"(soff) : "memory", "m0");
}
#define SET_M0(base, imm) asm volatile("s_add_u32 m0, %0, %1" ::"s"(base), "i"(imm) : "memory", "m0", "scc")
__device__ __forceinline__ void dma16_m0(rsrc_t rs, unsigned voff, unsigned soff) {
    asm volatile("buffer_load_dwordx4 %0, %1, %2 offen lds" ::"v"(voff), "s"(rs), "s"(soff) : "memory");
}
__device__ __forceinline__ void dma16_m0_sc1(rsrc_t rs, unsigned voff, unsigned soff) {
    asm volatile("buffer_load_dwordx4 %0, %1, %2 offen sc1 lds" ::"v"(voff), "s"(rs), "s"(soff) : "memory");
}
__device__ __forceinline__ void aload16(v4u& d, rsrc_t rs, unsigned voff) {
    asm volatile("buffer_load_dwordx4 %0, %1, %2, 0 offen" : "=v"(d) : "v"(voff), "s"(rs) : "memory");
}

__device__ __forceinline__ void wait_words(rsrc_t rsW, unsigned voff, unsigned e) {
    unsigned t0, left = SPIN_MAX;
    asm volatile(
        "1:\n\t"
        "buffer_load_dword %0, %2, %3, 0 offen sc1\n\t"
        "s_waitcnt vmcnt(0)\n\t"
        "v_cmp_ne_u32 vcc, %4, %0\n\t"
        "s_nop 4\n\t"
        "s_cbranch_vccz 2f\n\t"
        "s_sleep 8\n\t"
        "s_sub_u32 %1, %1, 1\n\t"
        "s_cbranch_scc0 1b\n\t"
        "2:"
        : "=&v"(t0), "+s"(left)
        : "v"(voff), "s"(rsW), "s"(e)
        : "memory", "vcc", "scc");
}
__device__ __forceinline__ void dma4(rsrc_t rs, unsigned voff, unsigned lds_addr) {
    asm volatile("s_mov_b32 m0, %0\n\ts_nop 0\n\tbuffer_load_dword %1, %2, 0 offen lds" ::"s"(lds_addr), "v"(voff), "s"(rs) : "memory", "m0");
}
__device__ __forceinline__ void dma4_sc1(rsrc_t rs, unsigned voff, unsigned lds_addr) {
    asm volatile("s_mov_b32 m0, %0\n\ts_nop 0\n\tbuffer_load_dword %1, %2, 0 offen sc1 lds" ::"s"(lds_addr), "v"(voff), "s"(rs) : "memory", "m0");
}
__device__ __forceinline__ unsigned add_count(const Params& p, int off, unsigned n) {
    return __hip_atomic_fetch_add((GAS unsigned*)(p.ws + off), n, RLX_AGENT);
}
template <int SLEEP>
__device__ __forceinline__ void wait_ready(const Params& p, int off, unsigned e) {
    GAS unsigned* c = (GAS unsigned*)(p.ws + off);
    for (unsigned it = 0; it < SPIN_MAX; ++it) {
        if ((unsigned)__builtin_amdgcn_readfirstlane(__hip_atomic_load(c, RLX_AGENT)) == e) break;
        __builtin_amdgcn_s_sleep(SLEEP);
    }
    __builtin_amdgcn_fence(__ATOMIC_ACQUIRE, "wavefront");
}
__device__ __forceinline__ void lds_arrive(unsigned off) {
    if ((threadIdx.x & 63) == 0) (void)__hip_atomic_fetch_add((LAS unsigned*)(uintptr_t)off, 1u, __ATOMIC_RELAXED, __HIP_MEMORY_SCOPE_WORKGROUP);
}
__device__ __forceinline__ void lds_wait(unsigned off, unsigned n) {
    for (unsigned it = 0; it < (SPIN_MAX << 4); ++it) {
        if ((unsigned)__builtin_amdgcn_readfirstlane(__hip_atomic_load((LAS unsigned*)(uintptr_t)off, __ATOMIC_RELAXED, __HIP_MEMORY_SCOPE_WORKGROUP)) >= n) break;
        __builtin_amdgcn_s_sleep(1);
    }
    asm volatile("" ::: "memory");
}
__device__ __forceinline__ void set_flag(const Params& p, int off, int idx, unsigned e) {
    __hip_atomic_store((GAS unsigned*)(p.ws + off) + idx, e, RLX_AGENT);
}

constexpr int G1_STAGE = 40960, G1_G = 16384, G1_U = 16384 + 12288;
constexpr int SCR = 3 * G1_STAGE;
constexpr int PR_K = SCR;
constexpr int PR_V = PR_K + 11264;
constexpr int PR_S = PR_V + 10240;
constexpr int PR_D = PR_S + 1024;
constexpr int CV_RAW = SCR;
constexpr int CV_XR = SCR + 16384, CV_XRS = 4608;
constexpr int FLG_SCR = CV_XR + 3 * CV_XRS;
constexpr int SD_SCR = FLG_SCR + 1024;
constexpr int DUMMY = PR_D + 10240;
constexpr int LC_B = 163808, LC_C = 163824;
constexpr int CV_CS = 4160, CV_HP = 0, CV_HPS = 4 * CV_CS;
constexpr int G2_Y = 3 * CV_HPS;
constexpr int G2_RED = 61440;
constexpr int LDS_BYTES = 163840;
static_assert(SD_SCR + 1024 <= DUMMY && DUMMY + 4096 <= LC_B && G2_Y + 80 * 128 <= G2_RED && G2_RED + 60 * 1024 <= SCR, "lds");
constexpr int CODE_BYTES = 24576;

__global__ void __launch_bounds__(256, 1) k_fused(Params p) {
    extern __shared__ __attribute__((aligned(16))) unsigned char smem[];
    const int tid = threadIdx.x, lane = tid & 63, w = __builtin_amdgcn_readfirstlane(tid >> 6);
    const int bid = blockIdx.x;
    const int xcd = bid & 7, bi = bid >> 3, mq = xcd >> 1, half = xcd & 1;
    STAMP(p, 0);
    {
        unsigned long long pc;
        asm volatile("s_getpc_b64 %0" : "=s"(pc));
        const rsrc_t rsC = mkrs((const void*)(pc & ~255ull), CODE_BYTES - 256);
        dma16(rsC, (unsigned)(((bi + 32 * w) << 10) + lane * 16), 0, DUMMY);
    }
    const int nch = bid < 128 ? 3 : 2, ch = (bid < 128 ? 3 * bid : 384 + 2 * (bid - 128)) + w;
    const bool has_ch = w < nch;
    asm volatile("" ::"s"(p.q), "s"(p.k), "s"(p.v), "s"(p.sk), "s"(p.hk), "s"(p.sd), "s"(p.wg), "s"(p.wu), "s"(p.wd), "s"(p.gw), "s"(p.out), "s"(p.ws));
    rsrc_t rsW = mkrs(p.ws, WS_BYTES);
    rsrc_t rsQ = mkrs(p.q, SEQ * DM * 2);

    const int ptt = bid & 31, pd8 = bid >> 5;
    auto prep_loads = [&]() {
        const rsrc_t rsK = mkrs(p.k, SEQ * DM * 2), rsV = mkrs(p.v, SEQ * DM * 2), rsS = mkrs(p.sk, DM * 4 * 2), rsD = mkrs(p.wd, FF * DM * 2);
        auto kdma = [&](int i) {
            const int c = 64 * i + lane, row = c / 10, col = c - 10 * row, t = 64 * ptt - 4 + row;
            dma16(rsK, (t >= 0 && c < 680) ? (unsigned)((t * DM + 80 * pd8 + 8 * col) * 2) : OOB, 0, PR_K + 1024 * i);
        };
        auto vdma = [&](int i) {
            const int c = 64 * i + lane, row = c / 10, col = c - 10 * row;
            dma16(rsV, (unsigned)(((64 * ptt + row) * DM + 80 * pd8 + 8 * col) * 2), 0, PR_V + 1024 * i);
        };
        auto ddma = [&](int j, int i) {
            const int g = 2 * bid + j, gdt = g / 48, gks = g - 48 * gdt;
            const int c = 64 * i + lane, row = c / 10, col = c - 10 * row;
            dma16(rsD, g < 384 ? (unsigned)(((32 * gks + row) * DM + 80 * gdt + 8 * col) * 2) : OOB, 0, PR_D + 5120 * j + 1024 * i);
        };
        if (w == 0) {
#pragma unroll
            for (int i = 0; i < 8; ++i) kdma(i);
        } else if (w == 1) {
#pragma unroll
            for (int i = 8; i < 11; ++i) kdma(i);
#pragma unroll
            for (int i = 0; i < 5; ++i) vdma(i);
        } else if (w == 2) {
#pragma unroll
            for (int i = 5; i < 10; ++i) vdma(i);
#pragma unroll
            for (int i = 0; i < 5; ++i) ddma(0, i);
        } else {
            dma16(rsS, lane < 40 ? (unsigned)(80 * pd8 * 8 + 16 * lane) : OOB, 0, PR_S);
#pragma unroll
            for (int i = 0; i < 5; ++i) ddma(1, i);
        }
    };
    v4u pkv[3], pwd[5];
    auto prep_compute = [&]() {
        const int i16 = lane & 15, q = (lane >> 2) & 3, pp = lane & 3;
#pragma unroll
        for (int ps = 0; ps < 3; ++ps) {
            const int quad = min(4 * ps + w, 9), cg = quad >> 1, tgp = 4 * (quad & 1) + (lane >> 4);
            const unsigned ka = (unsigned)(PR_K + (8 * tgp + q) * 160 + (16 * cg + 4 * pp) * 2), va = ka + (PR_V - PR_K);
            const v2u k0 = tr_read(ka), k1 = tr_read(ka + 640), k2 = tr_read(ka + 1280), v0 = tr_read(va), v1 = tr_read(va + 640);
            const v2u tw = lds_ld<v2u>((unsigned)(PR_S + (16 * cg + i16) * 8));
            const unsigned W10 = __builtin_amdgcn_alignbit(tw.x, tw.x, 16), W32 = __builtin_amdgcn_alignbit(tw.y, tw.y, 16);
            const unsigned P[6] = {k0.x, k0.y, k1.x, k1.y, k2.x, k2.y};
            unsigned Q[5];
#pragma unroll
            for (int m = 0; m < 5; ++m) Q[m] = __builtin_amdgcn_alignbit(P[m + 1], P[m], 16);
            const unsigned V[4] = {v0.x, v0.y, v1.x, v1.y};
            float r[8];
#pragma unroll
            for (int m = 0; m < 4; ++m) {
                r[2 * m] = dot2(Q[m], W32, dot2(Q[m + 1], W10, 0.f)) * blo(V[m]);
                r[2 * m + 1] = dot2(P[m + 1], W32, dot2(P[m + 2], W10, 0.f)) * bhi(V[m]);
            }
            pkv[ps] = v4u{pk2(r[7], r[6]), pk2(r[5], r[4]), pk2(r[3], r[2]), pk2(r[1], r[0])};
        }
        const unsigned T = (unsigned)(PR_D + 5120 * (w & 1));
        const int gq = lane >> 4, row = 16 * (gq >> 1) + 4 * (gq & 1) + q;
#pragma unroll
        for (int at = 0; at < 5; ++at) {
            const v2u lo = tr_read(T + row * 160 + (16 * at + 4 * pp) * 2);
            const v2u hi = tr_read(T + (row + 8) * 160 + (16 * at + 4 * pp) * 2);
            pwd[at] = v4u{lo.x, lo.y, hi.x, hi.y};
        }
    };
    prep_loads();
    STAMP(p, 14);

    const int mt1 = 4 * mq + (bi & 3), ft = 8 * half + (bi >> 2);
    const int wm = w & 1, wf = w >> 1;
    constexpr int NC = NCOMMON, NR = 10 - NC;
    const int rot1 = NR > 0 ? ((ft & 7) + ((NR * (mt1 & 3)) >> 2)) % (NR > 0 ? NR : 1) : 0;
    rsrc_t rsWg = mkrs(p.wg, DM * FF * 2), rsWu = mkrs(p.wu, DM * FF * 2);
    PIN_S(rsW); PIN_S(rsWg); PIN_S(rsWu); PIN_S(rsQ);
    unsigned xoff[4], goff[3], uoff[3];
#pragma unroll
    for (int n = 0; n < 4; ++n) {
        const int row = 32 * w + 8 * n + (lane >> 3);
        xoff[n] = (unsigned)((128 * mt1 + row) * 1280 + (((lane & 7) ^ ((4 * n + (lane >> 4)) & 7)) << 4));
    }
#pragma unroll
    for (int n = 0; n < 3; ++n) {
        const int Lc = 64 * (3 * w + n) + lane, k = Lc / 12, c = Lc - 12 * k;
        goff[n] = (unsigned)(k * 3072 + 192 * ft + 16 * c);
        uoff[n] = (unsigned)(k * 3072 + 192 * ft + 16 * (c ^ 2));
    }
    auto g1_issue = [&](int kt, int stg) {
#pragma unroll
        for (int n = 0; n < 4; ++n) dma16(rsQ, xoff[n], kt * 128, stg + (4 * w + n) * 1024);
#pragma unroll
        for (int n = 0; n < 3; ++n) {
            dma16(rsWg, goff[n], kt * 64 * 3072, stg + G1_G + (3 * w + n) * 1024);
            dma16(rsWu, uoff[n], kt * 64 * 3072, stg + G1_U + (3 * w + n) * 1024);
        }
    };
    g1_issue(0, 0);
#pragma unroll
    for (int i = 0; i < 7; ++i) {
        const int c = 4 * bid + w + 1024 * i;
        if (c < 6400) dma16(c < 2560 ? rsQ : c < 4480 ? rsWg : rsWu, (unsigned)(lane * 16), (unsigned)(c < 2560 ? c : c < 4480 ? c - 2560 : c - 4480) * 1024u, DUMMY);
    }
#define NPF (w < 1 && bid < 64 ? 7 : 6)
    const unsigned epoch = *(const __attribute__((address_space(4))) unsigned*)(uintptr_t)(p.ws + OFF_EPOCH) + 1u;
    f32x16 acc[3][2];
#pragma unroll
    for (int a = 0; a < 3; ++a)
#pragma unroll
        for (int b = 0; b < 2; ++b)
#pragma unroll
            for (int r = 0; r < 16; ++r) acc[a][b][r] = 0.f;
    const int l31 = lane & 31, hh = lane >> 5;
    const int mt2 = 8 * mq + (bi & 7), dt = 4 * half + (bi >> 3);
    unsigned favo;
    {
        const int c0 = ch - w, g0 = c0 / 80;
        int who = 24 * dt + min(max(lane - 32, 0), 23);
        if (lane < 32 && has_ch) who = 32 * (ch / 80) + lane;
        if (lane == 56) who = mt1;
        if (lane == 57) who = 16 + g0;
        if (lane == 58) who = 16 + min(g0 + 1, 7);
        favo = (unsigned)(OFF_FA + 128 * who);
    }
    const int rot2 = (3 * (bi & 7) / 2 + 3 * (bi >> 3)) % 12;
    unsigned a2o[12];
#pragma unroll
    for (int s = 0; s < 12; ++s) { const int ks = s + rot2; a2o[s] = (unsigned)(12 * w + (ks >= 12 ? ks - 12 : ks)) * 1024u; }
    const unsigned a2base = (unsigned)(OFF_WDT + dt * 48 * 5 * 1024);
    const int brow = 64 * wm + l31;
    unsigned ba[4];
#pragma unroll
    for (int kk = 0; kk < 4; ++kk) ba[kk] = (unsigned)(brow * 128 + (((2 * kk + hh) ^ ((brow >> 1) & 7)) << 4));
    unsigned aa[3];
#pragma unroll
    for (int fb = 0; fb < 3; ++fb) {
        const int fbg = 3 * wf + fb, G = lane >> 4;
        aa[fb] = (unsigned)(((G & 1) ? G1_U + (fbg ^ 1) * 32 : G1_G + fbg * 32) + (8 * (G >> 1) + ((lane >> 2) & 3)) * 192 + 8 * (lane & 3));
    }
    unsigned okv[3], owd, ozero, ofa, oraw, osd, okvr, oh[2];
    {
        const int i16 = lane & 15;
#pragma unroll
        for (int ps = 0; ps < 3; ++ps) {
            const int quad = min(4 * ps + w, 9), cg = quad >> 1, tgp = 4 * (quad & 1) + (lane >> 4);
            okv[ps] = 4 * ps + w < 10 ? (unsigned)(OFF_KVR + ((80 * pd8 + 16 * cg + i16) * SEQ + 2040 - 64 * ptt - 8 * tgp) * 2) : OOB;
        }
        const int g = 2 * bid + (w & 1), gdt = g / 48, gks = g - 48 * gdt;
        owd = (w >= 2 && g < 384) ? (unsigned)(OFF_WDT + (gdt * 48 + gks) * 5 * 1024 + lane * 16) : OOB;
        ozero = (bid < 24 && tid == 0) ? (unsigned)(bid < 16 ? OFF_CB + 256 * bid : OFF_CC + 256 * (bid - 16)) : OOB;
        ofa = tid == 0 ? (unsigned)(OFF_FA + 128 * bid) : OOB;
        oraw = has_ch ? (unsigned)(ch * SEQ * 2 + lane * 16) : OOB;
        osd = has_ch ? (unsigned)((ch & ~1) * 2) : OOB;
        okvr = has_ch ? (unsigned)(OFF_KVR + ch * SEQ * 2 + lane * 16) : OOB;
#pragma unroll
        for (int mt = 0; mt < 2; ++mt) oh[mt] = (unsigned)(OFF_HF + ((128 * mt1 + 64 * wm + 32 * mt + l31) >> 4) * 48 * 1024 + ((lane & 15) + 16 * hh) * 16);
    }
    unsigned a2v0 = (unsigned)(lane * 16), a2v4 = a2v0 + 4096;
    PIN_V(favo); PIN_V(okv[0]); PIN_V(okv[1]); PIN_V(okv[2]); PIN_V(owd); PIN_V(ozero); PIN_V(ofa); PIN_V(oraw); PIN_V(osd); PIN_V(okvr); PIN_V(oh[0]); PIN_V(oh[1]);
    PIN_V(ba[0]); PIN_V(ba[1]); PIN_V(ba[2]); PIN_V(ba[3]); PIN_V(aa[0]); PIN_V(aa[1]); PIN_V(aa[2]); PIN_V(a2v0); PIN_V(a2v4);
    rsrc_t rsH = mkrs(p.hk, DM * SEQ * 2), rsD1 = mkrs(p.sd, DM * 2);
    PIN_S(rsH); PIN_S(rsD1);
    STAMP(p, 1);
    WAIT_V(16);
    BAR();
    STAMP(p, 15);
    int kap = NC > 1 ? 1 : 1 + rot1;
    int kcnt = 1;
    auto knext = [&]() { ++kcnt; kap = kcnt < NC ? kcnt : kcnt == NC ? NC + rot1 : kap == 9 ? NC : kap + 1; };
    g1_issue(kap, G1_STAGE);
    knext();
    prep_compute();
    STAMP(p, 16);

    auto prep_store = [&]() {
        __builtin_amdgcn_raw_buffer_store_b32(0u, rsW, ozero, 0, SC1);
#pragma unroll
        for (int ps = 0; ps < 3; ++ps) bst16<SC1>(rsW, okv[ps], 0, pkv[ps]);
#pragma unroll
        for (int at = 0; at < 5; ++at) bst16<SC1>(rsW, owd, at * 1024, pwd[at]);
    };

    {
        v4u fb0[2], fb1[2];
        v2u fa0[3][2], fa1[3][2];
#define RDB(d, kk, mt) d = lds_ld<v4u>(ba[kk] + (mt) * 4096)
#define RDA(d, kk, fb, hi) d[fb][hi] = tr_read(aa[fb] + (kk) * 16 * 192 + (hi) * 4 * 192)
#define MM(A, B, fb, mt) acc[fb][mt] = mfma32(v4u{A[fb][0].x, A[fb][0].y, A[fb][1].x, A[fb][1].y}, B[mt], acc[fb][mt])
        WAIT_V(16);
        WAIT_L(0);
        BAR();
        STAMP(p, 2);
        RDB(fb0[0], 0, 0); RDA(fa0, 0, 0, 0); RDA(fa0, 0, 0, 1); RDB(fb0[1], 0, 1); RDA(fa0, 0, 1, 0); RDA(fa0, 0, 1, 1); RDA(fa0, 0, 2, 0); RDA(fa0, 0, 2, 1);
        int st = 0;
        unsigned mx = 2 * G1_STAGE + 4096u * w, mg = 2 * G1_STAGE + G1_G + 3072u * w, mu = 2 * G1_STAGE + G1_U + 3072u * w;
        unsigned sx = kap * 128, sw = kap * 64 * 3072;
#define DBGS(i)
#define STEP(FA, FB, GA, GB, kn, D0, D1, D2)                                                     \
            MM(FA, FB, 0, 0); SB(); RDB(GB[0], kn, 0); RDA(GA, kn, 0, 0); SB();                 \
            MM(FA, FB, 0, 1); SB(); RDA(GA, kn, 0, 1); RDB(GB[1], kn, 1); SB();                 \
            MM(FA, FB, 1, 0); SB(); RDA(GA, kn, 1, 0); RDA(GA, kn, 1, 1); D0##_M; SB();         \
            MM(FA, FB, 1, 1); SB(); D0##_L; RDA(GA, kn, 2, 0); D1##_M; SB();                    \
            MM(FA, FB, 2, 0); SB(); D1##_L; RDA(GA, kn, 2, 1); D2##_M; SB();                    \
            MM(FA, FB, 2, 1); SB(); D2##_L; SB();
#define X0_M SET_M0(mx, 0)
#define X0_L dma16_m0(rsQ, xoff[0], sx)
#define X1_M SET_M0(mx, 1024)
#define X1_L dma16_m0(rsQ, xoff[1], sx)
#define X2_M SET_M0(mx, 2048)
#define X2_L dma16_m0(rsQ, xoff[2], sx)
#define X3_M SET_M0(mx, 3072)
#define X3_L dma16_m0(rsQ, xoff[3], sx)
#define G0_M SET_M0(mg, 0)
#define G0_L dma16_m0(rsWg, goff[0], sw)
#define G1_M SET_M0(mg, 1024)
#define G1_L dma16_m0(rsWg, goff[1], sw)
#define G2_M SET_M0(mg, 2048)
#define G2_L dma16_m0(rsWg, goff[2], sw)
#define U0_M SET_M0(mu, 0)
#define U0_L dma16_m0(rsWu, uoff[0], sw)
#define U1_M SET_M0(mu, 1024)
#define U1_L dma16_m0(rsWu, uoff[1], sw)
#define U2_M SET_M0(mu, 2048)
#define U2_L dma16_m0(rsWu, uoff[2], sw)
#define KBODY(WAITV) { \
            const int dlt = (st == 2 * G1_STAGE) ? -2 * G1_STAGE : G1_STAGE; \
            SB(); \
            STEP(fa0, fb0, fa1, fb1, 1, X0, X1, X2) \
            DBGS(28) \
            STEP(fa1, fb1, fa0, fb0, 2, X3, G0, U0) \
            STEP(fa0, fb0, fa1, fb1, 3, G1, U1, G2) \
            DBGS(29) \
            U2_M; SB(); \
            MM(fa1, fb1, 0, 0); SB(); U2_L; ba[0] += dlt; aa[0] += dlt; SB(); \
            MM(fa1, fb1, 0, 1); SB(); aa[1] += dlt; aa[2] += dlt; \
            knext(); \
            mx = (unsigned)st + 4096u * w; mg = (unsigned)st + G1_G + 3072u * w; mu = (unsigned)st + G1_U + 3072u * w; sx = kap * 128; sw = kap * 64 * 3072; \
            SB(); \
            MM(fa1, fb1, 1, 0); SB(); \
            WAIT_L(0); \
            DBGS(12) \
            WAITV; \
            DBGS(13) \
            BAR(); \
            DBGS(17) \
            RDB(fb0[0], 0, 0); RDA(fa0, 0, 0, 0); RDA(fa0, 0, 0, 1); SB(); \
            MM(fa1, fb1, 1, 1); SB(); RDB(fb0[1], 0, 1); RDA(fa0, 0, 1, 0); RDA(fa0, 0, 1, 1); SB(); \
            MM(fa1, fb1, 2, 0); SB(); RDA(fa0, 0, 2, 0); RDA(fa0, 0, 2, 1); ba[1] += dlt; SB(); \
            MM(fa1, fb1, 2, 1); SB(); ba[2] += dlt; ba[3] += dlt; \
            st += dlt; \
            }
#pragma unroll 1
        for (int kt = 0; kt < 10; ++kt) {
            KBODY(if (__builtin_expect((0x24A >> kt) & 1, 0)) { if (kt == 1) WAIT_V(19); else if (kt == 3) WAIT_V(16); else if (kt == 6) WAIT_V(14); else WAIT_V(20); } else WAIT_V(10))
            if (__builtin_expect(kt == 7, 0)) {
                sx = 0x40000000u; mx = DUMMY; mg = DUMMY; mu = DUMMY;
                rsWg = rsW; rsWu = rsW;
#pragma unroll
                for (int n = 0; n < 3; ++n) { goff[n] = a2v0 + 2048 * n; uoff[n] = a2v0 + 2048 * n + 1024; }
                sw = a2base + a2o[0] * 5;
            }
            if (__builtin_expect(kt == 8, 0)) {
                sx = 0x40000000u; mx = DUMMY; mg = DUMMY; mu = DUMMY; sw = a2base + a2o[1] * 5 + 1024;
            }
            if (__builtin_expect((0x135 >> kt) & 1, 0)) {
                if (kt == 0) {
                    prep_store();
                }
                else if (kt == 2) {
                    __builtin_amdgcn_raw_buffer_store_b32(epoch, rsW, ofa, 0, SC1);
#pragma unroll
                    for (int n = 0; n < 4; ++n) dma16(rsH, oraw, n * 1024, CV_RAW + 4096u * w + 1024u * n);
                    dma4(rsD1, osd, SD_SCR + 256u * w);
                } else if (kt == 4) dma4_sc1(rsW, favo, FLG_SCR + 256u * w);
                else if (kt == 5) {
                    const unsigned flv = lds_ld<unsigned>((unsigned)(FLG_SCR + 256 * w + lane * 4));
                    if (__builtin_amdgcn_ballot_w64(flv != epoch) != 0ull) wait_words(rsW, favo, epoch);
#pragma unroll
                    for (int n = 0; n < 4; ++n) dma16_sc1(rsW, okvr, n * 1024, (has_ch ? (unsigned)(CV_XR + CV_XRS * w) : (unsigned)DUMMY) + n * 1024);
                } else {
                    if (tid == 0) { lds_st<unsigned>(LC_B, 0u); lds_st<unsigned>(LC_C, 0u); }
                }
            }
        }
#undef MM
#undef STEP
#undef KBODY
    }
    STAMP(p, 3);
    v4u A2[12][5];
    unsigned a2s[12];
#pragma unroll
    for (int s = 0; s < 12; ++s) a2s[s] = a2base + a2o[s] * 5;
#pragma unroll
    for (int fb = 0; fb < 3; ++fb)
#pragma unroll
        for (int mt = 0; mt < 2; ++mt) {
            const f32x16& a = acc[fb][mt];
            v4u o;
#pragma unroll
            for (int j = 0; j < 4; ++j) o[j] = pk2(a[2 * j] * a[8 + 2 * j], a[2 * j + 1] * a[8 + 2 * j + 1]);
            const int Ft = 6 * ft + 3 * wf + fb;
            bst16<SC1>(rsW, oh[mt], (unsigned)((Ft >> 1) * 1024 + (Ft & 1) * 512), o);
        }
    STAMP(p, 4);

    if (has_ch) {
        const unsigned HP = (unsigned)(CV_HP + w * CV_HPS), XR = (unsigned)(CV_XR + w * CV_XRS), RAW = (unsigned)(CV_RAW + w * 4096);
        v4u c0[5], c1[5];
#pragma unroll
        for (int ps = 0; ps < 5; ++ps) {
            const int n = 64 * ps + lane;
            c0[ps] = lds_ld<v4u>(RAW + 16 * (ps == 0 ? max(n - 2, 0) : ps < 4 ? n - 2 : 254 + (lane & 1)));
            c1[ps] = lds_ld<v4u>(RAW + 16 * (ps == 0 ? max(n - 1, 0) : ps < 4 ? n - 1 : 255));
        }
        if (lane < 2) c0[0] = v4u{0, 0, 0, 0};
        if (lane < 1) c1[0] = v4u{0, 0, 0, 0};
        if (lane & 1) c1[4] = v4u{0, 0, 0, 0};
#pragma unroll
        for (int ps = 0; ps < 5; ++ps) {
            const int n = 64 * ps + lane;
            const unsigned Dw[6] = {c0[ps].x, c0[ps].y, c0[ps].z, c0[ps].w, c1[ps].x, c1[ps].y};
            v4u o0, o1, o2, o3;
#pragma unroll
            for (int q = 0; q < 4; ++q) {
                o0[q] = __builtin_amdgcn_alignbit(Dw[q + 1], Dw[q], 16);
                o1[q] = Dw[q + 1];
                o2[q] = __builtin_amdgcn_alignbit(Dw[q + 2], Dw[q + 1], 16);
                o3[q] = Dw[q + 2];
            }
            if (ps < 4 || lane < 2) {
                lds_st<v4u>(HP + 0 * CV_CS + 16 * n, o0);
                lds_st<v4u>(HP + 1 * CV_CS + 16 * n, o1);
                lds_st<v4u>(HP + 2 * CV_CS + 16 * n, o2);
                lds_st<v4u>(HP + 3 * CV_CS + 16 * n, o3);
            }
        }
        if (lane < 32) lds_st<v4u>(XR + 4096 + 16 * lane, v4u{0, 0, 0, 0});
        asm volatile("" ::: "memory");
        STAMP(p, 5);

        const int r = lane & 15, g = lane >> 4;
        unsigned ap0 = HP + (unsigned)((r & 3) * CV_CS + 2 * ((r & ~3) + 8 * g));
        unsigned ap1 = ap0 + 8;
        PIN_V(ap0); PIN_V(ap1);
        unsigned bp = XR + (unsigned)(2 * (240 - 16 * r + 8 * g));
        PIN_V(bp);
        f32x4 cacc[8];
#pragma unroll
        for (int i = 0; i < 8; ++i) cacc[i] = f32x4{0.f, 0.f, 0.f, 0.f};
        v4u CA[2][8], CB[2][8];
        auto cvrd = [&](int set, int s, int i) {
            CB[set][i] = lds_ld<v4u>(bp + 512 * (7 - i) + 64 * s);
            const v2u lo = lds_ld<v2u>(ap0 + 512 * i + 64 * s), hi = lds_ld<v2u>(ap1 + 512 * i + 64 * s);
            CA[set][i] = v4u{lo.x, lo.y, hi.x, hi.y};
        };
#pragma unroll
        for (int i = 0; i < 8; ++i) cvrd(0, 0, i);
#pragma unroll
        for (int s = 0; s < 8; ++s) {
            const int cur = s & 1;
            SB();
#pragma unroll
            for (int gm = 0; gm < 8; ++gm) {
#pragma unroll
                for (int be = 0; be <= gm; ++be) cacc[gm] = mfma16(CA[cur][be], CB[cur][gm - be], cacc[gm]);
                SB();
                if (s < 7) cvrd(cur ^ 1, s + 1, gm);
                { const int slot = 8 * s + gm; if (slot < 60) A2[slot / 5][slot % 5] = bld16<SC1>(rsW, (slot % 5 == 4 ? a2v4 : a2v0 + (slot % 5) * 1024), a2s[slot / 5]); }
                SB();
            }
            if (s == 1) {
                WAIT_V(16);
                lds_arrive(LC_B);
            }
        }
        STAMP(p, 6);
        const unsigned sdv = lds_ld<unsigned>((unsigned)(SD_SCR + 256 * w));
        const float Dc = (ch & 1) ? bhi(sdv) : blo(sdv);
#pragma unroll
        for (int gm = 0; gm < 8; ++gm) {
            const int t0 = 256 * gm + 16 * r + 4 * g;
            const v2u kvp = lds_ld<v2u>(XR + 2 * (2044 - t0));
            const float y0 = cacc[gm][0] + bhi(kvp.y) * Dc, y1 = cacc[gm][1] + blo(kvp.y) * Dc, y2 = cacc[gm][2] + bhi(kvp.x) * Dc, y3 = cacc[gm][3] + blo(kvp.x) * Dc;
            lds_st<v2u>(HP + 2 * t0, v2u{pk2(y0, y1), pk2(y2, y3)});
        }
        asm volatile("" ::: "memory");
#pragma unroll
        for (int ps = 0; ps < 4; ++ps) {
            const int n = 64 * ps + lane;
            bst16<SC1>(rsW, (unsigned)(OFF_YT + (ch * SEQ + 8 * n) * 2), 0, lds_ld<v4u>(HP + 16 * n));
        }
    } else {
        if (w == 3) {
            const rsrc_t rsG = mkrs(p.gw, DM * 2);
            const int c16 = lane & 15, row0 = 128 * mt1 + 8 * ft + (lane >> 4);
            v4u gwv[5], xa[5], xb[5];
#pragma unroll
            for (int i = 0; i < 5; ++i) {
                gwv[i] = bld16<0>(rsG, (unsigned)(16 * (c16 + 16 * i)), 0);
                xa[i] = bld16<0>(rsQ, (unsigned)(row0 * DM * 2 + 16 * (c16 + 16 * i)), 0);
                xb[i] = bld16<0>(rsQ, (unsigned)((row0 + 4) * DM * 2 + 16 * (c16 + 16 * i)), 0);
            }
            float s0 = 0.f, s1 = 0.f;
#pragma unroll
            for (int i = 0; i < 5; ++i)
#pragma unroll
                for (int e = 0; e < 4; ++e) { s0 = dot2(xa[i][e], gwv[i][e], s0); s1 = dot2(xb[i][e], gwv[i][e], s1); }
#pragma unroll
            for (int m = 1; m < 16; m <<= 1) { s0 += __shfl_xor(s0, m); s1 += __shfl_xor(s1, m); }
            const float g0 = rb(1.0f / (1.0f + __expf(-rb(s0)))), g1 = rb(1.0f / (1.0f + __expf(-rb(s1))));
            __builtin_amdgcn_raw_buffer_store_b32(__builtin_bit_cast(unsigned, g0), rsW, c16 == 0 ? (unsigned)(OFF_GATE + row0 * 4) : OOB, 0, SC1);
            __builtin_amdgcn_raw_buffer_store_b32(__builtin_bit_cast(unsigned, g1), rsW, c16 == 0 ? (unsigned)(OFF_GATE + (row0 + 4) * 4) : OOB, 0, SC1);
        }
        WAIT_V(0);
        lds_arrive(LC_B);
        lds_wait(LC_B, 4);
        if (w == 3) {
            if (lane == 0) {
                const unsigned old = add_count(p, OFF_CB + 256 * mt1, 1u);
                if (old == 15u) set_flag(p, OFF_RB + 256 * mt1, 0, epoch);
            }
            if (bid == 0) {
#pragma unroll 1
                for (int i = 0; i < 4; ++i) wait_words(rsW, (unsigned)(OFF_FA + 128 * (64 * i + lane)), epoch);
                if (lane == 0) set_flag(p, OFF_EPOCH, 0, epoch);
            }
        }
#pragma unroll
        for (int slot = 0; slot < 60; ++slot) A2[slot / 5][slot % 5] = bld16<SC1>(rsW, (slot % 5 == 4 ? a2v4 : a2v0 + (slot % 5) * 1024), a2s[slot / 5]);
    }
    STAMP(p, 7);

    wait_ready<4>(p, OFF_RB + 256 * (mt2 >> 1), epoch);
    STAMP(p, 8);
    constexpr int W2 = 8;
    unsigned b2off[4];
#pragma unroll
    for (int n = 0; n < 4; ++n) b2off[n] = (unsigned)(OFF_HF + (4 * mt2 + ((w + n) & 3)) * 48 * 1024);
    v4u B2[W2][4];
#pragma unroll
    for (int s = 0; s < W2; ++s)
#pragma unroll
        for (int n = 0; n < 4; ++n) B2[s][n] = bld16<SC1>(rsW, lane * 16, b2off[n] + a2o[s]);
    SB();
    if (has_ch) {
        WAIT_V(32);
        lds_arrive(LC_C);
    }
    f32x4 acc2[5][4];
#pragma unroll
    for (int a = 0; a < 5; ++a)
#pragma unroll
        for (int n = 0; n < 4; ++n) acc2[a][n] = f32x4{0.f, 0.f, 0.f, 0.f};
    const int t = 64 * mt2 + 16 * w + (lane & 15), g4 = lane >> 4;
    v2u qv[5];
    float gt;
#pragma unroll
    for (int s = 0; s < 12; ++s) {
        v4u cb[4];
#pragma unroll
        for (int n = 0; n < 4; ++n) cb[n] = B2[s % W2][n];
        if (s + W2 < 12) {
#pragma unroll
            for (int n = 0; n < 4; ++n) B2[s % W2][n] = bld16<SC1>(rsW, lane * 16, b2off[n] + a2o[s + W2]);
        }
        if (s == 6) {
#pragma unroll
            for (int a = 0; a < 5; ++a) qv[a] = *(const GAS v2u*)(p.q + (size_t)t * DM + 80 * dt + 16 * a + 4 * g4);
            gt = __builtin_bit_cast(float, __builtin_amdgcn_raw_buffer_load_b32(rsW, (unsigned)(OFF_GATE + t * 4), 0, SC1));
        }
#pragma unroll
        for (int n = 0; n < 4; ++n)
#pragma unroll
            for (int a = 0; a < 5; ++a) acc2[a][n] = mfma16(A2[s][a], cb[n], acc2[a][n]);
        SB();
    }
    STAMP(p, 9);
    lds_wait(LC_B, 4);
#pragma unroll
    for (int a = 0; a < 5; ++a)
#pragma unroll
        for (int j = 1; j < 4; ++j) lds_st<f32x4>((unsigned)(G2_RED + ((((w + j) & 3) * 5 + a) * 3 + (j - 1)) * 1024 + lane * 16), acc2[a][j]);
    if (w == 3) {
        lds_wait(LC_C, (unsigned)nch);
        {
            const int c0 = ch - 3, g0 = c0 / 80, n0 = min(nch, 80 * (g0 + 1) - c0), gi = g0 + lane, ni = lane == 0 ? n0 : nch - n0;
            if (lane < 2 && ni > 0) {
                const unsigned old = add_count(p, OFF_CC + 256 * gi, (unsigned)ni);
                if (old + (unsigned)ni == 80u) set_flag(p, OFF_RC + 256 * gi, 0, epoch);
            }
        }
        wait_ready<4>(p, OFF_RC + 256 * dt, epoch);
        v4u yv[10];
#pragma unroll
        for (int it = 0; it < 10; ++it) {
            const int c = lane + 64 * it;
            yv[it] = bld16<SC1>(rsW, (unsigned)(OFF_YT + ((80 * dt + (c >> 3)) * SEQ + 64 * mt2 + 8 * (c & 7)) * 2), 0);
        }
#pragma unroll
        for (int it = 0; it < 10; ++it) lds_st<v4u>((unsigned)(G2_Y + (lane + 64 * it) * 16), yv[it]);
    }
    WAIT_L(0);
    BAR();
    const float omg = rb(1.0f - gt);
    const int q = (lane >> 2) & 3, pp = lane & 3;
#pragma unroll
    for (int a = 0; a < 5; ++a) {
        f32x4 b = acc2[a][0];
#pragma unroll
        for (int j = 0; j < 3; ++j) b += lds_ld<f32x4>((unsigned)(G2_RED + ((w * 5 + a) * 3 + j) * 1024 + lane * 16));
        const v2u y = tr_read((unsigned)(G2_Y + (16 * a + 4 * g4 + q) * 128 + 32 * w + 8 * pp));
        const int d0 = 80 * dt + 16 * a + 4 * g4;
        const float o0 = gt * (blo(qv[a].x) * blo(y.x)) + omg * b[0], o1 = gt * (bhi(qv[a].x) * bhi(y.x)) + omg * b[1];
        const float o2 = gt * (blo(qv[a].y) * blo(y.y)) + omg * b[2], o3 = gt * (bhi(qv[a].y) * bhi(y.y)) + omg * b[3];
        *(GAS v2u*)(p.out + (size_t)t * DM + d0) = v2u{pk2(o0, o1), pk2(o2, o3)};
    }
    STAMP(p, 11);
}

extern "C" void kernel_launch(void* const* d_in, const int* in_sizes, int n_in, void* d_out, int out_size, void* d_ws, size_t ws_size, hipStream_t stream) {
    Params p;
    p.q = (const u16*)d_in[0]; p.k = (const u16*)d_in[1]; p.v = (const u16*)d_in[2]; p.sk = (const u16*)d_in[3];
    p.hk = (const u16*)d_in[4]; p.sd = (const u16*)d_in[5];
    p.wg = (const u16*)d_in[7]; p.wu = (const u16*)d_in[8]; p.wd = (const u16*)d_in[9]; p.gw = (const u16*)d_in[10];
    p.out = (u16*)d_out; p.ws = (unsigned char*)d_ws;
    (void)hipFuncSetAttribute((const void*)k_fused, hipFuncAttributeMaxDynamicSharedMemorySize, LDS_BYTES);
    k_fused<<<256, 256, LDS_BYTES, stream>>>(p);
}
```

```cpp
#include <hip/hip_runtime.h>
#include <stdint.h>

#define LAS __attribute__((address_space(3)))
#define GAS __attribute__((address_space(1)))
typedef unsigned short u16;
typedef unsigned v4u __attribute__((ext_vector_type(4)));
typedef unsigned v2u __attribute__((ext_vector_type(2)));
typedef __bf16 bf8 __attribute__((ext_vector_type(8)));
typedef __bf16 bf2 __attribute__((ext_vector_type(2)));
typedef short s4 __attribute__((ext_vector_type(4)));
typedef float f32x2 __attribute__((ext_vector_type(2)));
typedef float f32x4 __attribute__((ext_vector_type(4)));
typedef float f32x16 __attribute__((ext_vector_type(16)));
typedef __amdgpu_buffer_rsrc_t rsrc_t;

constexpr int SEQ = 2048, DM = 640, FF = 1536;
constexpr int OFF_EPOCH = 0;
constexpr int OFF_FA = 32768;
constexpr int OFF_CB = 2048;
constexpr int OFF_CC = 8192;
constexpr int OFF_RB = 12288;
constexpr int OFF_RC = 16384;
constexpr int OFF_STAMP = 32 * 1024 * 1024;
constexpr int OFF_KVR = 65536;
constexpr int OFF_WDT = OFF_KVR + DM * SEQ * 2;
constexpr int OFF_HF = OFF_WDT + FF * DM * 2;
constexpr int OFF_YT = OFF_HF + SEQ * FF * 2;
constexpr int OFF_GATE = OFF_YT + DM * SEQ * 2;
constexpr int WS_BYTES = OFF_GATE + SEQ * 4;
constexpr int SC1 = 16;
constexpr unsigned OOB = 0x80000000u;
constexpr unsigned SPIN_MAX = 1u << 16;

struct Params {
    const u16 *q, *k, *v, *sk, *hk, *sd, *wg, *wu, *wd, *gw;
    u16* out;
    unsigned char* ws;
};

__device__ __forceinline__ unsigned pk2(float a, float b) {
    f32x2 x = {a, b};
    return __builtin_bit_cast(unsigned, __builtin_convertvector(x, bf2));
}
__device__ __forceinline__ float blo(unsigned u) { return __builtin_bit_cast(float, u << 16); }
__device__ __forceinline__ float bhi(unsigned u) { return __builtin_bit_cast(float, u & 0xffff0000u); }
__device__ __forceinline__ float rb(float x) { return blo(pk2(x, x)); }
__device__ __forceinline__ f32x4 mfma16(v4u a, v4u b, f32x4 c) {
    return __builtin_amdgcn_mfma_f32_16x16x32_bf16(__builtin_bit_cast(bf8, a), __builtin_bit_cast(bf8, b), c, 0, 0, 0);
}
__device__ __forceinline__ f32x16 mfma32(v4u a, v4u b, f32x16 c) {
    return __builtin_amdgcn_mfma_f32_32x32x16_bf16(__builtin_bit_cast(bf8, a), __builtin_bit_cast(bf8, b), c, 0, 0, 0);
}
__device__ __forceinline__ float dot2(unsigned a, unsigned b, float c) {
    return __builtin_amdgcn_fdot2_f32_bf16(__builtin_bit_cast(bf2, a), __builtin_bit_cast(bf2, b), c, false);
}
template <class T> __device__ __forceinline__ T lds_ld(unsigned a) { return *(LAS T*)(uintptr_t)a; }
template <class T> __device__ __forceinline__ void lds_st(unsigned a, T v) { *(LAS T*)(uintptr_t)a = v; }
__device__ __forceinline__ v2u tr_read(unsigned a) {
    return __builtin_bit_cast(v2u, __builtin_amdgcn_ds_read_tr16_b64_v4i16((LAS s4*)(uintptr_t)a));
}
__device__ __forceinline__ rsrc_t mkrs(const void* p, unsigned bytes) { return __builtin_amdgcn_make_buffer_rsrc((void*)p, 0, bytes, 0x00020000); }
template <int AUX> __device__ __forceinline__ v4u bld16(rsrc_t rs, unsigned voff, unsigned soff) { return __builtin_amdgcn_raw_buffer_load_b128(rs, voff, soff, AUX); }
template <int AUX> __device__ __forceinline__ void bst16(rsrc_t rs, unsigned voff, unsigned soff, v4u v) { __builtin_amdgcn_raw_buffer_store_b128(v, rs, voff, soff, AUX); }
#define WAIT_V(n) asm volatile("s_waitcnt vmcnt(" #n ")" ::: "memory")
#define WAIT_L(n) asm volatile("s_waitcnt lgkmcnt(" #n ")" ::: "memory")
#define BAR() do { asm volatile("" ::: "memory"); __builtin_amdgcn_s_barrier(); asm volatile("" ::: "memory"); } while (0)
#define SB() __builtin_amdgcn_sched_barrier(0)
#define PIN_S(x) asm volatile("" : "+s"(x))
#define PIN_V(x) asm volatile("" : "+v"(x))
#define RLX_AGENT __ATOMIC_RELAXED, __HIP_MEMORY_SCOPE_AGENT
#define STAMP(p, i) do {} while (0)

__device__ __forceinline__ void dma16(rsrc_t rs, unsigned voff, unsigned soff, unsigned lds_addr) {
    asm volatile("s_mov_b32 m0, %0\n\ts_nop 0\n\tbuffer_load_dwordx4 %1, %2, %3 offen lds" ::"s"(lds_addr), "v"(voff), "s"(rs), "s"(soff) : "memory", "m0");
}
__device__ __forceinline__ void dma16_sc1(rsrc_t rs, unsigned voff, unsigned soff, unsigned lds_addr) {
    asm volatile("s_mov_b32 m0, %0\n\ts_nop 0\n\tbuffer_load_dwordx4 %1, %2, %3 offen sc1 lds" ::"s"(lds_addr), "v"(voff), "s"(rs), "s"(soff) : "memory", "m0");
}
#define SET_M0(base, imm) asm volatile("s_add_u32 m0, %0, %1" ::"s"(base), "i"(imm) : "memory", "m0", "scc")
__device__ __forceinline__ void dma16_m0(rsrc_t rs, unsigned voff, unsigned soff) {
    asm volatile("buffer_load_dwordx4 %0, %1, %2 offen lds" ::"v"(voff), "s"(rs), "s"(soff) : "memory");
}
__device__ __forceinline__ void dma16_m0_sc1(rsrc_t rs, unsigned voff, unsigned soff) {
    asm volatile("buffer_load_dwordx4 %0, %1, %2 offen sc1 lds" ::"v"(voff), "s"(rs), "s"(soff) : "memory");
}
__device__ __forceinline__ void aload16(v4u& d, rsrc_t rs, unsigned voff) {
    asm volatile("buffer_load_dwordx4 %0, %1, %2, 0 offen" : "=v"(d) : "v"(voff), "s"(rs) : "memory");
}

__device__ __forceinline__ void wait_words(rsrc_t rsW, unsigned voff, unsigned e) {
    unsigned t0, left = SPIN_MAX;
    asm volatile(
        "1:\n\t"
        "buffer_load_dword %0, %2, %3, 0 offen sc1\n\t"
        "s_waitcnt vmcnt(0)\n\t"
        "v_cmp_ne_u32 vcc, %4, %0\n\t"
        "s_nop 4\n\t"
        "s_cbranch_vccz 2f\n\t"
        "s_sleep 8\n\t"
        "s_sub_u32 %1, %1, 1\n\t"
        "s_cbranch_scc0 1b\n\t"
        "2:"
        : "=&v"(t0), "+s"(left)
        : "v"(voff), "s"(rsW), "s"(e)
        : "memory", "vcc", "scc");
}
__device__ __forceinline__ void dma4(rsrc_t rs, unsigned voff, unsigned lds_addr) {
    asm volatile("s_mov_b32 m0, %0\n\ts_nop 0\n\tbuffer_load_dword %1, %2, 0 offen lds" ::"s"(lds_addr), "v"(voff), "s"(rs) : "memory", "m0");
}
__device__ __forceinline__ void dma4_sc1(rsrc_t rs, unsigned voff, unsigned lds_addr) {
    asm volatile("s_mov_b32 m0, %0\n\ts_nop 0\n\tbuffer_load_dword %1, %2, 0 offen sc1 lds" ::"s"(lds_addr), "v"(voff), "s"(rs) : "memory", "m0");
}
__device__ __forceinline__ unsigned add_count(const Params& p, int off, unsigned n) {
    return __hip_atomic_fetch_add((GAS unsigned*)(p.ws + off), n, RLX_AGENT);
}
template <int SLEEP>
__device__ __forceinline__ void wait_ready(const Params& p, int off, unsigned e) {
    GAS unsigned* c = (GAS unsigned*)(p.ws + off);
    for (unsigned it = 0; it < SPIN_MAX; ++it) {
        if ((unsigned)__builtin_amdgcn_readfirstlane(__hip_atomic_load(c, RLX_AGENT)) == e) break;
        __builtin_amdgcn_s_sleep(SLEEP);
    }
    __builtin_amdgcn_fence(__ATOMIC_ACQUIRE, "wavefront");
}
__device__ __forceinline__ void lds_arrive(unsigned off) {
    if ((threadIdx.x & 63) == 0) (void)__hip_atomic_fetch_add((LAS unsigned*)(uintptr_t)off, 1u, __ATOMIC_RELAXED, __HIP_MEMORY_SCOPE_WORKGROUP);
}
__device__ __forceinline__ void lds_wait(unsigned off, unsigned n) {
    for (unsigned it = 0; it < (SPIN_MAX << 4); ++it) {
        if ((unsigned)__builtin_amdgcn_readfirstlane(__hip_atomic_load((LAS unsigned*)(uintptr_t)off, __ATOMIC_RELAXED, __HIP_MEMORY_SCOPE_WORKGROUP)) >= n) break;
        __builtin_amdgcn_s_sleep(1);
    }
    asm volatile("" ::: "memory");
}
__device__ __forceinline__ void set_flag(const Params& p, int off, int idx, unsigned e) {
    __hip_atomic_store((GAS unsigned*)(p.ws + off) + idx, e, RLX_AGENT);
}

constexpr int G1_STAGE = 40960, G1_G = 16384, G1_U = 16384 + 12288;
constexpr int SCR = 3 * G1_STAGE;
constexpr int PR_K = SCR;
constexpr int PR_V = PR_K + 11264;
constexpr int PR_S = PR_V + 10240;
constexpr int PR_D = PR_S + 1024;
constexpr int CV_RAW = SCR;
constexpr int CV_XR = SCR + 16384, CV_XRS = 4608;
constexpr int FLG_SCR = CV_XR + 3 * CV_XRS;
constexpr int SD_SCR = FLG_SCR + 1024;
constexpr int DUMMY = PR_D + 10240;
constexpr int LC_B = 163808, LC_C = 163824;
constexpr int CV_CS = 4160, CV_HP = 0, CV_HPS = 4 * CV_CS;
constexpr int G2_Y = 3 * CV_HPS;
constexpr int G2_RED = 61440;
constexpr int LDS_BYTES = 163840;
static_assert(SD_SCR + 1024 <= DUMMY && DUMMY + 4096 <= LC_B && G2_Y + 80 * 128 <= G2_RED && G2_RED + 60 * 1024 <= SCR, "lds");
constexpr int CODE_BYTES = 24576;

__global__ void __launch_bounds__(256, 1) k_fused(Params p) {
    extern __shared__ __attribute__((aligned(16))) unsigned char smem[];
    const int tid = threadIdx.x, lane = tid & 63, w = __builtin_amdgcn_readfirstlane(tid >> 6);
    const int bid = blockIdx.x;
    const int xcd = bid & 7, bi = bid >> 3, mq = xcd >> 1, half = xcd & 1;
    STAMP(p, 0);
    {
        unsigned long long pc;
        asm volatile("s_getpc_b64 %0" : "=s"(pc));
        const rsrc_t rsC = mkrs((const void*)(pc & ~255ull), CODE_BYTES - 256);
        dma16(rsC, (unsigned)(((bi + 32 * w) << 10) + lane * 16), 0, DUMMY);
    }
    const int nch = bid < 128 ? 3 : 2, ch = (bid < 128 ? 3 * bid : 384 + 2 * (bid - 128)) + w;
    const bool has_ch = w < nch;
    asm volatile("" ::"s"(p.q), "s"(p.k), "s"(p.v), "s"(p.sk), "s"(p.hk), "s"(p.sd), "s"(p.wg), "s"(p.wu), "s"(p.wd), "s"(p.gw), "s"(p.out), "s"(p.ws));
    rsrc_t rsW = mkrs(p.ws, WS_BYTES);
    rsrc_t rsQ = mkrs(p.q, SEQ * DM * 2);

    const int ptt = bid & 31, pd8 = bid >> 5;
    auto prep_loads = [&]() {
        const rsrc_t rsK = mkrs(p.k, SEQ * DM * 2), rsV = mkrs(p.v, SEQ * DM * 2), rsS = mkrs(p.sk, DM * 4 * 2), rsD = mkrs(p.wd, FF * DM * 2);
        auto kdma = [&](int i) {
            const int c = 64 * i + lane, row = c / 10, col = c - 10 * row, t = 64 * ptt - 4 + row;
            dma16(rsK, (t >= 0 && c < 680) ? (unsigned)((t * DM + 80 * pd8 + 8 * col) * 2) : OOB, 0, PR_K + 1024 * i);
        };
        auto vdma = [&](int i) {
            const int c = 64 * i + lane, row = c / 10, col = c - 10 * row;
            dma16(rsV, (unsigned)(((64 * ptt + row) * DM + 80 * pd8 + 8 * col) * 2), 0, PR_V + 1024 * i);
        };
        auto ddma = [&](int j, int i) {
            const int g = 2 * bid + j, gdt = g / 48, gks = g - 48 * gdt;
            const int c = 64 * i + lane, row = c / 10, col = c - 10 * row;
            dma16(rsD, g < 384 ? (unsigned)(((32 * gks + row) * DM + 80 * gdt + 8 * col) * 2) : OOB, 0, PR_D + 5120 * j + 1024 * i);
        };
        if (w == 0) {
#pragma unroll
            for (int i = 0; i < 8; ++i) kdma(i);
        } else if (w == 1) {
#pragma unroll
            for (int i = 8; i < 11; ++i) kdma(i);
#pragma unroll
            for (int i = 0; i < 5; ++i) vdma(i);
        } else if (w == 2) {
#pragma unroll
            for (int i = 5; i < 10; ++i) vdma(i);
#pragma unroll
            for (int i = 0; i < 5; ++i) ddma(0, i);
        } else {
            dma16(rsS, lane < 40 ? (unsigned)(80 * pd8 * 8 + 16 * lane) : OOB, 0, PR_S);
#pragma unroll
            for (int i = 0; i < 5; ++i) ddma(1, i);
        }
    };
    v4u pkv[3], pwd[5];
    auto prep_compute = [&]() {
        const int i16 = lane & 15, q = (lane >> 2) & 3, pp = lane & 3;
#pragma unroll
        for (int ps = 0; ps < 3; ++ps) {
            const int quad = min(4 * ps + w, 9), cg = quad >> 1, tgp = 4 * (quad & 1) + (lane >> 4);
            const unsigned ka = (unsigned)(PR_K + (8 * tgp + q) * 160 + (16 * cg + 4 * pp) * 2), va = ka + (PR_V - PR_K);
            const v2u k0 = tr_read(ka), k1 = tr_read(ka + 640), k2 = tr_read(ka + 1280), v0 = tr_read(va), v1 = tr_read(va + 640);
            const v2u tw = lds_ld<v2u>((unsigned)(PR_S + (16 * cg + i16) * 8));
            const unsigned W10 = __builtin_amdgcn_alignbit(tw.x, tw.x, 16), W32 = __builtin_amdgcn_alignbit(tw.y, tw.y, 16);
            const unsigned P[6] = {k0.x, k0.y, k1.x, k1.y, k2.x, k2.y};
            unsigned Q[5];
#pragma unroll
            for (int m = 0; m < 5; ++m) Q[m] = __builtin_amdgcn_alignbit(P[m + 1], P[m], 16);
            const unsigned V[4] = {v0.x, v0.y, v1.x, v1.y};
            float r[8];
#pragma unroll
            for (int m = 0; m < 4; ++m) {
                r[2 * m] = dot2(Q[m], W32, dot2(Q[m + 1], W10, 0.f)) * blo(V[m]);
                r[2 * m + 1] = dot2(P[m + 1], W32, dot2(P[m + 2], W10, 0.f)) * bhi(V[m]);
            }
            pkv[ps] = v4u{pk2(r[7], r[6]), pk2(r[5], r[4]), pk2(r[3], r[2]), pk2(r[1], r[0])};
        }
        const unsigned T = (unsigned)(PR_D + 5120 * (w & 1));
        const int gq = lane >> 4, row = 16 * (gq >> 1) + 4 * (gq & 1) + q;
#pragma unroll
        for (int at = 0; at < 5; ++at) {
            const v2u lo = tr_read(T + row * 160 + (16 * at + 4 * pp) * 2);
            const v2u hi = tr_read(T + (row + 8) * 160 + (16 * at + 4 * pp) * 2);
            pwd[at] = v4u{lo.x, lo.y, hi.x, hi.y};
        }
    };
    prep_loads();
    STAMP(p, 14);

    const int mt1 = 4 * mq + (bi & 3), ft = 8 * half + (bi >> 2);
    const int wm = w & 1, wf = w >> 1;
#define NCOMMON 1
    constexpr int NC = NCOMMON, NR = 10 - NC;
    const int rot1 = NR > 0 ? ((ft & 7) + ((NR * (mt1 & 3)) >> 2)) % (NR > 0 ? NR : 1) : 0;
    rsrc_t rsWg = mkrs(p.wg, DM * FF * 2), rsWu = mkrs(p.wu, DM * FF * 2);
    PIN_S(rsW); PIN_S(rsWg); PIN_S(rsWu); PIN_S(rsQ);
    unsigned xoff[4], goff[3], uoff[3];
#pragma unroll
    for (int n = 0; n < 4; ++n) {
        const int row = 32 * w + 8 * n + (lane >> 3);
        xoff[n] = (unsigned)((128 * mt1 + row) * 1280 + (((lane & 7) ^ ((4 * n + (lane >> 4)) & 7)) << 4));
    }
#pragma unroll
    for (int n = 0; n < 3; ++n) {
        const int Lc = 64 * (3 * w + n) + lane, k = Lc / 12, c = Lc - 12 * k;
        goff[n] = (unsigned)(k * 3072 + 192 * ft + 16 * c);
        uoff[n] = (unsigned)(k * 3072 + 192 * ft + 16 * (c ^ 2));
    }
    auto g1_issue = [&](int kt, int stg) {
#pragma unroll
        for (int n = 0; n < 4; ++n) dma16(rsQ, xoff[n], kt * 128, stg + (4 * w + n) * 1024);
#pragma unroll
        for (int n = 0; n < 3; ++n) {
            dma16(rsWg, goff[n], kt * 64 * 3072, stg + G1_G + (3 * w + n) * 1024);
            dma16(rsWu, uoff[n], kt * 64 * 3072, stg + G1_U + (3 * w + n) * 1024);
        }
    };
    g1_issue(0, 0);
    const unsigned epoch = *(const __attribute__((address_space(4))) unsigned*)(uintptr_t)(p.ws + OFF_EPOCH) + 1u;
    f32x16 acc[3][2];
#pragma unroll
    for (int a = 0; a < 3; ++a)
#pragma unroll
        for (int b = 0; b < 2; ++b)
#pragma unroll
            for (int r = 0; r < 16; ++r) acc[a][b][r] = 0.f;
    const int l31 = lane & 31, hh = lane >> 5;
    const int mt2 = 8 * mq + (bi & 7), dt = 4 * half + (bi >> 3);
    unsigned favo;
    {
        const int c0 = ch - w, g0 = c0 / 80;
        int who = 24 * dt + min(max(lane - 32, 0), 23);
        if (lane < 32 && has_ch) who = 32 * (ch / 80) + lane;
        if (lane == 56) who = mt1;
        if (lane == 57) who = 16 + g0;
        if (lane == 58) who = 16 + min(g0 + 1, 7);
        favo = (unsigned)(OFF_FA + 128 * who);
    }
    const int rot2 = (3 * (bi & 7) / 2 + 3 * (bi >> 3)) % 12;
    unsigned a2o[12];
#pragma unroll
    for (int s = 0; s < 12; ++s) { const int ks = s + rot2; a2o[s] = (unsigned)(12 * w + (ks >= 12 ? ks - 12 : ks)) * 1024u; }
    const unsigned a2base = (unsigned)(OFF_WDT + dt * 48 * 5 * 1024);
    const int brow = 64 * wm + l31;
    unsigned ba[4];
#pragma unroll
    for (int kk = 0; kk < 4; ++kk) ba[kk] = (unsigned)(brow * 128 + (((2 * kk + hh) ^ ((brow >> 1) & 7)) << 4));
    unsigned aa[3];
#pragma unroll
    for (int fb = 0; fb < 3; ++fb) {
        const int fbg = 3 * wf + fb, G = lane >> 4;
        aa[fb] = (unsigned)(((G & 1) ? G1_U + (fbg ^ 1) * 32 : G1_G + fbg * 32) + (8 * (G >> 1) + ((lane >> 2) & 3)) * 192 + 8 * (lane & 3));
    }
    unsigned okv[3], owd, ozero, ofa, oraw, osd, okvr, oh[2];
    {
        const int i16 = lane & 15;
#pragma unroll
        for (int ps = 0; ps < 3; ++ps) {
            const int quad = min(4 * ps + w, 9), cg = quad >> 1, tgp = 4 * (quad & 1) + (lane >> 4);
            okv[ps] = 4 * ps + w < 10 ? (unsigned)(OFF_KVR + ((80 * pd8 + 16 * cg + i16) * SEQ + 2040 - 64 * ptt - 8 * tgp) * 2) : OOB;
        }
        const int g = 2 * bid + (w & 1), gdt = g / 48, gks = g - 48 * gdt;
        owd = (w >= 2 && g < 384) ? (unsigned)(OFF_WDT + (gdt * 48 + gks) * 5 * 1024 + lane * 16) : OOB;
        ozero = (bid < 24 && tid == 0) ? (unsigned)(bid < 16 ? OFF_CB + 256 * bid : OFF_CC + 256 * (bid - 16)) : OOB;
        ofa = tid == 0 ? (unsigned)(OFF_FA + 128 * bid) : OOB;
        oraw = has_ch ? (unsigned)(ch * SEQ * 2 + lane * 16) : OOB;
        osd = has_ch ? (unsigned)((ch & ~1) * 2) : OOB;
        okvr = has_ch ? (unsigned)(OFF_KVR + ch * SEQ * 2 + lane * 16) : OOB;
#pragma unroll
        for (int mt = 0; mt < 2; ++mt) oh[mt] = (unsigned)(OFF_HF + ((128 * mt1 + 64 * wm + 32 * mt + l31) >> 4) * 48 * 1024 + ((lane & 15) + 16 * hh) * 16);
    }
    unsigned a2v0 = (unsigned)(lane * 16), a2v4 = a2v0 + 4096;
    PIN_V(favo); PIN_V(okv[0]); PIN_V(okv[1]); PIN_V(okv[2]); PIN_V(owd); PIN_V(ozero); PIN_V(ofa); PIN_V(oraw); PIN_V(osd); PIN_V(okvr); PIN_V(oh[0]); PIN_V(oh[1]);
    PIN_V(ba[0]); PIN_V(ba[1]); PIN_V(ba[2]); PIN_V(ba[3]); PIN_V(aa[0]); PIN_V(aa[1]); PIN_V(aa[2]); PIN_V(a2v0); PIN_V(a2v4);
    rsrc_t rsH = mkrs(p.hk, DM * SEQ * 2), rsD1 = mkrs(p.sd, DM * 2);
    PIN_S(rsH); PIN_S(rsD1);
    STAMP(p, 1);
    WAIT_V(10);
    BAR();
    STAMP(p, 15);
    int kap = NC > 1 ? 1 : 1 + rot1;
    int kcnt = 1;
    auto knext = [&]() { ++kcnt; kap = kcnt < NC ? kcnt : kcnt == NC ? NC + rot1 : kap == 9 ? NC : kap + 1; };
    g1_issue(kap, G1_STAGE);
    knext();
    prep_compute();
    STAMP(p, 16);

    v4u A2[12][5];
    unsigned a2s[12];
#pragma unroll
    for (int s = 0; s < 12; ++s) a2s[s] = a2base + a2o[s] * 5;
#define A2LD(slot) A2[(slot) / 5][(slot) % 5] = bld16<SC1>(rsW, ((slot) % 5 == 4 ? a2v4 : a2v0 + ((slot) % 5) * 1024), a2s[(slot) / 5])
    auto prep_store = [&]() {
        __builtin_amdgcn_raw_buffer_store_b32(0u, rsW, ozero, 0, SC1);
#pragma unroll
        for (int ps = 0; ps < 3; ++ps) bst16<SC1>(rsW, okv[ps], 0, pkv[ps]);
#pragma unroll
        for (int at = 0; at < 5; ++at) bst16<SC1>(rsW, owd, at * 1024, pwd[at]);
    };

    {
        v4u fb0[2], fb1[2];
        v2u fa0[3][2], fa1[3][2];
#define RDB(d, kk, mt) d = lds_ld<v4u>(ba[kk] + (mt) * 4096)
#define RDA(d, kk, fb, hi) d[fb][hi] = tr_read(aa[fb] + (kk) * 16 * 192 + (hi) * 4 * 192)
#define MM(A, B, fb, mt) acc[fb][mt] = mfma32(v4u{A[fb][0].x, A[fb][0].y, A[fb][1].x, A[fb][1].y}, B[mt], acc[fb][mt])
        WAIT_V(10);
        WAIT_L(0);
        BAR();
        STAMP(p, 2);
        RDB(fb0[0], 0, 0); RDA(fa0, 0, 0, 0); RDA(fa0, 0, 0, 1); RDB(fb0[1], 0, 1); RDA(fa0, 0, 1, 0); RDA(fa0, 0, 1, 1); RDA(fa0, 0, 2, 0); RDA(fa0, 0, 2, 1);
        int st = 0;
        unsigned mx = 2 * G1_STAGE + 4096u * w, mg = 2 * G1_STAGE + G1_G + 3072u * w, mu = 2 * G1_STAGE + G1_U + 3072u * w;
        unsigned sx = kap * 128, sw = kap * 64 * 3072;
#define STEP(FA, FB, GA, GB, kn, D0, D1, D2)                                                     \
            MM(FA, FB, 0, 0); SB(); RDB(GB[0], kn, 0); RDA(GA, kn, 0, 0); SB();                 \
            MM(FA, FB, 0, 1); SB(); RDA(GA, kn, 0, 1); RDB(GB[1], kn, 1); SB();                 \
            MM(FA, FB, 1, 0); SB(); RDA(GA, kn, 1, 0); RDA(GA, kn, 1, 1); D0##_M; SB();         \
            MM(FA, FB, 1, 1); SB(); D0##_L; RDA(GA, kn, 2, 0); D1##_M; SB();                    \
            MM(FA, FB, 2, 0); SB(); D1##_L; RDA(GA, kn, 2, 1); D2##_M; SB();                    \
            MM(FA, FB, 2, 1); SB(); D2##_L; SB();
#define X0_M SET_M0(mx, 0)
#define X0_L dma16_m0(rsQ, xoff[0], sx)
#define X1_M SET_M0(mx, 1024)
#define X1_L dma16_m0(rsQ, xoff[1], sx)
#define X2_M SET_M0(mx, 2048)
#define X2_L dma16_m0(rsQ, xoff[2], sx)
#define X3_M SET_M0(mx, 3072)
#define X3_L dma16_m0(rsQ, xoff[3], sx)
#define G0_M SET_M0(mg, 0)
#define G0_L dma16_m0(rsWg, goff[0], sw)
#define G1_M SET_M0(mg, 1024)
#define G1_L dma16_m0(rsWg, goff[1], sw)
#define G2_M SET_M0(mg, 2048)
#define G2_L dma16_m0(rsWg, goff[2], sw)
#define U0_M SET_M0(mu, 0)
#define U0_L dma16_m0(rsWu, uoff[0], sw)
#define U1_M SET_M0(mu, 1024)
#define U1_L dma16_m0(rsWu, uoff[1], sw)
#define U2_M SET_M0(mu, 2048)
#define U2_L dma16_m0(rsWu, uoff[2], sw)
#define KBODY(WAITV) { \
            const int dlt = (st == 2 * G1_STAGE) ? -2 * G1_STAGE : G1_STAGE; \
            SB(); \
            STEP(fa0, fb0, fa1, fb1, 1, X0, X1, X2) \
            STEP(fa1, fb1, fa0, fb0, 2, X3, G0, U0) \
            STEP(fa0, fb0, fa1, fb1, 3, G1, U1, G2) \
            U2_M; SB(); \
            MM(fa1, fb1, 0, 0); SB(); U2_L; ba[0] += dlt; aa[0] += dlt; SB(); \
            MM(fa1, fb1, 0, 1); SB(); aa[1] += dlt; aa[2] += dlt; \
            knext(); \
            mx = (unsigned)st + 4096u * w; mg = (unsigned)st + G1_G + 3072u * w; mu = (unsigned)st + G1_U + 3072u * w; sx = kap * 128; sw = kap * 64 * 3072; \
            SB(); \
            MM(fa1, fb1, 1, 0); SB(); \
            WAIT_L(0); \
            WAITV; \
            BAR(); \
            RDB(fb0[0], 0, 0); RDA(fa0, 0, 0, 0); RDA(fa0, 0, 0, 1); SB(); \
            MM(fa1, fb1, 1, 1); SB(); RDB(fb0[1], 0, 1); RDA(fa0, 0, 1, 0); RDA(fa0, 0, 1, 1); SB(); \
            MM(fa1, fb1, 2, 0); SB(); RDA(fa0, 0, 2, 0); RDA(fa0, 0, 2, 1); ba[1] += dlt; SB(); \
            MM(fa1, fb1, 2, 1); SB(); ba[2] += dlt; ba[3] += dlt; \
            st += dlt; \
            }
#pragma unroll 1
        for (int kt = 0; kt < 8; ++kt) {
            KBODY(if (__builtin_expect((0x4A >> kt) & 1, 0)) { if (kt == 1) WAIT_V(19); else if (kt == 3) WAIT_V(16); else WAIT_V(14); } else WAIT_V(10))
            if (__builtin_expect((0x35 >> kt) & 1, 0)) {
                if (kt == 0) prep_store();
                else if (kt == 2) {
                    __builtin_amdgcn_raw_buffer_store_b32(epoch, rsW, ofa, 0, SC1);
#pragma unroll
                    for (int n = 0; n < 4; ++n) dma16(rsH, oraw, n * 1024, CV_RAW + 4096u * w + 1024u * n);
                    dma4(rsD1, osd, SD_SCR + 256u * w);
                } else if (kt == 4) dma4_sc1(rsW, favo, FLG_SCR + 256u * w);
                else {
                    const unsigned flv = lds_ld<unsigned>((unsigned)(FLG_SCR + 256 * w + lane * 4));
                    if (__builtin_amdgcn_ballot_w64(flv != epoch) != 0ull) wait_words(rsW, favo, epoch);
#pragma unroll
                    for (int n = 0; n < 4; ++n) dma16_sc1(rsW, okvr, n * 1024, (has_ch ? (unsigned)(CV_XR + CV_XRS * w) : (unsigned)DUMMY) + n * 1024);
                }
            }
        }
#define STEPA(FA, FB, GA, GB, kn, s0)                                                            \
            MM(FA, FB, 0, 0); SB(); RDB(GB[0], kn, 0); RDA(GA, kn, 0, 0); SB();                 \
            MM(FA, FB, 0, 1); SB(); RDA(GA, kn, 0, 1); RDB(GB[1], kn, 1); SB();                 \
            MM(FA, FB, 1, 0); SB(); RDA(GA, kn, 1, 0); RDA(GA, kn, 1, 1); SB();                 \
            MM(FA, FB, 1, 1); SB(); A2LD(s0); RDA(GA, kn, 2, 0); SB();                          \
            MM(FA, FB, 2, 0); SB(); A2LD(s0 + 1); RDA(GA, kn, 2, 1); SB();                      \
            MM(FA, FB, 2, 1); SB(); A2LD(s0 + 2); SB();
        {
            const int dlt = (st == 2 * G1_STAGE) ? -2 * G1_STAGE : G1_STAGE;
            SB();
            STEPA(fa0, fb0, fa1, fb1, 1, 0)
            STEPA(fa1, fb1, fa0, fb0, 2, 3)
            STEPA(fa0, fb0, fa1, fb1, 3, 6)
            MM(fa1, fb1, 0, 0); SB(); A2LD(9); ba[0] += dlt; aa[0] += dlt; SB();
            MM(fa1, fb1, 0, 1); SB(); A2LD(10); aa[1] += dlt; aa[2] += dlt; SB();
            MM(fa1, fb1, 1, 0); SB();
            if (tid == 0) { lds_st<unsigned>(LC_B, 0u); lds_st<unsigned>(LC_C, 0u); }
            WAIT_L(0);
            WAIT_V(11);
            BAR();
            RDB(fb0[0], 0, 0); RDA(fa0, 0, 0, 0); RDA(fa0, 0, 0, 1); SB();
            MM(fa1, fb1, 1, 1); SB(); RDB(fb0[1], 0, 1); RDA(fa0, 0, 1, 0); RDA(fa0, 0, 1, 1); SB();
            MM(fa1, fb1, 2, 0); SB(); RDA(fa0, 0, 2, 0); RDA(fa0, 0, 2, 1); ba[1] += dlt; SB();
            MM(fa1, fb1, 2, 1); SB(); A2LD(11); ba[2] += dlt; ba[3] += dlt; SB();
            STEPA(fa0, fb0, fa1, fb1, 1, 12)
            STEPA(fa1, fb1, fa0, fb0, 2, 15)
            STEPA(fa0, fb0, fa1, fb1, 3, 18)
            MM(fa1, fb1, 0, 0); SB(); A2LD(21); SB();
            MM(fa1, fb1, 0, 1); SB(); A2LD(22); SB();
            MM(fa1, fb1, 1, 0); SB();
            WAIT_L(0);
            BAR();
            A2LD(23); SB();
            MM(fa1, fb1, 1, 1); SB(); A2LD(24); SB();
            MM(fa1, fb1, 2, 0); SB(); A2LD(25); SB();
            MM(fa1, fb1, 2, 1); SB();
        }
#undef STEPA
#undef MM
#undef STEP
#undef KBODY
    }
    STAMP(p, 3);
#pragma unroll
    for (int fb = 0; fb < 3; ++fb)
#pragma unroll
        for (int mt = 0; mt < 2; ++mt) {
            const f32x16& a = acc[fb][mt];
            v4u o;
#pragma unroll
            for (int j = 0; j < 4; ++j) o[j] = pk2(a[2 * j] * a[8 + 2 * j], a[2 * j + 1] * a[8 + 2 * j + 1]);
            const int Ft = 6 * ft + 3 * wf + fb;
            bst16<SC1>(rsW, oh[mt], (unsigned)((Ft >> 1) * 1024 + (Ft & 1) * 512), o);
        }
    STAMP(p, 4);

    if (has_ch) {
        const unsigned HP = (unsigned)(CV_HP + w * CV_HPS), XR = (unsigned)(CV_XR + w * CV_XRS), RAW = (unsigned)(CV_RAW + w * 4096);
        v4u c0[5];
        v2u c1[5];
        unsigned ra0 = RAW + 16 * max(lane - 2, 0), ra1 = RAW + 16 * max(lane - 1, 0), ra2 = RAW + 16 * (lane - 2), ra3 = RAW + 16 * (lane - 1), ra4 = RAW + 16 * (254 + (lane & 1));
        PIN_V(ra0); PIN_V(ra1); PIN_V(ra2); PIN_V(ra3); PIN_V(ra4);
#pragma unroll
        for (int ps = 0; ps < 5; ++ps) {
            c0[ps] = lds_ld<v4u>(ps == 0 ? ra0 : ps < 4 ? ra2 + 1024 * ps : ra4);
            c1[ps] = lds_ld<v2u>(ps == 0 ? ra1 : ps < 4 ? ra3 + 1024 * ps : RAW + 16 * 255);
        }
        if (lane < 2) c0[0] = v4u{0, 0, 0, 0};
        if (lane < 1) c1[0] = v2u{0, 0};
        if (lane & 1) c1[4] = v2u{0, 0};
#pragma unroll
        for (int ps = 0; ps < 5; ++ps) {
            const int n = 64 * ps + lane;
            const unsigned Dw[6] = {c0[ps].x, c0[ps].y, c0[ps].z, c0[ps].w, c1[ps].x, c1[ps].y};
            v4u o0, o1, o2, o3;
#pragma unroll
            for (int q = 0; q < 4; ++q) {
                o0[q] = __builtin_amdgcn_alignbit(Dw[q + 1], Dw[q], 16);
                o1[q] = Dw[q + 1];
                o2[q] = __builtin_amdgcn_alignbit(Dw[q + 2], Dw[q + 1], 16);
                o3[q] = Dw[q + 2];
            }
            if (ps < 4 || lane < 2) {
                lds_st<v4u>(HP + 0 * CV_CS + 16 * n, o0);
                lds_st<v4u>(HP + 1 * CV_CS + 16 * n, o1);
                lds_st<v4u>(HP + 2 * CV_CS + 16 * n, o2);
                lds_st<v4u>(HP + 3 * CV_CS + 16 * n, o3);
            }
        }
        if (lane < 32) lds_st<v4u>(XR + 4096 + 16 * lane, v4u{0, 0, 0, 0});
        asm volatile("" ::: "memory");
        STAMP(p, 5);

        const int r = lane & 15, g = lane >> 4;
        unsigned ap0 = HP + (unsigned)((r & 3) * CV_CS + 2 * ((r & ~3) + 8 * g));
        unsigned ap1 = ap0 + 8;
        PIN_V(ap0); PIN_V(ap1);
        unsigned bp = XR + (unsigned)(2 * (240 - 16 * r + 8 * g));
        PIN_V(bp);
        f32x4 cacc[8];
#pragma unroll
        for (int i = 0; i < 8; ++i) cacc[i] = f32x4{0.f, 0.f, 0.f, 0.f};
        v4u CA[2][8], CB[2][8];
        auto cvrd = [&](int set, int s, int i) {
            CB[set][i] = lds_ld<v4u>(bp + 512 * (7 - i) + 64 * s);
            const v2u lo = lds_ld<v2u>(ap0 + 512 * i + 64 * s), hi = lds_ld<v2u>(ap1 + 512 * i + 64 * s);
            CA[set][i] = v4u{lo.x, lo.y, hi.x, hi.y};
        };
#pragma unroll
        for (int i = 0; i < 8; ++i) cvrd(0, 0, i);
#pragma unroll
        for (int s = 0; s < 8; ++s) {
            const int cur = s & 1;
            SB();
#pragma unroll
            for (int gm = 0; gm < 8; ++gm) {
#pragma unroll
                for (int be = 0; be <= gm; ++be) cacc[gm] = mfma16(CA[cur][be], CB[cur][gm - be], cacc[gm]);
                SB();
                if (s < 7) cvrd(cur ^ 1, s + 1, gm);
                { const int i = 8 * s + gm; if (((i + 1) * 34) / 64 != (i * 34) / 64) A2LD(26 + (i * 34) / 64); }
                SB();
            }
            if (s == 1) {
                WAIT_V(8);
                lds_arrive(LC_B);
            }
        }
        STAMP(p, 6);
        const unsigned sdv = lds_ld<unsigned>((unsigned)(SD_SCR + 256 * w));
        const float Dc = (ch & 1) ? bhi(sdv) : blo(sdv);
#pragma unroll
        for (int gm = 0; gm < 8; ++gm) {
            const int t0 = 256 * gm + 16 * r + 4 * g;
            const v2u kvp = lds_ld<v2u>(XR + 2 * (2044 - t0));
            const float y0 = cacc[gm][0] + bhi(kvp.y) * Dc, y1 = cacc[gm][1] + blo(kvp.y) * Dc, y2 = cacc[gm][2] + bhi(kvp.x) * Dc, y3 = cacc[gm][3] + blo(kvp.x) * Dc;
            lds_st<v2u>(HP + 2 * t0, v2u{pk2(y0, y1), pk2(y2, y3)});
        }
        asm volatile("" ::: "memory");
#pragma unroll
        for (int ps = 0; ps < 4; ++ps) {
            const int n = 64 * ps + lane;
            bst16<SC1>(rsW, (unsigned)(OFF_YT + (ch * SEQ + 8 * n) * 2), 0, lds_ld<v4u>(HP + 16 * n));
        }
    } else {
        if (w == 3) {
            const rsrc_t rsG = mkrs(p.gw, DM * 2);
            const int c16 = lane & 15, row0 = 128 * mt1 + 8 * ft + (lane >> 4);
            v4u gwv[5], xa[5], xb[5];
#pragma unroll
            for (int i = 0; i < 5; ++i) {
                gwv[i] = bld16<0>(rsG, (unsigned)(16 * (c16 + 16 * i)), 0);
                xa[i] = bld16<0>(rsQ, (unsigned)(row0 * DM * 2 + 16 * (c16 + 16 * i)), 0);
                xb[i] = bld16<0>(rsQ, (unsigned)((row0 + 4) * DM * 2 + 16 * (c16 + 16 * i)), 0);
            }
            float s0 = 0.f, s1 = 0.f;
#pragma unroll
            for (int i = 0; i < 5; ++i)
#pragma unroll
                for (int e = 0; e < 4; ++e) { s0 = dot2(xa[i][e], gwv[i][e], s0); s1 = dot2(xb[i][e], gwv[i][e], s1); }
#pragma unroll
            for (int m = 1; m < 16; m <<= 1) { s0 += __shfl_xor(s0, m); s1 += __shfl_xor(s1, m); }
            const float g0 = rb(1.0f / (1.0f + __expf(-rb(s0)))), g1 = rb(1.0f / (1.0f + __expf(-rb(s1))));
            __builtin_amdgcn_raw_buffer_store_b32(__builtin_bit_cast(unsigned, g0), rsW, c16 == 0 ? (unsigned)(OFF_GATE + row0 * 4) : OOB, 0, SC1);
            __builtin_amdgcn_raw_buffer_store_b32(__builtin_bit_cast(unsigned, g1), rsW, c16 == 0 ? (unsigned)(OFF_GATE + (row0 + 4) * 4) : OOB, 0, SC1);
        }
        WAIT_V(0);
        lds_arrive(LC_B);
        lds_wait(LC_B, 4);
        if (w == 3) {
            if (lane == 0) {
                const unsigned old = add_count(p, OFF_CB + 256 * mt1, 1u);
                if (old == 15u) set_flag(p, OFF_RB + 256 * mt1, 0, epoch);
            }
            if (bid == 0) {
#pragma unroll 1
                for (int i = 0; i < 4; ++i) wait_words(rsW, (unsigned)(OFF_FA + 128 * (64 * i + lane)), epoch);
                if (lane == 0) set_flag(p, OFF_EPOCH, 0, epoch);
            }
        }
#pragma unroll
        for (int slot = 26; slot < 60; ++slot) A2LD(slot);
    }
    STAMP(p, 7);

    wait_ready<4>(p, OFF_RB + 256 * (mt2 >> 1), epoch);
    STAMP(p, 8);
    constexpr int W2 = 8;
    unsigned b2off[4];
#pragma unroll
    for (int n = 0; n < 4; ++n) b2off[n] = (unsigned)(OFF_HF + (4 * mt2 + ((w + n) & 3)) * 48 * 1024);
    v4u B2[W2][4];
#pragma unroll
    for (int s = 0; s < W2; ++s)
#pragma unroll
        for (int n = 0; n < 4; ++n) B2[s][n] = bld16<SC1>(rsW, lane * 16, b2off[n] + a2o[s]);
    SB();
    if (has_ch) {
        WAIT_V(32);
        lds_arrive(LC_C);
    }
    f32x4 acc2[5][4];
#pragma unroll
    for (int a = 0; a < 5; ++a)
#pragma unroll
        for (int n = 0; n < 4; ++n) acc2[a][n] = f32x4{0.f, 0.f, 0.f, 0.f};
    const int t = 64 * mt2 + 16 * w + (lane & 15), g4 = lane >> 4;
    v2u qv[5];
    float gt;
#pragma unroll
    for (int s = 0; s < 12; ++s) {
        v4u cb[4];
#pragma unroll
        for (int n = 0; n < 4; ++n) cb[n] = B2[s % W2][n];
        if (s + W2 < 12) {
#pragma unroll
            for (int n = 0; n < 4; ++n) B2[s % W2][n] = bld16<SC1>(rsW, lane * 16, b2off[n] + a2o[s + W2]);
        }
        if (s == 6) {
#pragma unroll
            for (int a = 0; a < 5; ++a) qv[a] = *(const GAS v2u*)(p.q + (size_t)t * DM + 80 * dt + 16 * a + 4 * g4);
            gt = __builtin_bit_cast(float, __builtin_amdgcn_raw_buffer_load_b32(rsW, (unsigned)(OFF_GATE + t * 4), 0, SC1));
        }
#pragma unroll
        for (int n = 0; n < 4; ++n)
#pragma unroll
            for (int a = 0; a < 5; ++a) acc2[a][n] = mfma16(A2[s][a], cb[n], acc2[a][n]);
        SB();
    }
    STAMP(p, 9);
    lds_wait(LC_B, 4);
#pragma unroll
    for (int a = 0; a < 5; ++a)
#pragma unroll
        for (int j = 1; j < 4; ++j) lds_st<f32x4>((unsigned)(G2_RED + ((((w + j) & 3) * 5 + a) * 3 + (j - 1)) * 1024 + lane * 16), acc2[a][j]);
    if (w == 3) {
        lds_wait(LC_C, (unsigned)nch);
        {
            const int c0 = ch - 3, g0 = c0 / 80, n0 = min(nch, 80 * (g0 + 1) - c0), gi = g0 + lane, ni = lane == 0 ? n0 : nch - n0;
            if (lane < 2 && ni > 0) {
                const unsigned old = add_count(p, OFF_CC + 256 * gi, (unsigned)ni);
                if (old + (unsigned)ni == 80u) set_flag(p, OFF_RC + 256 * gi, 0, epoch);
            }
        }
        wait_ready<4>(p, OFF_RC + 256 * dt, epoch);
        v4u yv[10];
#pragma unroll
        for (int it = 0; it < 10; ++it) {
            const int c = lane + 64 * it;
            yv[it] = bld16<SC1>(rsW, (unsigned)(OFF_YT + ((80 * dt + (c >> 3)) * SEQ + 64 * mt2 + 8 * (c & 7)) * 2), 0);
        }
#pragma unroll
        for (int it = 0; it < 10; ++it) lds_st<v4u>((unsigned)(G2_Y + (lane + 64 * it) * 16), yv[it]);
    }
    WAIT_L(0);
    BAR();
    const float omg = rb(1.0f - gt);
    const int q = (lane >> 2) & 3, pp = lane & 3;
#pragma unroll
    for (int a = 0; a < 5; ++a) {
        f32x4 b = acc2[a][0];
#pragma unroll
        for (int j = 0; j < 3; ++j) b += lds_ld<f32x4>((unsigned)(G2_RED + ((w * 5 + a) * 3 + j) * 1024 + lane * 16));
        const v2u y = tr_read((unsigned)(G2_Y + (16 * a + 4 * g4 + q) * 128 + 32 * w + 8 * pp));
        const int d0 = 80 * dt + 16 * a + 4 * g4;
        const float o0 = gt * (blo(qv[a].x) * blo(y.x)) + omg * b[0], o1 = gt * (bhi(qv[a].x) * bhi(y.x)) + omg * b[1];
        const float o2 = gt * (blo(qv[a].y) * blo(y.y)) + omg * b[2], o3 = gt * (bhi(qv[a].y) * bhi(y.y)) + omg * b[3];
        *(GAS v2u*)(p.out + (size_t)t * DM + d0) = v2u{pk2(o0, o1), pk2(o2, o3)};
    }
    STAMP(p, 11);
}

extern "C" void kernel_launch(void* const* d_in, const int* in_sizes, int n_in, void* d_out, int out_size, void* d_ws, size_t ws_size, hipStream_t stream) {
    Params p;
    p.q = (const u16*)d_in[0]; p.k = (const u16*)d_in[1]; p.v = (const u16*)d_in[2]; p.sk = (const u16*)d_in[3];
    p.hk = (const u16*)d_in[4]; p.sd = (const u16*)d_in[5];
    p.wg = (const u16*)d_in[7]; p.wu = (const u16*)d_in[8]; p.wd = (const u16*)d_in[9]; p.gw = (const u16*)d_in[10];
    p.out = (u16*)d_out; p.ws = (unsigned char*)d_ws;
    (void)hipFuncSetAttribute((const void*)k_fused, hipFuncAttributeMaxDynamicSharedMemorySize, LDS_BYTES);
    k_fused<<<256, 256, LDS_BYTES, stream>>>(p);
}
```

```cpp
#include <hip/hip_runtime.h>
#include <stdint.h>

#define LAS __attribute__((address_space(3)))
#define GAS __attribute__((address_space(1)))
typedef unsigned short u16;
typedef unsigned v4u __attribute__((ext_vector_type(4)));
typedef unsigned v2u __attribute__((ext_vector_type(2)));
typedef __bf16 bf8 __attribute__((ext_vector_type(8)));
typedef __bf16 bf2 __attribute__((ext_vector_type(2)));
typedef short s4 __attribute__((ext_vector_type(4)));
typedef float f32x2 __attribute__((ext_vector_type(2)));
typedef float f32x4 __attribute__((ext_vector_type(4)));
typedef float f32x16 __attribute__((ext_vector_type(16)));
typedef __amdgpu_buffer_rsrc_t rsrc_t;

constexpr int SEQ = 2048, DM = 640, FF = 1536;
constexpr int OFF_EPOCH = 0;
constexpr int OFF_FA = 32768;
constexpr int OFF_CB = 2048;
constexpr int OFF_CC = 8192;
constexpr int OFF_RB = 12288;
constexpr int OFF_RC = 16384;
constexpr int OFF_STAMP = 32 * 1024 * 1024;
constexpr int OFF_KVR = 65536;
constexpr int OFF_WDT = OFF_KVR + DM * SEQ * 2;
constexpr int OFF_HF = OFF_WDT + FF * DM * 2;
constexpr int OFF_YT = OFF_HF + SEQ * FF * 2;
constexpr int OFF_GATE = OFF_YT + DM * SEQ * 2;
constexpr int WS_BYTES = OFF_GATE + SEQ * 4;
constexpr int SC1 = 16;
constexpr unsigned OOB = 0x80000000u;
constexpr unsigned SPIN_MAX = 1u << 16;

struct Params {
    const u16 *q, *k, *v, *sk, *hk, *sd, *wg, *wu, *wd, *gw;
    u16* out;
    unsigned char* ws;
};

__device__ __forceinline__ unsigned pk2(float a, float b) {
    f32x2 x = {a, b};
    return __builtin_bit_cast(unsigned, __builtin_convertvector(x, bf2));
}
__device__ __forceinline__ float blo(unsigned u) { return __builtin_bit_cast(float, u << 16); }
__device__ __forceinline__ float bhi(unsigned u) { return __builtin_bit_cast(float, u & 0xffff0000u); }
__device__ __forceinline__ float rb(float x) { return blo(pk2(x, x)); }
__device__ __forceinline__ f32x4 mfma16(v4u a, v4u b, f32x4 c) {
    return __builtin_amdgcn_mfma_f32_16x16x32_bf16(__builtin_bit_cast(bf8, a), __builtin_bit_cast(bf8, b), c, 0, 0, 0);
}
__device__ __forceinline__ f32x16 mfma32(v4u a, v4u b, f32x16 c) {
    return __builtin_amdgcn_mfma_f32_32x32x16_bf16(__builtin_bit_cast(bf8, a), __builtin_bit_cast(bf8, b), c, 0, 0, 0);
}
__device__ __forceinline__ float dot2(unsigned a, unsigned b, float c) {
    return __builtin_amdgcn_fdot2_f32_bf16(__builtin_bit_cast(bf2, a), __builtin_bit_cast(bf2, b), c, false);
}
template <class T> __device__ __forceinline__ T lds_ld(unsigned a) { return *(LAS T*)(uintptr_t)a; }
template <class T> __device__ __forceinline__ void lds_st(unsigned a, T v) { *(LAS T*)(uintptr_t)a = v; }
__device__ __forceinline__ v2u tr_read(unsigned a) {
    return __builtin_bit_cast(v2u, __builtin_amdgcn_ds_read_tr16_b64_v4i16((LAS s4*)(uintptr_t)a));
}
__device__ __forceinline__ rsrc_t mkrs(const void* p, unsigned bytes) { return __builtin_amdgcn_make_buffer_rsrc((void*)p, 0, bytes, 0x00020000); }
template <int AUX> __device__ __forceinline__ v4u bld16(rsrc_t rs, unsigned voff, unsigned soff) { return __builtin_amdgcn_raw_buffer_load_b128(rs, voff, soff, AUX); }
template <int AUX> __device__ __forceinline__ void bst16(rsrc_t rs, unsigned voff, unsigned soff, v4u v) { __builtin_amdgcn_raw_buffer_store_b128(v, rs, voff, soff, AUX); }
#define WAIT_V(n) asm volatile("s_waitcnt vmcnt(" #n ")" ::: "memory")
#define WAIT_L(n) asm volatile("s_waitcnt lgkmcnt(" #n ")" ::: "memory")
#define BAR() do { asm volatile("" ::: "memory"); __builtin_amdgcn_s_barrier(); asm volatile("" ::: "memory"); } while (0)
#define SB() __builtin_amdgcn_sched_barrier(0)
#define PIN_S(x) asm volatile("" : "+s"(x))
#define PIN_V(x) asm volatile("" : "+v"(x))
#define RLX_AGENT __ATOMIC_RELAXED, __HIP_MEMORY_SCOPE_AGENT
#define STAMP(p, i) do {} while (0)

__device__ __forceinline__ void dma16(rsrc_t rs, unsigned voff, unsigned soff, unsigned lds_addr) {
    asm volatile("s_mov_b32 m0, %0\n\ts_nop 0\n\tbuffer_load_dwordx4 %1, %2, %3 offen lds" ::"s"(lds_addr), "v"(voff), "s"(rs), "s"(soff) : "memory", "m0");
}
__device__ __forceinline__ void dma16_sc1(rsrc_t rs, unsigned voff, unsigned soff, unsigned lds_addr) {
    asm volatile("s_mov_b32 m0, %0\n\ts_nop 0\n\tbuffer_load_dwordx4 %1, %2, %3 offen sc1 lds" ::"s"(lds_addr), "v"(voff), "s"(rs), "s"(soff) : "memory", "m0");
}
#define SET_M0(base, imm) asm volatile("s_add_u32 m0, %0, %1" ::"s"(base), "i"(imm) : "memory", "m0", "scc")
__device__ __forceinline__ void dma16_m0(rsrc_t rs, unsigned voff, unsigned soff) {
    asm volatile("buffer_load_dwordx4 %0, %1, %2 offen lds" ::"v"(voff), "s"(rs), "s"(soff) : "memory");
}
__device__ __forceinline__ void dma16_m0_sc1(rsrc_t rs, unsigned voff, unsigned soff) {
    asm volatile("buffer_load_dwordx4 %0, %1, %2 offen sc1 lds" ::"v"(voff), "s"(rs), "s"(soff) : "memory");
}
__device__ __forceinline__ void aload16(v4u& d, rsrc_t rs, unsigned voff) {
    asm volatile("buffer_load_dwordx4 %0, %1, %2, 0 offen" : "=v"(d) : "v"(voff), "s"(rs) : "memory");
}

__device__ __forceinline__ void wait_words(rsrc_t rsW, unsigned voff, unsigned e) {
    unsigned t0, left = SPIN_MAX;
    asm volatile(
        "1:\n\t"
        "buffer_load_dword %0, %2, %3, 0 offen sc1\n\t"
        "s_waitcnt vmcnt(0)\n\t"
        "v_cmp_ne_u32 vcc, %4, %0\n\t"
        "s_nop 4\n\t"
        "s_cbranch_vccz 2f\n\t"
        "s_sleep 8\n\t"
        "s_sub_u32 %1, %1, 1\n\t"
        "s_cbranch_scc0 1b\n\t"
        "2:"
        : "=&v"(t0), "+s"(left)
        : "v"(voff), "s"(rsW), "s"(e)
        : "memory", "vcc", "scc");
}
__device__ __forceinline__ void dma4(rsrc_t rs, unsigned voff, unsigned lds_addr) {
    asm volatile("s_mov_b32 m0, %0\n\ts_nop 0\n\tbuffer_load_dword %1, %2, 0 offen lds" ::"s"(lds_addr), "v"(voff), "s"(rs) : "memory", "m0");
}
__device__ __forceinline__ void dma4_sc1(rsrc_t rs, unsigned voff, unsigned lds_addr) {
    asm volatile("s_mov_b32 m0, %0\n\ts_nop 0\n\tbuffer_load_dword %1, %2, 0 offen sc1 lds" ::"s"(lds_addr), "v"(voff), "s"(rs) : "memory", "m0");
}
__device__ __forceinline__ unsigned add_count(const Params& p, int off, unsigned n) {
    return __hip_atomic_fetch_add((GAS unsigned*)(p.ws + off), n, RLX_AGENT);
}
template <int SLEEP>
__device__ __forceinline__ void wait_ready(const Params& p, int off, unsigned e) {
    GAS unsigned* c = (GAS unsigned*)(p.ws + off);
    for (unsigned it = 0; it < SPIN_MAX; ++it) {
        if ((unsigned)__builtin_amdgcn_readfirstlane(__hip_atomic_load(c, RLX_AGENT)) == e) break;
        __builtin_amdgcn_s_sleep(SLEEP);
    }
    __builtin_amdgcn_fence(__ATOMIC_ACQUIRE, "wavefront");
}
__device__ __forceinline__ void lds_arrive(unsigned off) {
    if ((threadIdx.x & 63) == 0) (void)__hip_atomic_fetch_add((LAS unsigned*)(uintptr_t)off, 1u, __ATOMIC_RELAXED, __HIP_MEMORY_SCOPE_WORKGROUP);
}
__device__ __forceinline__ void lds_wait(unsigned off, unsigned n) {
    for (unsigned it = 0; it < (SPIN_MAX << 4); ++it) {
        if ((unsigned)__builtin_amdgcn_readfirstlane(__hip_atomic_load((LAS unsigned*)(uintptr_t)off, __ATOMIC_RELAXED, __HIP_MEMORY_SCOPE_WORKGROUP)) >= n) break;
        __builtin_amdgcn_s_sleep(1);
    }
    asm volatile("" ::: "memory");
}
__device__ __forceinline__ void set_flag(const Params& p, int off, int idx, unsigned e) {
    __hip_atomic_store((GAS unsigned*)(p.ws + off) + idx, e, RLX_AGENT);
}

constexpr int G1_STAGE = 40960, G1_G = 16384, G1_U = 16384 + 12288;
constexpr int SCR = 3 * G1_STAGE;
constexpr int PR_K = SCR;
constexpr int PR_V = PR_K + 11264;
constexpr int PR_S = PR_V + 10240;
constexpr int PR_D = PR_S + 1024;
constexpr int CV_RAW = SCR;
constexpr int CV_XR = SCR + 16384, CV_XRS = 4608;
constexpr int FLG_SCR = CV_XR + 3 * CV_XRS;
constexpr int SD_SCR = FLG_SCR + 1024;
constexpr int DUMMY = PR_D + 10240;
constexpr int LC_B = 163808, LC_C = 163824;
constexpr int CV_CS = 4160, CV_HP = 0, CV_HPS = 4 * CV_CS;
constexpr int G2_Y = 3 * CV_HPS;
constexpr int G2_RED = 61440;
constexpr int LDS_BYTES = 163840;
static_assert(SD_SCR + 1024 <= DUMMY && DUMMY + 4096 <= LC_B && G2_Y + 80 * 128 <= G2_RED && G2_RED + 60 * 1024 <= SCR, "lds");
constexpr int CODE_BYTES = 24576;

__global__ void __launch_bounds__(256, 1) k_fused(Params p) {
    extern __shared__ __attribute__((aligned(16))) unsigned char smem[];
    const int tid = threadIdx.x, lane = tid & 63, w = __builtin_amdgcn_readfirstlane(tid >> 6);
    const int bid = blockIdx.x;
    const int xcd = bid & 7, bi = bid >> 3, mq = xcd >> 1, half = xcd & 1;
    STAMP(p, 0);
    {
        unsigned long long pc;
        asm volatile("s_getpc_b64 %0" : "=s"(pc));
        const rsrc_t rsC = mkrs((const void*)(pc & ~255ull), CODE_BYTES - 256);
        dma16(rsC, (unsigned)(((bi + 32 * w) << 10) + lane * 16), 0, DUMMY);
    }
    const int nch = bid < 128 ? 3 : 2, ch = (bid < 128 ? 3 * bid : 384 + 2 * (bid - 128)) + w;
    const bool has_ch = w < nch;
    asm volatile("" ::"s"(p.q), "s"(p.k), "s"(p.v), "s"(p.sk), "s"(p.hk), "s"(p.sd), "s"(p.wg), "s"(p.wu), "s"(p.wd), "s"(p.gw), "s"(p.out), "s"(p.ws));
    rsrc_t rsW = mkrs(p.ws, WS_BYTES);
    rsrc_t rsQ = mkrs(p.q, SEQ * DM * 2);

    const int ptt = bid & 31, pd8 = bid >> 5;
    auto prep_loads = [&]() {
        const rsrc_t rsK = mkrs(p.k, SEQ * DM * 2), rsV = mkrs(p.v, SEQ * DM * 2), rsS = mkrs(p.sk, DM * 4 * 2), rsD = mkrs(p.wd, FF * DM * 2);
        auto kdma = [&](int i) {
            const int c = 64 * i + lane, row = c / 10, col = c - 10 * row, t = 64 * ptt - 4 + row;
            dma16(rsK, (t >= 0 && c < 680) ? (unsigned)((t * DM + 80 * pd8 + 8 * col) * 2) : OOB, 0, PR_K + 1024 * i);
        };
        auto vdma = [&](int i) {
            const int c = 64 * i + lane, row = c / 10, col = c - 10 * row;
            dma16(rsV, (unsigned)(((64 * ptt + row) * DM + 80 * pd8 + 8 * col) * 2), 0, PR_V + 1024 * i);
        };
        auto ddma = [&](int j, int i) {
            const int g = 2 * bid + j, gdt = g / 48, gks = g - 48 * gdt;
            const int c = 64 * i + lane, row = c / 10, col = c - 10 * row;
            dma16(rsD, g < 384 ? (unsigned)(((32 * gks + row) * DM + 80 * gdt + 8 * col) * 2) : OOB, 0, PR_D + 5120 * j + 1024 * i);
        };
        if (w == 0) {
#pragma unroll
            for (int i = 0; i < 8; ++i) kdma(i);
        } else if (w == 1) {
#pragma unroll
            for (int i = 8; i < 11; ++i) kdma(i);
#pragma unroll
            for (int i = 0; i < 5; ++i) vdma(i);
        } else if (w == 2) {
#pragma unroll
            for (int i = 5; i < 10; ++i) vdma(i);
#pragma unroll
            for (int i = 0; i < 5; ++i) ddma(0, i);
        } else {
            dma16(rsS, lane < 40 ? (unsigned)(80 * pd8 * 8 + 16 * lane) : OOB, 0, PR_S);
#pragma unroll
            for (int i = 0; i < 5; ++i) ddma(1, i);
        }
    };
    v4u pkv[3], pwd[5];
    auto prep_compute = [&]() {
        const int i16 = lane & 15, q = (lane >> 2) & 3, pp = lane & 3;
#pragma unroll
        for (int ps = 0; ps < 3; ++ps) {
            const int quad = min(4 * ps + w, 9), cg = quad >> 1, tgp = 4 * (quad & 1) + (lane >> 4);
            const unsigned ka = (unsigned)(PR_K + (8 * tgp + q) * 160 + (16 * cg + 4 * pp) * 2), va = ka + (PR_V - PR_K);
            const v2u k0 = tr_read(ka), k1 = tr_read(ka + 640), k2 = tr_read(ka + 1280), v0 = tr_read(va), v1 = tr_read(va + 640);
            const v2u tw = lds_ld<v2u>((unsigned)(PR_S + (16 * cg + i16) * 8));
            const unsigned W10 = __builtin_amdgcn_alignbit(tw.x, tw.x, 16), W32 = __builtin_amdgcn_alignbit(tw.y, tw.y, 16);
            const unsigned P[6] = {k0.x, k0.y, k1.x, k1.y, k2.x, k2.y};
            unsigned Q[5];
#pragma unroll
            for (int m = 0; m < 5; ++m) Q[m] = __builtin_amdgcn_alignbit(P[m + 1], P[m], 16);
            const unsigned V[4] = {v0.x, v0.y, v1.x, v1.y};
            float r[8];
#pragma unroll
            for (int m = 0; m < 4; ++m) {
                r[2 * m] = dot2(Q[m], W32, dot2(Q[m + 1], W10, 0.f)) * blo(V[m]);
                r[2 * m + 1] = dot2(P[m + 1], W32, dot2(P[m + 2], W10, 0.f)) * bhi(V[m]);
            }
            pkv[ps] = v4u{pk2(r[7], r[6]), pk2(r[5], r[4]), pk2(r[3], r[2]), pk2(r[1], r[0])};
        }
        const unsigned T = (unsigned)(PR_D + 5120 * (w & 1));
        const int gq = lane >> 4, row = 16 * (gq >> 1) + 4 * (gq & 1) + q;
#pragma unroll
        for (int at = 0; at < 5; ++at) {
            const v2u lo = tr_read(T + row * 160 + (16 * at + 4 * pp) * 2);
            const v2u hi = tr_read(T + (row + 8) * 160 + (16 * at + 4 * pp) * 2);
            pwd[at] = v4u{lo.x, lo.y, hi.x, hi.y};
        }
    };
    prep_loads();
    STAMP(p, 14);

    const int mt1 = 4 * mq + (bi & 3), ft = 8 * half + (bi >> 2);
    const int wm = w & 1, wf = w >> 1;
#define NCOMMON 1
    constexpr int NC = NCOMMON, NR = 10 - NC;
    const int rot1 = NR > 0 ? ((ft & 7) + ((NR * (mt1 & 3)) >> 2)) % (NR > 0 ? NR : 1) : 0;
    rsrc_t rsWg = mkrs(p.wg, DM * FF * 2), rsWu = mkrs(p.wu, DM * FF * 2);
    PIN_S(rsW); PIN_S(rsWg); PIN_S(rsWu); PIN_S(rsQ);
    unsigned xoff[4], goff[3], uoff[3];
#pragma unroll
    for (int n = 0; n < 4; ++n) {
        const int row = 32 * w + 8 * n + (lane >> 3);
        xoff[n] = (unsigned)((128 * mt1 + row) * 1280 + (((lane & 7) ^ ((4 * n + (lane >> 4)) & 7)) << 4));
    }
#pragma unroll
    for (int n = 0; n < 3; ++n) {
        const int Lc = 64 * (3 * w + n) + lane, k = Lc / 12, c = Lc - 12 * k;
        goff[n] = (unsigned)(k * 3072 + 192 * ft + 16 * c);
        uoff[n] = (unsigned)(k * 3072 + 192 * ft + 16 * (c ^ 2));
    }
    auto g1_issue = [&](int kt, int stg) {
#pragma unroll
        for (int n = 0; n < 4; ++n) dma16(rsQ, xoff[n], kt * 128, stg + (4 * w + n) * 1024);
#pragma unroll
        for (int n = 0; n < 3; ++n) {
            dma16(rsWg, goff[n], kt * 64 * 3072, stg + G1_G + (3 * w + n) * 1024);
            dma16(rsWu, uoff[n], kt * 64 * 3072, stg + G1_U + (3 * w + n) * 1024);
        }
    };
    g1_issue(0, 0);
    const unsigned epoch = *(const __attribute__((address_space(4))) unsigned*)(uintptr_t)(p.ws + OFF_EPOCH) + 1u;
    f32x16 acc[3][2];
#pragma unroll
    for (int a = 0; a < 3; ++a)
#pragma unroll
        for (int b = 0; b < 2; ++b)
#pragma unroll
            for (int r = 0; r < 16; ++r) acc[a][b][r] = 0.f;
    const int l31 = lane & 31, hh = lane >> 5;
    const int mt2 = 8 * mq + (bi & 7), dt = 4 * half + (bi >> 3);
    unsigned favo;
    {
        const int c0 = ch - w, g0 = c0 / 80;
        int who = 24 * dt + min(max(lane - 32, 0), 23);
        if (lane < 32 && has_ch) who = 32 * (ch / 80) + lane;
        if (lane == 56) who = mt1;
        if (lane == 57) who = 16 + g0;
        if (lane == 58) who = 16 + min(g0 + 1, 7);
        favo = (unsigned)(OFF_FA + 128 * who);
    }
    const int rot2 = (3 * (bi & 7) / 2 + 3 * (bi >> 3)) % 12;
    unsigned a2o[12];
#pragma unroll
    for (int s = 0; s < 12; ++s) { const int ks = s + rot2; a2o[s] = (unsigned)(12 * w + (ks >= 12 ? ks - 12 : ks)) * 1024u; }
    const unsigned a2base = (unsigned)(OFF_WDT + dt * 48 * 5 * 1024);
    const int brow = 64 * wm + l31;
    unsigned ba[4];
#pragma unroll
    for (int kk = 0; kk < 4; ++kk) ba[kk] = (unsigned)(brow * 128 + (((2 * kk + hh) ^ ((brow >> 1) & 7)) << 4));
    unsigned aa[3];
#pragma unroll
    for (int fb = 0; fb < 3; ++fb) {
        const int fbg = 3 * wf + fb, G = lane >> 4;
        aa[fb] = (unsigned)(((G & 1) ? G1_U + (fbg ^ 1) * 32 : G1_G + fbg * 32) + (8 * (G >> 1) + ((lane >> 2) & 3)) * 192 + 8 * (lane & 3));
    }
    unsigned okv[3], owd, ozero, ofa, oraw, osd, okvr, oh[2];
    {
        const int i16 = lane & 15;
#pragma unroll
        for (int ps = 0; ps < 3; ++ps) {
            const int quad = min(4 * ps + w, 9), cg = quad >> 1, tgp = 4 * (quad & 1) + (lane >> 4);
            okv[ps] = 4 * ps + w < 10 ? (unsigned)(OFF_KVR + ((80 * pd8 + 16 * cg + i16) * SEQ + 2040 - 64 * ptt - 8 * tgp) * 2) : OOB;
        }
        const int g = 2 * bid + (w & 1), gdt = g / 48, gks = g - 48 * gdt;
        owd = (w >= 2 && g < 384) ? (unsigned)(OFF_WDT + (gdt * 48 + gks) * 5 * 1024 + lane * 16) : OOB;
        ozero = (bid < 24 && tid == 0) ? (unsigned)(bid < 16 ? OFF_CB + 256 * bid : OFF_CC + 256 * (bid - 16)) : OOB;
        ofa = tid == 0 ? (unsigned)(OFF_FA + 128 * bid) : OOB;
        oraw = has_ch ? (unsigned)(ch * SEQ * 2 + lane * 16) : OOB;
        osd = has_ch ? (unsigned)((ch & ~1) * 2) : OOB;
        okvr = has_ch ? (unsigned)(OFF_KVR + ch * SEQ * 2 + lane * 16) : OOB;
#pragma unroll
        for (int mt = 0; mt < 2; ++mt) oh[mt] = (unsigned)(OFF_HF + ((128 * mt1 + 64 * wm + 32 * mt + l31) >> 4) * 48 * 1024 + ((lane & 15) + 16 * hh) * 16);
    }
    unsigned a2v0 = (unsigned)(lane * 16), a2v4 = a2v0 + 4096;
    PIN_V(favo); PIN_V(okv[0]); PIN_V(okv[1]); PIN_V(okv[2]); PIN_V(owd); PIN_V(ozero); PIN_V(ofa); PIN_V(oraw); PIN_V(osd); PIN_V(okvr); PIN_V(oh[0]); PIN_V(oh[1]);
    PIN_V(ba[0]); PIN_V(ba[1]); PIN_V(ba[2]); PIN_V(ba[3]); PIN_V(aa[0]); PIN_V(aa[1]); PIN_V(aa[2]); PIN_V(a2v0); PIN_V(a2v4);
    rsrc_t rsH = mkrs(p.hk, DM * SEQ * 2), rsD1 = mkrs(p.sd, DM * 2);
    PIN_S(rsH); PIN_S(rsD1);
    STAMP(p, 1);
    WAIT_V(10);
    BAR();
    STAMP(p, 15);
    int kap = NC > 1 ? 1 : 1 + rot1;
    int kcnt = 1;
    auto knext = [&]() { ++kcnt; kap = kcnt < NC ? kcnt : kcnt == NC ? NC + rot1 : kap == 9 ? NC : kap + 1; };
    g1_issue(kap, G1_STAGE);
    knext();
    prep_compute();
    STAMP(p, 16);

    v4u A2[12][5];
    unsigned a2s[12];
#pragma unroll
    for (int s = 0; s < 12; ++s) a2s[s] = a2base + a2o[s] * 5;
#define A2LD(slot) A2[(slot) / 5][(slot) % 5] = bld16<SC1>(rsW, ((slot) % 5 == 4 ? a2v4 : a2v0 + ((slot) % 5) * 1024), a2s[(slot) / 5])
    auto prep_store = [&]() {
        __builtin_amdgcn_raw_buffer_store_b32(0u, rsW, ozero, 0, SC1);
#pragma unroll
        for (int ps = 0; ps < 3; ++ps) bst16<SC1>(rsW, okv[ps], 0, pkv[ps]);
#pragma unroll
        for (int at = 0; at < 5; ++at) bst16<SC1>(rsW, owd, at * 1024, pwd[at]);
    };

    {
        v4u fb0[2], fb1[2];
        v2u fa0[3][2], fa1[3][2];
#define RDB(d, kk, mt) d = lds_ld<v4u>(ba[kk] + (mt) * 4096)
#define RDA(d, kk, fb, hi) d[fb][hi] = tr_read(aa[fb] + (kk) * 16 * 192 + (hi) * 4 * 192)
#define MM(A, B, fb, mt) acc[fb][mt] = mfma32(v4u{A[fb][0].x, A[fb][0].y, A[fb][1].x, A[fb][1].y}, B[mt], acc[fb][mt])
        WAIT_V(10);
        WAIT_L(0);
        BAR();
        STAMP(p, 2);
        RDB(fb0[0], 0, 0); RDA(fa0, 0, 0, 0); RDA(fa0, 0, 0, 1); RDB(fb0[1], 0, 1); RDA(fa0, 0, 1, 0); RDA(fa0, 0, 1, 1); RDA(fa0, 0, 2, 0); RDA(fa0, 0, 2, 1);
        int st = 0;
        unsigned mx = 2 * G1_STAGE + 4096u * w, mg = 2 * G1_STAGE + G1_G + 3072u * w, mu = 2 * G1_STAGE + G1_U + 3072u * w;
        unsigned sx = kap * 128, sw = kap * 64 * 3072;
#define STEP(FA, FB, GA, GB, kn, D0, D1, D2)                                                     \
            MM(FA, FB, 0, 0); SB(); RDB(GB[0], kn, 0); RDA(GA, kn, 0, 0); SB();                 \
            MM(FA, FB, 0, 1); SB(); RDA(GA, kn, 0, 1); RDB(GB[1], kn, 1); SB();                 \
            MM(FA, FB, 1, 0); SB(); RDA(GA, kn, 1, 0); RDA(GA, kn, 1, 1); D0##_M; SB();         \
            MM(FA, FB, 1, 1); SB(); D0##_L; RDA(GA, kn, 2, 0); D1##_M; SB();                    \
            MM(FA, FB, 2, 0); SB(); D1##_L; RDA(GA, kn, 2, 1); D2##_M; SB();                    \
            MM(FA, FB, 2, 1); SB(); D2##_L; SB();
#define X0_M SET_M0(mx, 0)
#define X0_L dma16_m0(rsQ, xoff[0], sx)
#define X1_M SET_M0(mx, 1024)
#define X1_L dma16_m0(rsQ, xoff[1], sx)
#define X2_M SET_M0(mx, 2048)
#define X2_L dma16_m0(rsQ, xoff[2], sx)
#define X3_M SET_M0(mx, 3072)
#define X3_L dma16_m0(rsQ, xoff[3], sx)
#define G0_M SET_M0(mg, 0)
#define G0_L dma16_m0(rsWg, goff[0], sw)
#define G1_M SET_M0(mg, 1024)
#define G1_L dma16_m0(rsWg, goff[1], sw)
#define G2_M SET_M0(mg, 2048)
#define G2_L dma16_m0(rsWg, goff[2], sw)
#define U0_M SET_M0(mu, 0)
#define U0_L dma16_m0(rsWu, uoff[0], sw)
#define U1_M SET_M0(mu, 1024)
#define U1_L dma16_m0(rsWu, uoff[1], sw)
#define U2_M SET_M0(mu, 2048)
#define U2_L dma16_m0(rsWu, uoff[2], sw)
#define KBODY(WAITV) { \
            const int dlt = (st == 2 * G1_STAGE) ? -2 * G1_STAGE : G1_STAGE; \
            SB(); \
            STEP(fa0, fb0, fa1, fb1, 1, X0, X1, X2) \
            STEP(fa1, fb1, fa0, fb0, 2, X3, G0, U0) \
            STEP(fa0, fb0, fa1, fb1, 3, G1, U1, G2) \
            U2_M; SB(); \
            MM(fa1, fb1, 0, 0); SB(); U2_L; ba[0] += dlt; aa[0] += dlt; SB(); \
            MM(fa1, fb1, 0, 1); SB(); aa[1] += dlt; aa[2] += dlt; \
            knext(); \
            mx = (unsigned)st + 4096u * w; mg = (unsigned)st + G1_G + 3072u * w; mu = (unsigned)st + G1_U + 3072u * w; sx = kap * 128; sw = kap * 64 * 3072; \
            SB(); \
            MM(fa1, fb1, 1, 0); SB(); \
            WAIT_L(0); \
            WAITV; \
            BAR(); \
            RDB(fb0[0], 0, 0); RDA(fa0, 0, 0, 0); RDA(fa0, 0, 0, 1); SB(); \
            MM(fa1, fb1, 1, 1); SB(); RDB(fb0[1], 0, 1); RDA(fa0, 0, 1, 0); RDA(fa0, 0, 1, 1); SB(); \
            MM(fa1, fb1, 2, 0); SB(); RDA(fa0, 0, 2, 0); RDA(fa0, 0, 2, 1); ba[1] += dlt; SB(); \
            MM(fa1, fb1, 2, 1); SB(); ba[2] += dlt; ba[3] += dlt; \
            st += dlt; \
            }
#pragma unroll 1
        for (int kt = 0; kt < 8; ++kt) {
            KBODY(if (__builtin_expect((0x70 >> kt) & 1, 0)) { if (kt == 4) WAIT_V(19); else if (kt == 5) WAIT_V(15); else WAIT_V(11); } else WAIT_V(10))
            if (__builtin_expect((0xB8 >> kt) & 1, 0)) {
                if (kt == 3) prep_store();
                else if (kt == 4) {
#pragma unroll
                    for (int n = 0; n < 4; ++n) dma16(rsH, oraw, n * 1024, CV_RAW + 4096u * w + 1024u * n);
                    dma4(rsD1, osd, SD_SCR + 256u * w);
                } else if (kt == 5) {
                    __builtin_amdgcn_raw_buffer_store_b32(epoch, rsW, ofa, 0, SC1);
                } else dma4_sc1(rsW, favo, FLG_SCR + 256u * w);
            }
        }
#define N_M do {} while (0)
#define N_L do {} while (0)
#define STEPA(FA, FB, GA, GB, kn, s0)                                                            \
            MM(FA, FB, 0, 0); SB(); A2LD(s0); RDB(GB[0], kn, 0); RDA(GA, kn, 0, 0); SB();       \
            MM(FA, FB, 0, 1); SB(); A2LD(s0 + 1); RDA(GA, kn, 0, 1); RDB(GB[1], kn, 1); SB();   \
            MM(FA, FB, 1, 0); SB(); A2LD(s0 + 2); RDA(GA, kn, 1, 0); RDA(GA, kn, 1, 1); SB();   \
            MM(FA, FB, 1, 1); SB(); A2LD(s0 + 3); RDA(GA, kn, 2, 0); SB();                      \
            MM(FA, FB, 2, 0); SB(); A2LD(s0 + 4); RDA(GA, kn, 2, 1); SB();                      \
            MM(FA, FB, 2, 1); SB(); A2LD(s0 + 5); SB();
        {
            const int dlt = (st == 2 * G1_STAGE) ? -2 * G1_STAGE : G1_STAGE;
            SB();
            STEP(fa0, fb0, fa1, fb1, 1, N, N, N)
            STEP(fa1, fb1, fa0, fb0, 2, N, N, N)
            STEP(fa0, fb0, fa1, fb1, 3, N, N, N)
            MM(fa1, fb1, 0, 0); SB(); ba[0] += dlt; aa[0] += dlt; SB();
            MM(fa1, fb1, 0, 1); SB(); aa[1] += dlt; aa[2] += dlt; SB();
            MM(fa1, fb1, 1, 0); SB();
            if (tid == 0) { lds_st<unsigned>(LC_B, 0u); lds_st<unsigned>(LC_C, 0u); }
            WAIT_L(0);
            WAIT_V(0);
            BAR();
            RDB(fb0[0], 0, 0); RDA(fa0, 0, 0, 0); RDA(fa0, 0, 0, 1); SB();
            MM(fa1, fb1, 1, 1); SB(); RDB(fb0[1], 0, 1); RDA(fa0, 0, 1, 0); RDA(fa0, 0, 1, 1); SB();
            MM(fa1, fb1, 2, 0); SB(); RDA(fa0, 0, 2, 0); RDA(fa0, 0, 2, 1); ba[1] += dlt; SB();
            MM(fa1, fb1, 2, 1); SB(); ba[2] += dlt; ba[3] += dlt;
            {
                const unsigned flv = lds_ld<unsigned>((unsigned)(FLG_SCR + 256 * w + lane * 4));
                if (__builtin_amdgcn_ballot_w64(flv != epoch) != 0ull) wait_words(rsW, favo, epoch);
#pragma unroll
                for (int n = 0; n < 4; ++n) dma16_sc1(rsW, okvr, n * 1024, (has_ch ? (unsigned)(CV_XR + CV_XRS * w) : (unsigned)DUMMY) + n * 1024);
            }
            A2LD(0); SB();
            STEPA(fa0, fb0, fa1, fb1, 1, 1)
            STEPA(fa1, fb1, fa0, fb0, 2, 7)
            STEPA(fa0, fb0, fa1, fb1, 3, 13)
            MM(fa1, fb1, 0, 0); SB(); A2LD(19); SB();
            MM(fa1, fb1, 0, 1); SB(); A2LD(20); SB();
            MM(fa1, fb1, 1, 0); SB();
            WAIT_L(0);
            BAR();
            A2LD(21); SB();
            MM(fa1, fb1, 1, 1); SB(); A2LD(22); SB();
            MM(fa1, fb1, 2, 0); SB(); A2LD(23); SB();
            MM(fa1, fb1, 2, 1); SB(); A2LD(24); A2LD(25); SB();
        }
#undef STEPA
#undef MM
#undef STEP
#undef KBODY
    }
    STAMP(p, 3);
#pragma unroll
    for (int fb = 0; fb < 3; ++fb)
#pragma unroll
        for (int mt = 0; mt < 2; ++mt) {
            const f32x16& a = acc[fb][mt];
            v4u o;
#pragma unroll
            for (int j = 0; j < 4; ++j) o[j] = pk2(a[2 * j] * a[8 + 2 * j], a[2 * j + 1] * a[8 + 2 * j + 1]);
            const int Ft = 6 * ft + 3 * wf + fb;
            bst16<SC1>(rsW, oh[mt], (unsigned)((Ft >> 1) * 1024 + (Ft & 1) * 512), o);
        }
    STAMP(p, 4);

    constexpr int W2 = 6;
    unsigned b2off[4];
#pragma unroll
    for (int n = 0; n < 4; ++n) b2off[n] = (unsigned)(OFF_HF + (4 * mt2 + ((w + n) & 3)) * 48 * 1024);
    v4u B2[W2][4];
    auto b2_ring = [&]() {
#pragma unroll
        for (int s = 0; s < W2; ++s)
#pragma unroll
            for (int n = 0; n < 4; ++n) B2[s][n] = bld16<SC1>(rsW, a2v0, b2off[n] + a2o[s]);
    };
    if (has_ch) {
        const unsigned HP = (unsigned)(CV_HP + w * CV_HPS), XR = (unsigned)(CV_XR + w * CV_XRS), RAW = (unsigned)(CV_RAW + w * 4096);
        WAIT_V(32);
        v4u c0[5];
        v2u c1[5];
        unsigned ra0 = RAW + 16 * max(lane - 2, 0), ra1 = RAW + 16 * max(lane - 1, 0), ra2 = RAW + 16 * (lane - 2), ra3 = RAW + 16 * (lane - 1), ra4 = RAW + 16 * (254 + (lane & 1));
        PIN_V(ra0); PIN_V(ra1); PIN_V(ra2); PIN_V(ra3); PIN_V(ra4);
#pragma unroll
        for (int ps = 0; ps < 5; ++ps) {
            c0[ps] = lds_ld<v4u>(ps == 0 ? ra0 : ps < 4 ? ra2 + 1024 * ps : ra4);
            c1[ps] = lds_ld<v2u>(ps == 0 ? ra1 : ps < 4 ? ra3 + 1024 * ps : RAW + 16 * 255);
        }
        if (lane < 2) c0[0] = v4u{0, 0, 0, 0};
        if (lane < 1) c1[0] = v2u{0, 0};
        if (lane & 1) c1[4] = v2u{0, 0};
#pragma unroll
        for (int ps = 0; ps < 5; ++ps) {
            const int n = 64 * ps + lane;
            const unsigned Dw[6] = {c0[ps].x, c0[ps].y, c0[ps].z, c0[ps].w, c1[ps].x, c1[ps].y};
            v4u o0, o1, o2, o3;
#pragma unroll
            for (int q = 0; q < 4; ++q) {
                o0[q] = __builtin_amdgcn_alignbit(Dw[q + 1], Dw[q], 16);
                o1[q] = Dw[q + 1];
                o2[q] = __builtin_amdgcn_alignbit(Dw[q + 2], Dw[q + 1], 16);
                o3[q] = Dw[q + 2];
            }
            if (ps < 4 || lane < 2) {
                lds_st<v4u>(HP + 0 * CV_CS + 16 * n, o0);
                lds_st<v4u>(HP + 1 * CV_CS + 16 * n, o1);
                lds_st<v4u>(HP + 2 * CV_CS + 16 * n, o2);
                lds_st<v4u>(HP + 3 * CV_CS + 16 * n, o3);
            }
        }
        if (lane < 32) lds_st<v4u>(XR + 4096 + 16 * lane, v4u{0, 0, 0, 0});
        asm volatile("" ::: "memory");
        STAMP(p, 5);

        const int r = lane & 15, g = lane >> 4;
        unsigned ap0 = HP + (unsigned)((r & 3) * CV_CS + 2 * ((r & ~3) + 8 * g));
        unsigned ap1 = ap0 + 8;
        PIN_V(ap0); PIN_V(ap1);
        unsigned bp = XR + (unsigned)(2 * (240 - 16 * r + 8 * g));
        PIN_V(bp);
        unsigned rbv = 0;
        f32x4 cacc[8];
#pragma unroll
        for (int i = 0; i < 8; ++i) cacc[i] = f32x4{0.f, 0.f, 0.f, 0.f};
        v4u CA[2][8], CB[2][8];
        auto cvrd = [&](int set, int s, int i) {
            CB[set][i] = lds_ld<v4u>(bp + 512 * (7 - i) + 64 * s);
            const v2u lo = lds_ld<v2u>(ap0 + 512 * i + 64 * s), hi = lds_ld<v2u>(ap1 + 512 * i + 64 * s);
            CA[set][i] = v4u{lo.x, lo.y, hi.x, hi.y};
        };
#pragma unroll
        for (int i = 0; i < 8; ++i) cvrd(0, 0, i);
#pragma unroll
        for (int s = 0; s < 8; ++s) {
            const int cur = s & 1;
            SB();
#pragma unroll
            for (int gm = 0; gm < 8; ++gm) {
#pragma unroll
                for (int be = 0; be <= gm; ++be) cacc[gm] = mfma16(CA[cur][be], CB[cur][gm - be], cacc[gm]);
                SB();
                if (s < 7) cvrd(cur ^ 1, s + 1, gm);
                { const int i = 8 * s + gm; if (((i + 1) * 34) / 64 != (i * 34) / 64) A2LD(26 + (i * 34) / 64); }
                SB();
            }
            if (s == 1) {
                WAIT_V(8);
                lds_arrive(LC_B);
            }
            if (s == 5) rbv = __hip_atomic_load((GAS unsigned*)(p.ws + OFF_RB + 256 * (mt2 >> 1)), RLX_AGENT);
        }
        STAMP(p, 6);
        if ((unsigned)__builtin_amdgcn_readfirstlane(rbv) != epoch) wait_ready<4>(p, OFF_RB + 256 * (mt2 >> 1), epoch);
        __builtin_amdgcn_fence(__ATOMIC_ACQUIRE, "wavefront");
        b2_ring();
        SB();
        const unsigned sdv = lds_ld<unsigned>((unsigned)(SD_SCR + 256 * w));
        const float Dc = (ch & 1) ? bhi(sdv) : blo(sdv);
#pragma unroll
        for (int gm = 0; gm < 8; ++gm) {
            const int t0 = 256 * gm + 16 * r + 4 * g;
            const v2u kvp = lds_ld<v2u>(XR + 2 * (2044 - t0));
            const float y0 = cacc[gm][0] + bhi(kvp.y) * Dc, y1 = cacc[gm][1] + blo(kvp.y) * Dc, y2 = cacc[gm][2] + bhi(kvp.x) * Dc, y3 = cacc[gm][3] + blo(kvp.x) * Dc;
            lds_st<v2u>(HP + 2 * t0, v2u{pk2(y0, y1), pk2(y2, y3)});
        }
        asm volatile("" ::: "memory");
#pragma unroll
        for (int ps = 0; ps < 4; ++ps) {
            const int n = 64 * ps + lane;
            bst16<SC1>(rsW, (unsigned)(OFF_YT + (ch * SEQ + 8 * n) * 2), 0, lds_ld<v4u>(HP + 16 * n));
        }
    } else {
        if (w == 3) {
            const rsrc_t rsG = mkrs(p.gw, DM * 2);
            const int c16 = lane & 15, row0 = 128 * mt1 + 8 * ft + (lane >> 4);
            v4u gwv[5], xa[5], xb[5];
#pragma unroll
            for (int i = 0; i < 5; ++i) {
                gwv[i] = bld16<0>(rsG, (unsigned)(16 * (c16 + 16 * i)), 0);
                xa[i] = bld16<0>(rsQ, (unsigned)(row0 * DM * 2 + 16 * (c16 + 16 * i)), 0);
                xb[i] = bld16<0>(rsQ, (unsigned)((row0 + 4) * DM * 2 + 16 * (c16 + 16 * i)), 0);
            }
            float s0 = 0.f, s1 = 0.f;
#pragma unroll
            for (int i = 0; i < 5; ++i)
#pragma unroll
                for (int e = 0; e < 4; ++e) { s0 = dot2(xa[i][e], gwv[i][e], s0); s1 = dot2(xb[i][e], gwv[i][e], s1); }
#pragma unroll
            for (int m = 1; m < 16; m <<= 1) { s0 += __shfl_xor(s0, m); s1 += __shfl_xor(s1, m); }
            const float g0 = rb(1.0f / (1.0f + __expf(-rb(s0)))), g1 = rb(1.0f / (1.0f + __expf(-rb(s1))));
            __builtin_amdgcn_raw_buffer_store_b32(__builtin_bit_cast(unsigned, g0), rsW, c16 == 0 ? (unsigned)(OFF_GATE + row0 * 4) : OOB, 0, SC1);
            __builtin_amdgcn_raw_buffer_store_b32(__builtin_bit_cast(unsigned, g1), rsW, c16 == 0 ? (unsigned)(OFF_GATE + (row0 + 4) * 4) : OOB, 0, SC1);
        }
        WAIT_V(0);
        lds_arrive(LC_B);
        lds_wait(LC_B, 4);
        if (w == 3) {
            if (lane == 0) {
                const unsigned old = add_count(p, OFF_CB + 256 * mt1, 1u);
                if (old == 15u) set_flag(p, OFF_RB + 256 * mt1, 0, epoch);
            }
            if (bid == 0) {
#pragma unroll 1
                for (int i = 0; i < 4; ++i) wait_words(rsW, (unsigned)(OFF_FA + 128 * (64 * i + lane)), epoch);
                if (lane == 0) set_flag(p, OFF_EPOCH, 0, epoch);
            }
        }
#pragma unroll
        for (int slot = 26; slot < 60; ++slot) A2LD(slot);
        wait_ready<4>(p, OFF_RB + 256 * (mt2 >> 1), epoch);
        b2_ring();
    }
    STAMP(p, 7);

    f32x4 acc2[5][4];
#pragma unroll
    for (int a = 0; a < 5; ++a)
#pragma unroll
        for (int n = 0; n < 4; ++n) acc2[a][n] = f32x4{0.f, 0.f, 0.f, 0.f};
    const int t = 64 * mt2 + 16 * w + (lane & 15), g4 = lane >> 4;
    v2u qv[5];
    float gt;
#pragma unroll
    for (int s = 0; s < 12; ++s) {
        v4u cb[4];
#pragma unroll
        for (int n = 0; n < 4; ++n) cb[n] = B2[s % W2][n];
        if (s + W2 < 12) {
#pragma unroll
            for (int n = 0; n < 4; ++n) B2[s % W2][n] = bld16<SC1>(rsW, a2v0, b2off[n] + a2o[s + W2]);
        }
        if (s == 2 && has_ch) {
            WAIT_V(12);
            lds_arrive(LC_C);
        }
        if (s == 6) {
#pragma unroll
            for (int a = 0; a < 5; ++a) qv[a] = *(const GAS v2u*)(p.q + (size_t)t * DM + 80 * dt + 16 * a + 4 * g4);
            gt = __builtin_bit_cast(float, __builtin_amdgcn_raw_buffer_load_b32(rsW, (unsigned)(OFF_GATE + t * 4), 0, SC1));
        }
#pragma unroll
        for (int n = 0; n < 4; ++n)
#pragma unroll
            for (int a = 0; a < 5; ++a) acc2[a][n] = mfma16(A2[s][a], cb[n], acc2[a][n]);
        SB();
    }
    STAMP(p, 9);
    lds_wait(LC_B, 4);
#pragma unroll
    for (int a = 0; a < 5; ++a)
#pragma unroll
        for (int j = 1; j < 4; ++j) lds_st<f32x4>((unsigned)(G2_RED + ((((w + j) & 3) * 5 + a) * 3 + (j - 1)) * 1024 + lane * 16), acc2[a][j]);
    if (w == 3) {
        lds_wait(LC_C, (unsigned)nch);
        {
            const int c0 = ch - 3, g0 = c0 / 80, n0 = min(nch, 80 * (g0 + 1) - c0), gi = g0 + lane, ni = lane == 0 ? n0 : nch - n0;
            if (lane < 2 && ni > 0) {
                const unsigned old = add_count(p, OFF_CC + 256 * gi, (unsigned)ni);
                if (old + (unsigned)ni == 80u) set_flag(p, OFF_RC + 256 * gi, 0, epoch);
            }
        }
        wait_ready<4>(p, OFF_RC + 256 * dt, epoch);
        v4u yv[10];
#pragma unroll
        for (int it = 0; it < 10; ++it) {
            const int c = lane + 64 * it;
            yv[it] = bld16<SC1>(rsW, (unsigned)(OFF_YT + ((80 * dt + (c >> 3)) * SEQ + 64 * mt2 + 8 * (c & 7)) * 2), 0);
        }
#pragma unroll
        for (int it = 0; it < 10; ++it) lds_st<v4u>((unsigned)(G2_Y + (lane + 64 * it) * 16), yv[it]);
    }
    WAIT_L(0);
    BAR();
    const float omg = rb(1.0f - gt);
    const int q = (lane >> 2) & 3, pp = lane & 3;
#pragma unroll
    for (int a = 0; a < 5; ++a) {
        f32x4 b = acc2[a][0];
#pragma unroll
        for (int j = 0; j < 3; ++j) b += lds_ld<f32x4>((unsigned)(G2_RED + ((w * 5 + a) * 3 + j) * 1024 + lane * 16));
        const v2u y = tr_read((unsigned)(G2_Y + (16 * a + 4 * g4 + q) * 128 + 32 * w + 8 * pp));
        const int d0 = 80 * dt + 16 * a + 4 * g4;
        const float o0 = gt * (blo(qv[a].x) * blo(y.x)) + omg * b[0], o1 = gt * (bhi(qv[a].x) * bhi(y.x)) + omg * b[1];
        const float o2 = gt * (blo(qv[a].y) * blo(y.y)) + omg * b[2], o3 = gt * (bhi(qv[a].y) * bhi(y.y)) + omg * b[3];
        *(GAS v2u*)(p.out + (size_t)t * DM + d0) = v2u{pk2(o0, o1), pk2(o2, o3)};
    }
    STAMP(p, 11);
}

extern "C" void kernel_launch(void* const* d_in, const int* in_sizes, int n_in, void* d_out, int out_size, void* d_ws, size_t ws_size, hipStream_t stream) {
    Params p;
    p.q = (const u16*)d_in[0]; p.k = (const u16*)d_in[1]; p.v = (const u16*)d_in[2]; p.sk = (const u16*)d_in[3];
    p.hk = (const u16*)d_in[4]; p.sd = (const u16*)d_in[5];
    p.wg = (const u16*)d_in[7]; p.wu = (const u16*)d_in[8]; p.wd = (const u16*)d_in[9]; p.gw = (const u16*)d_in[10];
    p.out = (u16*)d_out; p.ws = (unsigned char*)d_ws;
    (void)hipFuncSetAttribute((const void*)k_fused, hipFuncAttributeMaxDynamicSharedMemorySize, LDS_BYTES);
    k_fused<<<256, 256, LDS_BYTES, stream>>>(p);
}
```

```cpp
#include <hip/hip_runtime.h>
#include <stdint.h>

#define LAS __attribute__((address_space(3)))
#define GAS __attribute__((address_space(1)))
typedef unsigned short u16;
typedef unsigned v4u __attribute__((ext_vector_type(4)));
typedef unsigned v2u __attribute__((ext_vector_type(2)));
typedef __bf16 bf8 __attribute__((ext_vector_type(8)));
typedef __bf16 bf2 __attribute__((ext_vector_type(2)));
typedef short s4 __attribute__((ext_vector_type(4)));
typedef float f32x2 __attribute__((ext_vector_type(2)));
typedef float f32x4 __attribute__((ext_vector_type(4)));
typedef float f32x16 __attribute__((ext_vector_type(16)));
typedef __amdgpu_buffer_rsrc_t rsrc_t;

constexpr int SEQ = 2048, DM = 640, FF = 1536;
constexpr int OFF_EPOCH = 0;
constexpr int OFF_FA = 32768;
constexpr int OFF_KVR = 65536;
constexpr int OFF_WDT = OFF_KVR + DM * SEQ * 2;
constexpr int OFF_HF = OFF_WDT + FF * DM * 2;
constexpr int OFF_YT = OFF_HF + SEQ * FF * 2;
constexpr int OFF_GATE = OFF_YT + DM * SEQ * 2;
constexpr int OFF_FB = OFF_GATE + SEQ * 4;
constexpr int OFF_FC = OFF_FB + 256 * 128;
constexpr int WS_BYTES = OFF_FC + 256 * 128;
constexpr int SC1 = 16;
constexpr unsigned OOB = 0x80000000u;
constexpr unsigned SPIN_MAX = 1u << 16;

struct Params {
    const u16 *q, *k, *v, *sk, *hk, *sd, *wg, *wu, *wd, *gw;
    u16* out;
    unsigned char* ws;
};

__device__ __forceinline__ unsigned pk2(float a, float b) {
    f32x2 x = {a, b};
    return __builtin_bit_cast(unsigned, __builtin_convertvector(x, bf2));
}
__device__ __forceinline__ float blo(unsigned u) { return __builtin_bit_cast(float, u << 16); }
__device__ __forceinline__ float bhi(unsigned u) { return __builtin_bit_cast(float, u & 0xffff0000u); }
__device__ __forceinline__ float rb(float x) { return blo(pk2(x, x)); }
__device__ __forceinline__ f32x4 mfma16(v4u a, v4u b, f32x4 c) {
    return __builtin_amdgcn_mfma_f32_16x16x32_bf16(__builtin_bit_cast(bf8, a), __builtin_bit_cast(bf8, b), c, 0, 0, 0);
}
__device__ __forceinline__ f32x16 mfma32(v4u a, v4u b, f32x16 c) {
    return __builtin_amdgcn_mfma_f32_32x32x16_bf16(__builtin_bit_cast(bf8, a), __builtin_bit_cast(bf8, b), c, 0, 0, 0);
}
__device__ __forceinline__ float dot2(unsigned a, unsigned b, float c) {
    return __builtin_amdgcn_fdot2_f32_bf16(__builtin_bit_cast(bf2, a), __builtin_bit_cast(bf2, b), c, false);
}
template <class T> __device__ __forceinline__ T lds_ld(unsigned a) { return *(LAS T*)(uintptr_t)a; }
template <class T> __device__ __forceinline__ void lds_st(unsigned a, T v) { *(LAS T*)(uintptr_t)a = v; }
__device__ __forceinline__ v2u tr_read(unsigned a) {
    return __builtin_bit_cast(v2u, __builtin_amdgcn_ds_read_tr16_b64_v4i16((LAS s4*)(uintptr_t)a));
}
__device__ __forceinline__ rsrc_t mkrs(const void* p, unsigned bytes) { return __builtin_amdgcn_make_buffer_rsrc((void*)p, 0, bytes, 0x00020000); }
template <int AUX> __device__ __forceinline__ v4u bld16(rsrc_t rs, unsigned voff, unsigned soff) { return __builtin_amdgcn_raw_buffer_load_b128(rs, voff, soff, AUX); }
template <int AUX> __device__ __forceinline__ void bst16(rsrc_t rs, unsigned voff, unsigned soff, v4u v) { __builtin_amdgcn_raw_buffer_store_b128(v, rs, voff, soff, AUX); }
#define WAIT_V(n) asm volatile("s_waitcnt vmcnt(" #n ")" ::: "memory")
#define WAIT_L(n) asm volatile("s_waitcnt lgkmcnt(" #n ")" ::: "memory")
#define BAR() do { asm volatile("" ::: "memory"); __builtin_amdgcn_s_barrier(); asm volatile("" ::: "memory"); } while (0)
#define SB() __builtin_amdgcn_sched_barrier(0)
#define PIN_S(x) asm volatile("" : "+s"(x))
#define PIN_V(x) asm volatile("" : "+v"(x))
#define RLX_AGENT __ATOMIC_RELAXED, __HIP_MEMORY_SCOPE_AGENT

__device__ __forceinline__ void dma16(rsrc_t rs, unsigned voff, unsigned soff, unsigned lds_addr) {
    asm volatile("s_mov_b32 m0, %0\n\ts_nop 0\n\tbuffer_load_dwordx4 %1, %2, %3 offen lds" ::"s"(lds_addr), "v"(voff), "s"(rs), "s"(soff) : "memory", "m0");
}
__device__ __forceinline__ void dma16_sc1(rsrc_t rs, unsigned voff, unsigned soff, unsigned lds_addr) {
    asm volatile("s_mov_b32 m0, %0\n\ts_nop 0\n\tbuffer_load_dwordx4 %1, %2, %3 offen sc1 lds" ::"s"(lds_addr), "v"(voff), "s"(rs), "s"(soff) : "memory", "m0");
}
#define SET_M0(base, imm) asm volatile("s_add_u32 m0, %0, %1" ::"s"(base), "i"(imm) : "memory", "m0", "scc")
__device__ __forceinline__ void dma16_m0(rsrc_t rs, unsigned voff, unsigned soff) {
    asm volatile("buffer_load_dwordx4 %0, %1, %2 offen lds" ::"v"(voff), "s"(rs), "s"(soff) : "memory");
}
__device__ __forceinline__ void wait_words(rsrc_t rsW, unsigned voff, unsigned e) {
    unsigned t0, left = SPIN_MAX;
    asm volatile(
        "1:\n\t"
        "buffer_load_dword %0, %2, %3, 0 offen sc1\n\t"
        "s_waitcnt vmcnt(0)\n\t"
        "v_cmp_ne_u32 vcc, %4, %0\n\t"
        "s_nop 4\n\t"
        "s_cbranch_vccz 2f\n\t"
        "s_sleep 8\n\t"
        "s_sub_u32 %1, %1, 1\n\t"
        "s_cbranch_scc0 1b\n\t"
        "2:"
        : "=&v"(t0), "+s"(left)
        : "v"(voff), "s"(rsW), "s"(e)
        : "memory", "vcc", "scc");
}
__device__ __forceinline__ void dma4(rsrc_t rs, unsigned voff, unsigned lds_addr) {
    asm volatile("s_mov_b32 m0, %0\n\ts_nop 0\n\tbuffer_load_dword %1, %2, 0 offen lds" ::"s"(lds_addr), "v"(voff), "s"(rs) : "memory", "m0");
}
__device__ __forceinline__ void dma4_sc1(rsrc_t rs, unsigned voff, unsigned lds_addr) {
    asm volatile("s_mov_b32 m0, %0\n\ts_nop 0\n\tbuffer_load_dword %1, %2, 0 offen sc1 lds" ::"s"(lds_addr), "v"(voff), "s"(rs) : "memory", "m0");
}
template <int SLEEP>
__device__ __forceinline__ void wait_words_c(rsrc_t rsW, unsigned voff, unsigned e) {
    for (unsigned it = 0; it < SPIN_MAX; ++it) {
        asm volatile("" ::: "memory");
        const unsigned v = __builtin_amdgcn_raw_buffer_load_b32(rsW, voff, 0, SC1);
        if (__builtin_amdgcn_ballot_w64(v != e) == 0ull) break;
        __builtin_amdgcn_s_sleep(SLEEP);
    }
    __builtin_amdgcn_fence(__ATOMIC_ACQUIRE, "wavefront");
}
template <unsigned SK_>
__device__ __forceinline__ void kwait(unsigned kt) {
    asm volatile("s_bitcmp1_b32 %1, %0\n\ts_cbranch_scc1 1f\n\ts_waitcnt vmcnt(10)\n"
                 "4:\n\t.subsection 1\n"
                 "1:\n\ts_cmp_eq_u32 %0, %2\n\ts_cbranch_scc0 2f\n\ts_waitcnt vmcnt(18)\n\ts_branch 4b\n"
                 "2:\n\ts_cmp_eq_u32 %0, %3\n\ts_cbranch_scc0 3f\n\ts_waitcnt vmcnt(15)\n\ts_branch 4b\n"
                 "3:\n\ts_waitcnt vmcnt(11)\n\ts_branch 4b\n\t.subsection 0" ::"s"(kt), "s"(7u << (SK_ + 1)), "n"(SK_ + 1), "n"(SK_ + 2) : "memory", "scc");
}
__device__ __forceinline__ void lds_arrive(unsigned off) {
    if ((threadIdx.x & 63) == 0) (void)__hip_atomic_fetch_add((LAS unsigned*)(uintptr_t)off, 1u, __ATOMIC_RELAXED, __HIP_MEMORY_SCOPE_WORKGROUP);
}
__device__ __forceinline__ void lds_wait(unsigned off, unsigned n) {
    for (unsigned it = 0; it < (SPIN_MAX << 4); ++it) {
        if ((unsigned)__builtin_amdgcn_readfirstlane(__hip_atomic_load((LAS unsigned*)(uintptr_t)off, __ATOMIC_RELAXED, __HIP_MEMORY_SCOPE_WORKGROUP)) >= n) break;
        __builtin_amdgcn_s_sleep(1);
    }
    asm volatile("" ::: "memory");
}
__device__ __forceinline__ void set_flag(const Params& p, int off, int idx, unsigned e) {
    __hip_atomic_store((GAS unsigned*)(p.ws + off) + idx, e, RLX_AGENT);
}

constexpr int G1_STAGE = 40960, G1_G = 16384, G1_U = 16384 + 12288;
constexpr int SCR = 3 * G1_STAGE;
constexpr int PR_K = SCR;
constexpr int PR_V = PR_K + 11264;
constexpr int PR_S = PR_V + 10240;
constexpr int PR_D = PR_S + 1024;
constexpr int CV_RAW = SCR;
constexpr int CV_XR = SCR + 16384, CV_XRS = 4608;
constexpr int FLG_SCR = CV_XR + 3 * CV_XRS;
constexpr int SD_SCR = FLG_SCR + 1024;
constexpr int DUMMY = PR_D + 10240;
constexpr int GW_SCR = DUMMY + 4096;
constexpr int LC_B = 163808, LC_C = 163824;
constexpr int CV_CS = 4160, CV_HP = 0, CV_HPS = 4 * CV_CS;
constexpr int G2_Y = 3 * CV_HPS;
constexpr int G2_RED = 61440;
constexpr int LDS_BYTES = 163840;
static_assert(SD_SCR + 1024 <= DUMMY && GW_SCR + 2048 <= LC_B && G2_Y + 80 * 128 <= G2_RED && G2_RED + 60 * 1024 <= SCR, "lds");
constexpr unsigned SK = 2;
constexpr int CODE_BYTES = 28160;

__global__ void __launch_bounds__(256, 1) k_fused(Params p) {
    extern __shared__ __attribute__((aligned(16))) unsigned char smem[];
    const int tid = threadIdx.x, lane = tid & 63, w = __builtin_amdgcn_readfirstlane(tid >> 6);
    const int bid = blockIdx.x;
    const int xcd = bid & 7, bi = bid >> 3, mq = xcd >> 1, half = xcd & 1;
    {
        unsigned long long pc;
        asm volatile("s_getpc_b64 %0" : "=s"(pc));
        const rsrc_t rsC = mkrs((const void*)(pc & ~255ull), CODE_BYTES - 256);
        dma16(rsC, (unsigned)(((bi + 32 * w) << 10) + lane * 16), 0, DUMMY);
    }
    const int nch = bid < 128 ? 3 : 2, ch = (bid < 128 ? 3 * bid : 384 + 2 * (bid - 128)) + w;
    const bool has_ch = w < nch;
    asm volatile("" ::"s"(p.q), "s"(p.k), "s"(p.v), "s"(p.sk), "s"(p.hk), "s"(p.sd), "s"(p.wg), "s"(p.wu), "s"(p.wd), "s"(p.gw), "s"(p.out), "s"(p.ws));
    rsrc_t rsW = mkrs(p.ws, WS_BYTES);
    rsrc_t rsQ = mkrs(p.q, SEQ * DM * 2);

    const int ptt = bid & 31, pd8 = bid >> 5;
    auto prep_loads = [&]() {
        const rsrc_t rsK = mkrs(p.k, SEQ * DM * 2), rsV = mkrs(p.v, SEQ * DM * 2), rsS = mkrs(p.sk, DM * 4 * 2), rsD = mkrs(p.wd, FF * DM * 2);
        auto kdma = [&](int i) {
            const int c = 64 * i + lane, row = c / 10, col = c - 10 * row, t = 64 * ptt - 4 + row;
            dma16(rsK, (t >= 0 && c < 680) ? (unsigned)((t * DM + 80 * pd8 + 8 * col) * 2) : OOB, 0, PR_K + 1024 * i);
        };
        auto vdma = [&](int i) {
            const int c = 64 * i + lane, row = c / 10, col = c - 10 * row;
            dma16(rsV, (unsigned)(((64 * ptt + row) * DM + 80 * pd8 + 8 * col) * 2), 0, PR_V + 1024 * i);
        };
        auto ddma = [&](int j, int i) {
            const int g = 2 * bid + j, gdt = g / 48, gks = g - 48 * gdt;
            const int c = 64 * i + lane, row = c / 10, col = c - 10 * row;
            dma16(rsD, g < 384 ? (unsigned)(((32 * gks + row) * DM + 80 * gdt + 8 * col) * 2) : OOB, 0, PR_D + 5120 * j + 1024 * i);
        };
        if (w == 0) {
#pragma unroll
            for (int i = 0; i < 8; ++i) kdma(i);
        } else if (w == 1) {
#pragma unroll
            for (int i = 8; i < 11; ++i) kdma(i);
#pragma unroll
            for (int i = 0; i < 5; ++i) vdma(i);
        } else if (w == 2) {
#pragma unroll
            for (int i = 5; i < 10; ++i) vdma(i);
#pragma unroll
            for (int i = 0; i < 5; ++i) ddma(0, i);
        } else {
            dma16(rsS, lane < 40 ? (unsigned)(80 * pd8 * 8 + 16 * lane) : OOB, 0, PR_S);
            const rsrc_t rsG = mkrs(p.gw, DM * 2);
            dma16(rsG, (unsigned)(16 * lane), 0, GW_SCR);
            dma16(rsG, (unsigned)(16 * lane), 1024, GW_SCR + 1024);
#pragma unroll
            for (int i = 0; i < 5; ++i) ddma(1, i);
        }
    };
    v4u pkv[3], pwd[5];
    auto prep_compute = [&]() {
        const int i16 = lane & 15, q = (lane >> 2) & 3, pp = lane & 3;
#pragma unroll
        for (int ps = 0; ps < 3; ++ps) {
            const int quad = min(4 * ps + w, 9), cg = quad >> 1, tgp = 4 * (quad & 1) + (lane >> 4);
            const unsigned ka = (unsigned)(PR_K + (8 * tgp + q) * 160 + (16 * cg + 4 * pp) * 2), va = ka + (PR_V - PR_K);
            const v2u k0 = tr_read(ka), k1 = tr_read(ka + 640), k2 = tr_read(ka + 1280), v0 = tr_read(va), v1 = tr_read(va + 640);
            const v2u tw = lds_ld<v2u>((unsigned)(PR_S + (16 * cg + i16) * 8));
            const unsigned W10 = __builtin_amdgcn_alignbit(tw.x, tw.x, 16), W32 = __builtin_amdgcn_alignbit(tw.y, tw.y, 16);
            const unsigned P[6] = {k0.x, k0.y, k1.x, k1.y, k2.x, k2.y};
            unsigned Q[5];
#pragma unroll
            for (int m = 0; m < 5; ++m) Q[m] = __builtin_amdgcn_alignbit(P[m + 1], P[m], 16);
            const unsigned V[4] = {v0.x, v0.y, v1.x, v1.y};
            float r[8];
#pragma unroll
            for (int m = 0; m < 4; ++m) {
                r[2 * m] = dot2(Q[m], W32, dot2(Q[m + 1], W10, 0.f)) * blo(V[m]);
                r[2 * m + 1] = dot2(P[m + 1], W32, dot2(P[m + 2], W10, 0.f)) * bhi(V[m]);
            }
            pkv[ps] = v4u{pk2(r[7], r[6]), pk2(r[5], r[4]), pk2(r[3], r[2]), pk2(r[1], r[0])};
        }
        const unsigned T = (unsigned)(PR_D + 5120 * (w & 1));
        const int gq = lane >> 4, row = 16 * (gq >> 1) + 4 * (gq & 1) + q;
#pragma unroll
        for (int at = 0; at < 5; ++at) {
            const v2u lo = tr_read(T + row * 160 + (16 * at + 4 * pp) * 2);
            const v2u hi = tr_read(T + (row + 8) * 160 + (16 * at + 4 * pp) * 2);
            pwd[at] = v4u{lo.x, lo.y, hi.x, hi.y};
        }
    };
    const int mt1 = 8 * (xcd >> 2) + (bi & 7), ft = 4 * (xcd & 3) + (bi >> 3);
    const int wm = w & 1, wf = w >> 1;
    const int rot1 = (10 * (((bi & 7) + 2 * (bi >> 3)) & 7)) / 8;
    rsrc_t rsWg = mkrs(p.wg, DM * FF * 2), rsWu = mkrs(p.wu, DM * FF * 2);
    PIN_S(rsW); PIN_S(rsWg); PIN_S(rsWu); PIN_S(rsQ);
    unsigned xoff[4], goff[3], uoff[3];
#pragma unroll
    for (int n = 0; n < 4; ++n) {
        const int row = 32 * w + 8 * n + (lane >> 3);
        xoff[n] = (unsigned)((128 * mt1 + row) * 1280 + (((lane & 7) ^ ((4 * n + (lane >> 4)) & 7)) << 4));
    }
#pragma unroll
    for (int n = 0; n < 3; ++n) {
        const int Lc = 64 * (3 * w + n) + lane, k = Lc / 12, c = Lc - 12 * k;
        goff[n] = (unsigned)(k * 3072 + 192 * ft + 16 * c);
        uoff[n] = (unsigned)(k * 3072 + 192 * ft + 16 * (c ^ 2));
    }
    auto g1_issue = [&](int kt, int stg) {
#pragma unroll
        for (int n = 0; n < 4; ++n) dma16(rsQ, xoff[n], kt * 128, stg + (4 * w + n) * 1024);
#pragma unroll
        for (int n = 0; n < 3; ++n) {
            dma16(rsWg, goff[n], kt * 64 * 3072, stg + G1_G + (3 * w + n) * 1024);
            dma16(rsWu, uoff[n], kt * 64 * 3072, stg + G1_U + (3 * w + n) * 1024);
        }
    };
    g1_issue(rot1, 0);
    prep_loads();
    const unsigned epoch = *(const __attribute__((address_space(4))) unsigned*)(uintptr_t)(p.ws + OFF_EPOCH) + 1u;
    f32x16 acc[3][2];
#pragma unroll
    for (int a = 0; a < 3; ++a)
#pragma unroll
        for (int b = 0; b < 2; ++b)
#pragma unroll
            for (int r = 0; r < 16; ++r) acc[a][b][r] = 0.f;
    const int l31 = lane & 31, hh = lane >> 5;
    const int mt2 = 8 * mq + (bi & 7), dt = 4 * half + (bi >> 3);
    unsigned favo;
    {
        int who = 24 * dt + min(max(lane - 32, 0), 23);
        if (lane < 32 && has_ch) who = 32 * (ch / 80) + lane;
        favo = (unsigned)(OFF_FA + 128 * who);
    }
    const int rot2 = (3 * (bi & 7) / 2 + 3 * (bi >> 3)) % 12;
    unsigned a2o[12];
#pragma unroll
    for (int s = 0; s < 12; ++s) { const int ks = s + rot2; a2o[s] = (unsigned)(12 * w + (ks >= 12 ? ks - 12 : ks)) * 1024u; }
    const unsigned a2base = (unsigned)(OFF_WDT + dt * 48 * 5 * 1024);
    const int brow = 64 * wm + l31;
    unsigned ba[4];
#pragma unroll
    for (int kk = 0; kk < 4; ++kk) ba[kk] = (unsigned)(brow * 128 + (((2 * kk + hh) ^ ((brow >> 1) & 7)) << 4));
    unsigned aa[3];
#pragma unroll
    for (int fb = 0; fb < 3; ++fb) {
        const int fbg = 3 * wf + fb, G = lane >> 4;
        aa[fb] = (unsigned)(((G & 1) ? G1_U + (fbg ^ 1) * 32 : G1_G + fbg * 32) + (8 * (G >> 1) + ((lane >> 2) & 3)) * 192 + 8 * (lane & 3));
    }
    unsigned okv[3], owd, ofa, oraw, osd, okvr, oh[2], fbvo, fcvo;
    {
        const int i16 = lane & 15;
#pragma unroll
        for (int ps = 0; ps < 3; ++ps) {
            const int quad = min(4 * ps + w, 9), cg = quad >> 1, tgp = 4 * (quad & 1) + (lane >> 4);
            okv[ps] = 4 * ps + w < 10 ? (unsigned)(OFF_KVR + ((80 * pd8 + 16 * cg + i16) * SEQ + 2040 - 64 * ptt - 8 * tgp) * 2) : OOB;
        }
        const int g = 2 * bid + (w & 1), gdt = g / 48, gks = g - 48 * gdt;
        owd = (w >= 2 && g < 384) ? (unsigned)(OFF_WDT + (gdt * 48 + gks) * 5 * 1024 + lane * 16) : OOB;
        fbvo = (unsigned)(OFF_FB + 128 * (16 * (mt2 >> 1) + (lane & 15)));
        const int cl = 80 * dt, chh = cl + 79, bl = cl < 384 ? cl / 3 : 128 + (cl - 384) / 2, bh = chh < 384 ? chh / 3 : 128 + (chh - 384) / 2;
        fcvo = (unsigned)(OFF_FC + 128 * min(bl + lane, bh));
        ofa = tid == 0 ? (unsigned)(OFF_FA + 128 * bid) : OOB;
        oraw = has_ch ? (unsigned)(ch * SEQ * 2 + lane * 16) : OOB;
        osd = has_ch ? (unsigned)((ch & ~1) * 2) : OOB;
        okvr = has_ch ? (unsigned)(OFF_KVR + ch * SEQ * 2 + lane * 16) : OOB;
#pragma unroll
        for (int mt = 0; mt < 2; ++mt) oh[mt] = (unsigned)(OFF_HF + ((128 * mt1 + 64 * wm + 32 * mt + l31) >> 4) * 48 * 1024 + ((lane & 15) + 16 * hh) * 16);
    }
    const int grow = 8 * ft + 2 * w + ((lane >> 3) & 1);
    unsigned gxa = (unsigned)(grow * 128 + (((lane & 7) ^ ((grow >> 1) & 7)) << 4)), gwa = (unsigned)(GW_SCR + rot1 * 128 + (lane & 7) * 16);
    float gsum = 0.f;
    PIN_V(gxa); PIN_V(gwa);
    unsigned a2v0 = (unsigned)(lane * 16), a2v4 = a2v0 + 4096;
    PIN_V(favo); PIN_V(okv[0]); PIN_V(okv[1]); PIN_V(okv[2]); PIN_V(owd); PIN_V(fbvo); PIN_V(fcvo); PIN_V(ofa); PIN_V(oraw); PIN_V(osd); PIN_V(okvr); PIN_V(oh[0]); PIN_V(oh[1]);
    PIN_V(ba[0]); PIN_V(ba[1]); PIN_V(ba[2]); PIN_V(ba[3]); PIN_V(aa[0]); PIN_V(aa[1]); PIN_V(aa[2]); PIN_V(a2v0); PIN_V(a2v4);
    rsrc_t rsH = mkrs(p.hk, DM * SEQ * 2), rsD1 = mkrs(p.sd, DM * 2);
    PIN_S(rsH); PIN_S(rsD1);
    int kap = rot1 + 1;
    auto knext = [&]() { kap = kap == 9 ? 0 : kap + 1; };

    v4u A2[12][5];
    unsigned a2s[12];
#pragma unroll
    for (int s = 0; s < 12; ++s) a2s[s] = a2base + a2o[s] * 5;
#define A2LD(slot) A2[(slot) / 5][(slot) % 5] = bld16<SC1>(rsW, ((slot) % 5 == 4 ? a2v4 : a2v0 + ((slot) % 5) * 1024), a2s[(slot) / 5])
    auto prep_store = [&]() {
#pragma unroll
        for (int ps = 0; ps < 3; ++ps) bst16<SC1>(rsW, okv[ps], 0, pkv[ps]);
#pragma unroll
        for (int at = 0; at < 5; ++at) bst16<SC1>(rsW, owd, at * 1024, pwd[at]);
    };

    {
        v4u fb0[2], fb1[2];
        v2u fa0[3][2], fa1[3][2];
#define RDB(d, kk, mt) d = lds_ld<v4u>(ba[kk] + (mt) * 4096)
#define RDA(d, kk, fb, hi) d[fb][hi] = tr_read(aa[fb] + (kk) * 16 * 192 + (hi) * 4 * 192)
#define MM(A, B, fb, mt) acc[fb][mt] = mfma32(v4u{A[fb][0].x, A[fb][0].y, A[fb][1].x, A[fb][1].y}, B[mt], acc[fb][mt])
        if (w == 2) WAIT_V(10); else WAIT_V(8);
        BAR();
        RDB(fb0[0], 0, 0); RDA(fa0, 0, 0, 0); RDA(fa0, 0, 0, 1); RDB(fb0[1], 0, 1); RDA(fa0, 0, 1, 0); RDA(fa0, 0, 1, 1); RDA(fa0, 0, 2, 0); RDA(fa0, 0, 2, 1);
        int st = G1_STAGE;
        unsigned mx = 4096u * w, mg = G1_G + 3072u * w, mu = G1_U + 3072u * w;
        unsigned sx, sw;
        int kcur = rot1;
        unsigned mxw = 4096u * w, mgw = G1_G + 3072u * w, muw = G1_U + 3072u * w;
        PIN_S(mxw); PIN_S(mgw); PIN_S(muw);
#define GT_RD() v4u gtx = lds_ld<v4u>(gxa), gtw = lds_ld<v4u>(gwa)
#define GT_DOT(dl, kc) do { gsum = dot2(gtx.x, gtw.x, gsum); gsum = dot2(gtx.y, gtw.y, gsum); gsum = dot2(gtx.z, gtw.z, gsum); gsum = dot2(gtx.w, gtw.w, gsum); \
                            gxa += (dl); gwa += ((kc) == 9 ? -9 * 128 : 128); PIN_V(gsum); PIN_V(gxa); PIN_V(gwa); } while (0)
#define STEP(FA, FB, GA, GB, kn, D0, D1, D2)                                                     \
            MM(FA, FB, 0, 0); SB(); RDB(GB[0], kn, 0); RDA(GA, kn, 0, 0); SB();                 \
            MM(FA, FB, 0, 1); SB(); RDA(GA, kn, 0, 1); RDB(GB[1], kn, 1); SB();                 \
            MM(FA, FB, 1, 0); SB(); RDA(GA, kn, 1, 0); RDA(GA, kn, 1, 1); D0##_M; SB();         \
            MM(FA, FB, 1, 1); SB(); D0##_L; RDA(GA, kn, 2, 0); D1##_M; SB();                    \
            MM(FA, FB, 2, 0); SB(); D1##_L; RDA(GA, kn, 2, 1); D2##_M; SB();                    \
            MM(FA, FB, 2, 1); SB(); D2##_L; SB();
#define X0_M SET_M0(mx, 0)
#define X0_L dma16_m0(rsQ, xoff[0], sx)
#define X1_M SET_M0(mx, 1024)
#define X1_L dma16_m0(rsQ, xoff[1], sx)
#define X2_M SET_M0(mx, 2048)
#define X2_L dma16_m0(rsQ, xoff[2], sx)
#define X3_M SET_M0(mx, 3072)
#define X3_L dma16_m0(rsQ, xoff[3], sx)
#define G0_M SET_M0(mg, 0)
#define G0_L dma16_m0(rsWg, goff[0], sw)
#define G1_M SET_M0(mg, 1024)
#define G1_L dma16_m0(rsWg, goff[1], sw)
#define G2_M SET_M0(mg, 2048)
#define G2_L dma16_m0(rsWg, goff[2], sw)
#define U0_M SET_M0(mu, 0)
#define U0_L dma16_m0(rsWu, uoff[0], sw)
#define U1_M SET_M0(mu, 1024)
#define U1_L dma16_m0(rsWu, uoff[1], sw)
#define U2_M SET_M0(mu, 2048)
#define U2_L dma16_m0(rsWu, uoff[2], sw)
#define KBODY(WAITV) { \
            int dlt = (st == 2 * G1_STAGE) ? -2 * G1_STAGE : G1_STAGE; \
            PIN_S(dlt); \
            SB(); \
            STEP(fa0, fb0, fa1, fb1, 1, X0, X1, X2) \
            STEP(fa1, fb1, fa0, fb0, 2, X3, G0, U0) \
            STEP(fa0, fb0, fa1, fb1, 3, G1, U1, G2) \
            U2_M; GT_RD(); SB(); \
            MM(fa1, fb1, 0, 0); SB(); U2_L; ba[0] += dlt; aa[0] += dlt; aa[1] += dlt; aa[2] += dlt; PIN_V(ba[0]); PIN_V(aa[0]); PIN_V(aa[1]); PIN_V(aa[2]); \
            knext(); PIN_S(kap); SB(); \
            MM(fa1, fb1, 0, 1); SB(); \
            mx = (unsigned)st + mxw; mg = (unsigned)st + mgw; mu = (unsigned)st + muw; sx = kap * 128; sw = kap * 64 * 3072; st += dlt; \
            PIN_S(mx); PIN_S(mg); PIN_S(mu); PIN_S(sx); PIN_S(sw); PIN_S(st); \
            SB(); \
            MM(fa1, fb1, 1, 0); SB(); \
            WAIT_L(0); \
            GT_DOT(dlt, kcur); kcur = kcur == 9 ? 0 : kcur + 1; PIN_S(kcur); \
            WAITV; \
            BAR(); \
            RDB(fb0[0], 0, 0); RDA(fa0, 0, 0, 0); RDA(fa0, 0, 0, 1); SB(); \
            MM(fa1, fb1, 1, 1); SB(); RDB(fb0[1], 0, 1); RDA(fa0, 0, 1, 0); RDA(fa0, 0, 1, 1); ba[1] += dlt; PIN_V(ba[1]); SB(); \
            MM(fa1, fb1, 2, 0); SB(); RDA(fa0, 0, 2, 0); RDA(fa0, 0, 2, 1); ba[2] += dlt; ba[3] += dlt; PIN_V(ba[2]); PIN_V(ba[3]); SB(); \
            MM(fa1, fb1, 2, 1); SB(); \
            }
#define N_M do {} while (0)
#define N_L do {} while (0)
        {
            SB();
            STEP(fa0, fb0, fa1, fb1, 1, N, N, N)
            STEP(fa1, fb1, fa0, fb0, 2, N, N, N)
            STEP(fa0, fb0, fa1, fb1, 3, N, N, N)
            MM(fa1, fb1, 0, 0); SB(); ba[0] += G1_STAGE; aa[0] += G1_STAGE; aa[1] += G1_STAGE; aa[2] += G1_STAGE; SB();
            MM(fa1, fb1, 0, 1); SB();
            MM(fa1, fb1, 1, 0); SB();
            WAIT_V(0);
            BAR();
            { GT_RD(); GT_DOT(G1_STAGE, kcur); kcur = kcur == 9 ? 0 : kcur + 1; }
            g1_issue(kap, G1_STAGE);
            knext();
            prep_compute();
            g1_issue(kap, 2 * G1_STAGE);
            knext();
            sx = kap * 128; sw = kap * 64 * 3072;
            WAIT_L(0);
            WAIT_V(10);
            BAR();
            RDB(fb0[0], 0, 0); RDA(fa0, 0, 0, 0); RDA(fa0, 0, 0, 1); SB();
            MM(fa1, fb1, 1, 1); SB(); RDB(fb0[1], 0, 1); RDA(fa0, 0, 1, 0); RDA(fa0, 0, 1, 1); ba[1] += G1_STAGE; SB();
            MM(fa1, fb1, 2, 0); SB(); RDA(fa0, 0, 2, 0); RDA(fa0, 0, 2, 1); ba[2] += G1_STAGE; ba[3] += G1_STAGE; SB();
            MM(fa1, fb1, 2, 1); SB();
        }
#pragma unroll 1
        for (unsigned kt = 1; kt < 8; ++kt) {
            KBODY(kwait<SK>(kt))
            if (__builtin_expect((((7u << SK) | 0x40u) >> kt) & 1, 0)) {
                if (kt == SK) prep_store();
                else if (kt == SK + 1) {
#pragma unroll
                    for (int n = 0; n < 4; ++n) dma16(rsH, oraw, n * 1024, CV_RAW + 4096u * w + 1024u * n);
                    dma4(rsD1, osd, SD_SCR + 256u * w);
                } else if (kt == SK + 2) {
                    __builtin_amdgcn_raw_buffer_store_b32(epoch, rsW, ofa, 0, SC1);
                } else dma4_sc1(rsW, favo, FLG_SCR + 256u * w);
            }
        }
        {
            const unsigned flv = lds_ld<unsigned>((unsigned)(FLG_SCR + 256 * w + lane * 4));
            if (__builtin_amdgcn_ballot_w64(flv != epoch) != 0ull) wait_words(rsW, favo, epoch);
#pragma unroll
            for (int n = 0; n < 4; ++n) dma16_sc1(rsW, okvr, n * 1024, (has_ch ? (unsigned)(CV_XR + CV_XRS * w) : (unsigned)DUMMY) + n * 1024);
            dma4_sc1(rsW, lane < 60 ? a2base + (unsigned)(128 * (240 * (bi & 7) + 60 * w + lane)) : OOB, DUMMY);
        }
#define STEPA(FA, FB, GA, GB, kn, s0)                                                            \
            MM(FA, FB, 0, 0); SB(); A2LD(s0); RDB(GB[0], kn, 0); RDA(GA, kn, 0, 0); SB();       \
            MM(FA, FB, 0, 1); SB(); A2LD(s0 + 1); RDA(GA, kn, 0, 1); RDB(GB[1], kn, 1); SB();   \
            MM(FA, FB, 1, 0); SB(); A2LD(s0 + 2); RDA(GA, kn, 1, 0); RDA(GA, kn, 1, 1); SB();   \
            MM(FA, FB, 1, 1); SB(); A2LD(s0 + 3); RDA(GA, kn, 2, 0); SB();                      \
            MM(FA, FB, 2, 0); SB(); A2LD(s0 + 4); RDA(GA, kn, 2, 1); SB();                      \
            MM(FA, FB, 2, 1); SB(); A2LD(s0 + 5); SB();
        {
            const int dlt = (st == 2 * G1_STAGE) ? -2 * G1_STAGE : G1_STAGE;
            SB();
            STEP(fa0, fb0, fa1, fb1, 1, N, N, N)
            STEP(fa1, fb1, fa0, fb0, 2, N, N, N)
            STEP(fa0, fb0, fa1, fb1, 3, N, N, N)
            { GT_RD(); SB();
            MM(fa1, fb1, 0, 0); SB(); ba[0] += dlt; aa[0] += dlt; SB();
            MM(fa1, fb1, 0, 1); SB(); aa[1] += dlt; aa[2] += dlt; GT_DOT(dlt, kcur); kcur = kcur == 9 ? 0 : kcur + 1; SB(); }
            MM(fa1, fb1, 1, 0); SB();
            if (tid == 0) { lds_st<unsigned>(LC_B, 0u); lds_st<unsigned>(LC_C, 0u); }
            WAIT_L(0);
            WAIT_V(5);
            BAR();
            RDB(fb0[0], 0, 0); RDA(fa0, 0, 0, 0); RDA(fa0, 0, 0, 1); SB();
            MM(fa1, fb1, 1, 1); SB(); RDB(fb0[1], 0, 1); RDA(fa0, 0, 1, 0); RDA(fa0, 0, 1, 1); SB();
            MM(fa1, fb1, 2, 0); SB(); RDA(fa0, 0, 2, 0); RDA(fa0, 0, 2, 1); ba[1] += dlt; SB();
            MM(fa1, fb1, 2, 1); SB(); ba[2] += dlt; ba[3] += dlt;
            A2LD(0); SB();
            STEPA(fa0, fb0, fa1, fb1, 1, 1)
            STEPA(fa1, fb1, fa0, fb0, 2, 7)
            STEPA(fa0, fb0, fa1, fb1, 3, 13)
            { GT_RD(); SB();
            MM(fa1, fb1, 0, 0); SB(); A2LD(19); SB();
            MM(fa1, fb1, 0, 1); SB(); A2LD(20); GT_DOT(0, kcur); SB(); }
            MM(fa1, fb1, 1, 0); SB();
            WAIT_L(0);
            BAR();
            A2LD(21); SB();
            MM(fa1, fb1, 1, 1); SB(); A2LD(22); SB();
            MM(fa1, fb1, 2, 0); SB(); A2LD(23); SB();
            MM(fa1, fb1, 2, 1); SB(); A2LD(24); A2LD(25); SB();
        }
#undef STEPA
#undef MM
#undef STEP
#undef KBODY
    }
    {
        gsum += __shfl_xor(gsum, 1); gsum += __shfl_xor(gsum, 2); gsum += __shfl_xor(gsum, 4);
        const float gv = rb(1.0f / (1.0f + __expf(-rb(gsum))));
        __builtin_amdgcn_raw_buffer_store_b32(__builtin_bit_cast(unsigned, gv), rsW, (lane < 16 && (lane & 7) == 0) ? (unsigned)(OFF_GATE + (128 * mt1 + grow) * 4) : OOB, 0, SC1);
    }
#pragma unroll
    for (int fb = 0; fb < 3; ++fb)
#pragma unroll
        for (int mt = 0; mt < 2; ++mt) {
            const f32x16& a = acc[fb][mt];
            v4u o;
#pragma unroll
            for (int j = 0; j < 4; ++j) o[j] = pk2(a[2 * j] * a[8 + 2 * j], a[2 * j + 1] * a[8 + 2 * j + 1]);
            const int Ft = 6 * ft + 3 * wf + fb;
            bst16<SC1>(rsW, oh[mt], (unsigned)((Ft >> 1) * 1024 + (Ft & 1) * 512), o);
        }

    constexpr int W2 = 6;
    unsigned b2off[4];
#pragma unroll
    for (int n = 0; n < 4; ++n) b2off[n] = (unsigned)(OFF_HF + (4 * mt2 + ((w + n) & 3)) * 48 * 1024);
    v4u B2[W2][4];
    auto b2_ring = [&]() {
#pragma unroll
        for (int s = 0; s < W2; ++s)
#pragma unroll
            for (int n = 0; n < 4; ++n) B2[s][n] = bld16<SC1>(rsW, a2v0, b2off[n] + a2o[s]);
    };
    if (has_ch) {
        const unsigned HP = (unsigned)(CV_HP + w * CV_HPS), XR = (unsigned)(CV_XR + w * CV_XRS), RAW = (unsigned)(CV_RAW + w * 4096);
        WAIT_V(33);
        v4u c0[5];
        v2u c1[5];
        unsigned ra0 = RAW + 16 * max(lane - 2, 0), ra1 = RAW + 16 * max(lane - 1, 0), ra2 = RAW + 16 * (lane - 2), ra3 = RAW + 16 * (lane - 1), ra4 = RAW + 16 * (254 + (lane & 1));
        PIN_V(ra0); PIN_V(ra1); PIN_V(ra2); PIN_V(ra3); PIN_V(ra4);
#pragma unroll
        for (int ps = 0; ps < 5; ++ps) {
            c0[ps] = lds_ld<v4u>(ps == 0 ? ra0 : ps < 4 ? ra2 + 1024 * ps : ra4);
            c1[ps] = lds_ld<v2u>(ps == 0 ? ra1 : ps < 4 ? ra3 + 1024 * ps : RAW + 16 * 255);
        }
        if (lane < 2) c0[0] = v4u{0, 0, 0, 0};
        if (lane < 1) c1[0] = v2u{0, 0};
        if (lane & 1) c1[4] = v2u{0, 0};
#pragma unroll
        for (int ps = 0; ps < 5; ++ps) {
            const int n = 64 * ps + lane;
            const unsigned Dw[6] = {c0[ps].x, c0[ps].y, c0[ps].z, c0[ps].w, c1[ps].x, c1[ps].y};
            v4u o0, o1, o2, o3;
#pragma unroll
            for (int q = 0; q < 4; ++q) {
                o0[q] = __builtin_amdgcn_alignbit(Dw[q + 1], Dw[q], 16);
                o1[q] = Dw[q + 1];
                o2[q] = __builtin_amdgcn_alignbit(Dw[q + 2], Dw[q + 1], 16);
                o3[q] = Dw[q + 2];
            }
            if (ps < 4 || lane < 2) {
                lds_st<v4u>(HP + 0 * CV_CS + 16 * n, o0);
                lds_st<v4u>(HP + 1 * CV_CS + 16 * n, o1);
                lds_st<v4u>(HP + 2 * CV_CS + 16 * n, o2);
                lds_st<v4u>(HP + 3 * CV_CS + 16 * n, o3);
            }
        }
        if (lane < 32) lds_st<v4u>(XR + 4096 + 16 * lane, v4u{0, 0, 0, 0});
        asm volatile("" ::: "memory");

        const int r = lane & 15, g = lane >> 4;
        unsigned ap0 = HP + (unsigned)((r & 3) * CV_CS + 2 * ((r & ~3) + 8 * g));
        unsigned ap1 = ap0 + 8;
        PIN_V(ap0); PIN_V(ap1);
        unsigned bp = XR + (unsigned)(2 * (240 - 16 * r + 8 * g));
        PIN_V(bp);
        unsigned rbv = 0;
        f32x4 cacc[8];
#pragma unroll
        for (int i = 0; i < 8; ++i) cacc[i] = f32x4{0.f, 0.f, 0.f, 0.f};
        v4u CA[2][8], CB[2][8];
        auto cvrd = [&](int set, int s, int i) {
            CB[set][i] = lds_ld<v4u>(bp + 512 * (7 - i) + 64 * s);
            const v2u lo = lds_ld<v2u>(ap0 + 512 * i + 64 * s), hi = lds_ld<v2u>(ap1 + 512 * i + 64 * s);
            CA[set][i] = v4u{lo.x, lo.y, hi.x, hi.y};
        };
#pragma unroll
        for (int i = 0; i < 8; ++i) cvrd(0, 0, i);
#pragma unroll
        for (int s = 0; s < 8; ++s) {
            const int cur = s & 1;
            SB();
#pragma unroll
            for (int gm = 0; gm < 8; ++gm) {
#pragma unroll
                for (int be = 0; be <= gm; ++be) cacc[gm] = mfma16(CA[cur][be], CB[cur][gm - be], cacc[gm]);
                SB();
                if (s < 7) cvrd(cur ^ 1, s + 1, gm);
                { const int i = 8 * s + gm; if (((i + 1) * 34) / 64 != (i * 34) / 64) A2LD(26 + (i * 34) / 64); }
                SB();
            }
            if (s == 1) {
                WAIT_V(8);
                lds_arrive(LC_B);
            }
            if (s == 5) rbv = __builtin_amdgcn_raw_buffer_load_b32(rsW, fbvo, 0, SC1);
        }
        if (__builtin_amdgcn_ballot_w64(rbv != epoch) != 0ull) wait_words_c<4>(rsW, fbvo, epoch);
        __builtin_amdgcn_fence(__ATOMIC_ACQUIRE, "wavefront");
        b2_ring();
        SB();
        const unsigned sdv = lds_ld<unsigned>((unsigned)(SD_SCR + 256 * w));
        const float Dc = (ch & 1) ? bhi(sdv) : blo(sdv);
#pragma unroll
        for (int gm = 0; gm < 8; ++gm) {
            const int t0 = 256 * gm + 16 * r + 4 * g;
            const v2u kvp = lds_ld<v2u>(XR + 2 * (2044 - t0));
            const float y0 = cacc[gm][0] + bhi(kvp.y) * Dc, y1 = cacc[gm][1] + blo(kvp.y) * Dc, y2 = cacc[gm][2] + bhi(kvp.x) * Dc, y3 = cacc[gm][3] + blo(kvp.x) * Dc;
            lds_st<v2u>(HP + 2 * t0, v2u{pk2(y0, y1), pk2(y2, y3)});
        }
        asm volatile("" ::: "memory");
#pragma unroll
        for (int ps = 0; ps < 4; ++ps) {
            const int n = 64 * ps + lane;
            bst16<SC1>(rsW, (unsigned)(OFF_YT + (ch * SEQ + 8 * n) * 2), 0, lds_ld<v4u>(HP + 16 * n));
        }
    } else {
        WAIT_V(0);
        lds_arrive(LC_B);
        lds_wait(LC_B, 4);
        if (w == 3) {
            if (lane == 0) set_flag(p, OFF_FB, 32 * (16 * mt1 + ft), epoch);
            if (bid == 0) {
#pragma unroll 1
                for (int i = 0; i < 4; ++i) wait_words(rsW, (unsigned)(OFF_FA + 128 * (64 * i + lane)), epoch);
                if (lane == 0) set_flag(p, OFF_EPOCH, 0, epoch);
            }
        }
#pragma unroll
        for (int slot = 26; slot < 60; ++slot) A2LD(slot);
        wait_words_c<6>(rsW, fbvo, epoch);
        b2_ring();
    }

    f32x4 acc2[5][4];
#pragma unroll
    for (int a = 0; a < 5; ++a)
#pragma unroll
        for (int n = 0; n < 4; ++n) acc2[a][n] = f32x4{0.f, 0.f, 0.f, 0.f};
    const int t = 64 * mt2 + 16 * w + (lane & 15), g4 = lane >> 4;
    v2u qv[5];
    float gt;
#pragma unroll
    for (int s = 0; s < 12; ++s) {
        v4u cb[4];
#pragma unroll
        for (int n = 0; n < 4; ++n) cb[n] = B2[s % W2][n];
        if (s + W2 < 12) {
#pragma unroll
            for (int n = 0; n < 4; ++n) B2[s % W2][n] = bld16<SC1>(rsW, a2v0, b2off[n] + a2o[s + W2]);
        }
        if (s == 2 && has_ch) {
            WAIT_V(12);
            lds_arrive(LC_C);
        }
        if (s == 6) {
#pragma unroll
            for (int a = 0; a < 5; ++a) qv[a] = *(const GAS v2u*)(p.q + (size_t)t * DM + 80 * dt + 16 * a + 4 * g4);
            gt = __builtin_bit_cast(float, __builtin_amdgcn_raw_buffer_load_b32(rsW, (unsigned)(OFF_GATE + t * 4), 0, SC1));
        }
#pragma unroll
        for (int n = 0; n < 4; ++n)
#pragma unroll
            for (int a = 0; a < 5; ++a) acc2[a][n] = mfma16(A2[s][a], cb[n], acc2[a][n]);
        SB();
    }
    lds_wait(LC_B, 4);
#pragma unroll
    for (int a = 0; a < 5; ++a)
#pragma unroll
        for (int j = 1; j < 4; ++j) lds_st<f32x4>((unsigned)(G2_RED + ((((w + j) & 3) * 5 + a) * 3 + (j - 1)) * 1024 + lane * 16), acc2[a][j]);
    if (w == 3) {
        lds_wait(LC_C, (unsigned)nch);
        if (lane == 0) set_flag(p, OFF_FC, 32 * bid, epoch);
        wait_words_c<6>(rsW, fcvo, epoch);
        v4u yv[10];
#pragma unroll
        for (int it = 0; it < 10; ++it) {
            const int c = lane + 64 * it;
            yv[it] = bld16<SC1>(rsW, (unsigned)(OFF_YT + ((80 * dt + (c >> 3)) * SEQ + 64 * mt2 + 8 * (c & 7)) * 2), 0);
        }
#pragma unroll
        for (int it = 0; it < 10; ++it) lds_st<v4u>((unsigned)(G2_Y + (lane + 64 * it) * 16), yv[it]);
    }
    WAIT_L(0);
    BAR();
    const float omg = rb(1.0f - gt);
    const int q = (lane >> 2) & 3, pp = lane & 3;
#pragma unroll
    for (int a = 0; a < 5; ++a) {
        f32x4 b = acc2[a][0];
#pragma unroll
        for (int j = 0; j < 3; ++j) b += lds_ld<f32x4>((unsigned)(G2_RED + ((w * 5 + a) * 3 + j) * 1024 + lane * 16));
        const v2u y = tr_read((unsigned)(G2_Y + (16 * a + 4 * g4 + q) * 128 + 32 * w + 8 * pp));
        const int d0 = 80 * dt + 16 * a + 4 * g4;
        const float o0 = gt * (blo(qv[a].x) * blo(y.x)) + omg * b[0], o1 = gt * (bhi(qv[a].x) * bhi(y.x)) + omg * b[1];
        const float o2 = gt * (blo(qv[a].y) * blo(y.y)) + omg * b[2], o3 = gt * (bhi(qv[a].y) * bhi(y.y)) + omg * b[3];
        *(GAS v2u*)(p.out + (size_t)t * DM + d0) = v2u{pk2(o0, o1), pk2(o2, o3)};
    }
}

extern "C" void kernel_launch(void* const* d_in, const int* in_sizes, int n_in, void* d_out, int out_size, void* d_ws, size_t ws_size, hipStream_t stream) {
    Params p;
    p.q = (const u16*)d_in[0]; p.k = (const u16*)d_in[1]; p.v = (const u16*)d_in[2]; p.sk = (const u16*)d_in[3];
    p.hk = (const u16*)d_in[4]; p.sd = (const u16*)d_in[5];
    p.wg = (const u16*)d_in[7]; p.wu = (const u16*)d_in[8]; p.wd = (const u16*)d_in[9]; p.gw = (const u16*)d_in[10];
    p.out = (u16*)d_out; p.ws = (unsigned char*)d_ws;
    (void)hipFuncSetAttribute((const void*)k_fused, hipFuncAttributeMaxDynamicSharedMemorySize, LDS_BYTES);
    k_fused<<<256, 256, LDS_BYTES, stream>>>(p);
}
```
